```python
import torch

def _softmax(x, axis=-1):
    return torch.softmax(x, dim=axis)

def _gelu(x):
    return torch.nn.functional.gelu(x, approximate='tanh')

def _silu(x):
    return torch.nn.functional.silu(x)

def _sigmoid(x):
    return torch.sigmoid(x)

def _layernorm(x, axis=-1, eps=1e-05):
    m = x.mean(dim=axis, keepdim=True)
    v = x.var(dim=axis, keepdim=True, unbiased=False)
    return (x - m) / torch.sqrt(v + eps)

def _scatter_add(tgt, idx, src, axis=0):
    return tgt.clone().index_add_(axis, idx, src)

def _segment_sum(src, seg, n):
    out = torch.zeros((n,) + src.shape[1:], dtype=src.dtype)
    return out.index_add_(0, seg, src)

def _pad(x, pw):
    flat = tuple((v for pair in reversed(pw) for v in pair))
    return torch.nn.functional.pad(x, flat)
kernel_name = 'adaLN_cond__sliding_block_cached__mixer_s_mlp__prompt'

def setup_inputs():

    _w = lambda a: (lambda t: t.to(torch.bfloat16) if t.is_floating_point() else t)(torch.as_tensor(a))

    def _A_setup():
        B, Sq, D = (1, 2048, 512)
        Hq, Hkv, Sk, Dh = (32, 8, 2048, 128)
        assert Hq % Hkv == 0, (Hq, Hkv)
        return {'x': _w((lambda *s: torch.randn(*s, dtype=torch.bfloat16) * 0.05)(B, Sq, D)), 'k_cache': _w((lambda *s: torch.randn(*s, dtype=torch.bfloat16) * 3.2)(B, Hkv, Sk, Dh)), 'v_cache': _w((lambda *s: torch.randn(*s, dtype=torch.bfloat16) * 0.4)(B, Hkv, Sk, Dh)), 'wq': _w((lambda *s: torch.randn(*s, dtype=torch.bfloat16) * 3.2)(D, Hq * Dh) * D ** (-0.5)), 'wo': _w((lambda *s: torch.randn(*s, dtype=torch.bfloat16) * 0.4)(Hq * Dh, D) * (Hq * Dh) ** (-0.5))}
    _A_in = _A_setup()

    def _B_setup():
        B, S, D, F = (1, 2048, 512, 1792)
        return {'x': _w((lambda *s: torch.randn(*s, dtype=torch.bfloat16) * 0.05)(B, S, D)), 'w1': _w((lambda *s: torch.randn(*s, dtype=torch.bfloat16) * 3.2)(D, D) * D ** (-0.5)), 'w2': _w((lambda *s: torch.randn(*s, dtype=torch.bfloat16) * 3.2)(D, D) * D ** (-0.5)), 'w3': _w((lambda *s: torch.randn(*s, dtype=torch.bfloat16) * 3.2)(D, F) * D ** (-0.5)), 'w4': _w((lambda *s: torch.randn(*s, dtype=torch.bfloat16) * 3.2)(F, D) * F ** (-0.5))}
    _B_in = _B_setup()
    return {'A_x': _A_in['x'], 'A_k_cache': _A_in['k_cache'], 'A_v_cache': _A_in['v_cache'], 'A_wq': _A_in['wq'], 'A_wo': _A_in['wo'], 'B_x': _B_in['x'], 'B_w1': _B_in['w1'], 'B_w2': _B_in['w2'], 'B_w3': _B_in['w3'], 'B_w4': _B_in['w4'], 'c': _w((lambda *s: torch.randn(*s, dtype=torch.bfloat16) * 0.1)(1, 512) * 8.0), 'w_mod': _w((lambda *s: torch.randn(*s, dtype=torch.bfloat16) * 0.1)(512, 6 * 512) * 512 ** (-0.5) * 8.0)}

def reference(A_x, A_k_cache, A_v_cache, A_wq, A_wo, B_x, B_w1, B_w2, B_w3, B_w4, c, w_mod):

    def _A_ref(x, k_cache, v_cache, wq, wo):
        B, Sq, D = x.shape
        Hq, Dh = (32, 128)
        Hkv, Sk = (k_cache.shape[1], k_cache.shape[2])
        W = min(4096, max(256, Sk // 4))
        rep = Hq // Hkv
        q = (lambda x, *d: x.permute(*d))((x @ wq).reshape(B, Sq, Hq, Dh), 0, 2, 1, 3)
        k = (lambda x, n, axis: torch.repeat_interleave(x, n, dim=axis))(k_cache[:, :, Sk - W:, :], rep, axis=1)
        v = (lambda x, n, axis: torch.repeat_interleave(x, n, dim=axis))(v_cache[:, :, Sk - W:, :], rep, axis=1)
        scores = torch.einsum('bhqd,bhkd->bhqk', q, k) * Dh ** (-0.5)
        p = _softmax(scores, axis=-1)
        o = torch.einsum('bhqk,bhkd->bhqd', p, v)
        return (lambda x, *d: x.permute(*d))(o, 0, 2, 1, 3).reshape(B, Sq, Hq * Dh) @ wo

    def _B_ref(x, w1, w2, w3, w4):
        B, S, D = x.shape
        h = _gelu(_layernorm(x, axis=-1) @ w1)
        c = D // 4
        g0 = _pad(h[:, :, 0 * c:1 * c], ((0, 0), (1, 0), (0, 0)))[:, :S, :]
        g1 = _pad(h[:, :, 1 * c:2 * c], ((0, 0), (0, 1), (0, 0)))[:, 1:, :]
        g2 = _pad(h[:, :, 2 * c:3 * c], ((0, 0), (2, 0), (0, 0)))[:, :S, :]
        g3 = _pad(h[:, :, 3 * c:], ((0, 0), (0, 2), (0, 0)))[:, 2:, :]
        hs = torch.concatenate([g0, g1, g2, g3], axis=-1) @ w2
        x1 = x + hs
        h2 = _gelu(_layernorm(x1, axis=-1) @ w3) @ w4
        return x1 + h2
    x = A_x
    mod = (_silu(c) @ w_mod).reshape(c.shape[0], 6, 1, -1)
    sa, ca, ga, sb, cb, gb = (mod[:, 0].clone(), mod[:, 1].clone(), mod[:, 2].clone(), mod[:, 3].clone(), mod[:, 4].clone(), mod[:, 5].clone())
    h = _layernorm(x, axis=-1) * (1 + ca) + sa
    x = x + ga * _A_ref(h, A_k_cache, A_v_cache, A_wq, A_wo)
    h = _layernorm(x, axis=-1) * (1 + cb) + sb
    x = x + gb * _B_ref(h, B_w1, B_w2, B_w3, B_w4)
    return x
```

```cpp
#include <hip/hip_runtime.h>
#include <hip/hip_bf16.h>
typedef unsigned short u16;
__device__ inline float bf2f(u16 v){ return __uint_as_float(((unsigned)v)<<16); }
__device__ inline u16 f2bf(float f){ unsigned u=__float_as_uint(f); u += 0x7fffu + ((u>>16)&1u); return (u16)(u>>16); }
__device__ inline float rbf(float f){ return bf2f(f2bf(f)); }
__device__ inline float gelu(float x){ return 0.5f*x*(1.f+tanhf(0.7978845608028654f*(x+0.044715f*x*x*x))); }

__global__ void k_mod(const u16* c, const u16* wmod, float* mod){
  int n = blockIdx.x*blockDim.x+threadIdx.x; if(n>=3072) return;
  float acc=0; for(int k=0;k<512;k++){ float cv=bf2f(c[k]); float s=cv/(1.f+__expf(-cv)); acc+= rbf(s)*bf2f(wmod[k*3072+n]); }
  mod[n]=rbf(acc);
}
__global__ void k_ln(const float* in, float* out, const float* sc, const float* sh){
  int r=blockIdx.x; int t=threadIdx.x; __shared__ float red[2];
  float s=0; for(int k=t;k<512;k+=64) s+=in[r*512+k];
  for(int o=32;o;o>>=1) s+=__shfl_xor(s,o);
  float m=s/512; float v=0; for(int k=t;k<512;k+=64){float d=in[r*512+k]-m; v+=d*d;}
  for(int o=32;o;o>>=1) v+=__shfl_xor(v,o);
  v/=512; float rs=1.f/sqrtf(v+1e-5f);
  for(int k=t;k<512;k+=64){ float y=(in[r*512+k]-m)*rs; if(sc) y=y*(1.f+sc[k])+sh[k]; out[r*512+k]=y; }
}
__global__ void k_cvt(const u16* in, float* out, int n){ int i=blockIdx.x*blockDim.x+threadIdx.x; if(i<n) out[i]=bf2f(in[i]); }
__global__ void k_gemm(const float* A, const u16* W, float* C, int M,int N,int K,int act){
  __shared__ float As[16][17], Ws[16][17];
  int tx=threadIdx.x, ty=threadIdx.y; int row=blockIdx.y*16+ty, col=blockIdx.x*16+tx; float acc=0;
  for(int k0=0;k0<K;k0+=16){ As[ty][tx]=rbf(A[(long)row*K+k0+tx]); Ws[ty][tx]=bf2f(W[(long)(k0+ty)*N+col]); __syncthreads();
    for(int k=0;k<16;k++) acc+=As[ty][k]*Ws[k][tx]; __syncthreads(); }
  if(act) acc=gelu(acc);
  C[(long)row*N+col]=acc;
}
__global__ void k_attn(const float* q, const u16* kc, const u16* vc, float* o){
  int r=blockIdx.x, h=blockIdx.y, g=h/4, t=threadIdx.x; __shared__ float qs[128]; __shared__ float p[512]; __shared__ float red[128];
  qs[t]=rbf(q[(long)r*4096+h*128+t]); __syncthreads();
  const u16* K=kc+((long)g*2048+1536)*128; const u16* V=vc+((long)g*2048+1536)*128;
  float mx=-1e30f;
  for(int j=t;j<512;j+=128){ float s=0; for(int d=0;d<128;d++) s+=qs[d]*bf2f(K[j*128+d]); s*=0.08838834764831845f; p[j]=s; mx=fmaxf(mx,s);}
  red[t]=mx; __syncthreads(); for(int o2=64;o2;o2>>=1){ if(t<o2) red[t]=fmaxf(red[t],red[t+o2]); __syncthreads(); } mx=red[0]; __syncthreads();
  float sum=0; for(int j=t;j<512;j+=128){ float e=__expf(p[j]-mx); p[j]=e; sum+=e; }
  red[t]=sum; __syncthreads(); for(int o2=64;o2;o2>>=1){ if(t<o2) red[t]+=red[t+o2]; __syncthreads(); } sum=red[0]; __syncthreads();
  float acc=0; for(int j=0;j<512;j++) acc+=rbf(p[j])*bf2f(V[j*128+t]);
  o[(long)r*4096+h*128+t]=acc/sum;
}
__global__ void k_resid(const float* x, const float* a, const float* g, float* out, int n){ int i=blockIdx.x*blockDim.x+threadIdx.x; if(i<n) out[i]=x[i]+g[i&511]*a[i]; }
__global__ void k_shift(const float* h, float* out){ int i=blockIdx.x*blockDim.x+threadIdx.x; int r=i>>9,c=i&511; int g=c>>7; int sr = g==0? r-1 : g==1? r+1 : g==2? r-2 : r+2; out[i]=(sr>=0&&sr<2048)? h[sr*512+c]:0.f; }
__global__ void k_add(const float* a,const float* b,float* o,int n){ int i=blockIdx.x*blockDim.x+threadIdx.x; if(i<n) o[i]=a[i]+b[i]; }
__global__ void k_final(const float* x2,const float* x1,const float* h2,const float* g,u16* out,int n){ int i=blockIdx.x*blockDim.x+threadIdx.x; if(i<n) out[i]=f2bf(x2[i]+g[i&511]*(x1[i]+h2[i])); }

extern "C" void kernel_launch(void* const* d_in, const int* in_sizes, int n_in, void* d_out, int out_size, void* d_ws, size_t ws_size, hipStream_t stream){
  const u16* x=(const u16*)d_in[0]; const u16* kc=(const u16*)d_in[1]; const u16* vc=(const u16*)d_in[2]; const u16* wq=(const u16*)d_in[3]; const u16* wo=(const u16*)d_in[4];
  const u16* w1=(const u16*)d_in[6]; const u16* w2=(const u16*)d_in[7]; const u16* w3=(const u16*)d_in[8]; const u16* w4=(const u16*)d_in[9]; const u16* c=(const u16*)d_in[10]; const u16* wmod=(const u16*)d_in[11];
  float* ws=(float*)d_ws; const int NX=2048*512;
  float* mod=ws; float* xf=ws+4096; float* h=xf+NX; float* q=h+NX; float* o=q+2048*4096; float* a=o+2048*4096; float* x2=a+NX; float* hb=x2+NX; float* hn=hb+NX; float* h1=hn+NX; float* hs=h1+NX; float* t=hs+NX; float* x1=t+NX; float* x1n=x1+NX; float* u=x1n+NX; float* h2=u+2048*1792;
  k_mod<<<12,256,0,stream>>>(c,wmod,mod);
  k_cvt<<<NX/256,256,0,stream>>>(x,xf,NX);
  k_ln<<<2048,64,0,stream>>>(xf,h,mod+512,mod);
  k_gemm<<<dim3(4096/16,2048/16),dim3(16,16),0,stream>>>(h,wq,q,2048,4096,512,0);
  k_attn<<<dim3(2048,32),128,0,stream>>>(q,kc,vc,o);
  k_gemm<<<dim3(512/16,2048/16),dim3(16,16),0,stream>>>(o,wo,a,2048,512,4096,0);
  k_resid<<<NX/256,256,0,stream>>>(xf,a,mod+1024,x2,NX);
  k_ln<<<2048,64,0,stream>>>(x2,hb,mod+2048,mod+1536);
  k_ln<<<2048,64,0,stream>>>(hb,hn,nullptr,nullptr);
  k_gemm<<<dim3(512/16,2048/16),dim3(16,16),0,stream>>>(hn,w1,h1,2048,512,512,1);
  k_shift<<<NX/256,256,0,stream>>>(h1,hs);
  k_gemm<<<dim3(512/16,2048/16),dim3(16,16),0,stream>>>(hs,w2,t,2048,512,512,0);
  k_add<<<NX/256,256,0,stream>>>(hb,t,x1,NX);
  k_ln<<<2048,64,0,stream>>>(x1,x1n,nullptr,nullptr);
  k_gemm<<<dim3(1792/16,2048/16),dim3(16,16),0,stream>>>(x1n,w3,u,2048,1792,512,1);
  k_gemm<<<dim3(512/16,2048/16),dim3(16,16),0,stream>>>(u,w4,h2,2048,512,1792,0);
  k_final<<<NX/256,256,0,stream>>>(x2,x1,h2,mod+2560,(u16*)d_out,NX);
}
```

```cpp
#include <hip/hip_runtime.h>
#include <hip/hip_bf16.h>

typedef unsigned short u16;
typedef __bf16 bf16x8 __attribute__((ext_vector_type(8)));
typedef __bf16 bf16x4 __attribute__((ext_vector_type(4)));
typedef __bf16 bf16x2 __attribute__((ext_vector_type(2)));
typedef float f32x16 __attribute__((ext_vector_type(16)));
typedef float f32x4 __attribute__((ext_vector_type(4)));
typedef float f32x2 __attribute__((ext_vector_type(2)));
typedef unsigned u32x2 __attribute__((ext_vector_type(2)));
typedef unsigned u32x4 __attribute__((ext_vector_type(4)));
typedef __amdgpu_buffer_rsrc_t rsrc_t;
#define DI __device__ __forceinline__
#define LDSP(T, p) ((__attribute__((address_space(3))) T*)(p))
#define SCHED_FENCE() __builtin_amdgcn_sched_barrier(0)

constexpr int S_ = 2048, D_ = 512, HQ = 32, HKV = 8, DH = 128, SK = 2048, WIN = 512, F_ = 1792;

constexpr size_t WS_MOD = 0;
constexpr size_t WS_Y   = 8192;
constexpr size_t WS_X2  = WS_Y  + (size_t)S_ * D_ * 2;
constexpr size_t WS_HB  = WS_X2 + (size_t)S_ * D_ * 2;
constexpr size_t WS_H1  = WS_HB + (size_t)S_ * D_ * 2;
constexpr size_t WS_X1  = WS_H1 + (size_t)S_ * D_ * 2;
constexpr size_t WS_U   = WS_X1 + (size_t)S_ * D_ * 2;
constexpr size_t WS_PART = WS_U + (size_t)S_ * F_ * 2;
constexpr size_t WS_END = WS_PART + (size_t)8 * S_ * D_ * 2;

DI float bflo(unsigned u) { return __uint_as_float(u << 16); }
DI float bfhi(unsigned u) { return __uint_as_float(u & 0xffff0000u); }
DI unsigned pk2(float lo, float hi) { f32x2 v = {lo, hi}; return __builtin_bit_cast(unsigned, __builtin_convertvector(v, bf16x2)); }
DI float rb(float x) { return bflo(pk2(x, x) & 0xffffu); }
DI rsrc_t mk_rsrc(const void* p, unsigned bytes) { return __builtin_amdgcn_make_buffer_rsrc((void*)p, (short)0, (int)bytes, 0x00020000); }
DI unsigned rfl(unsigned x) { return __builtin_amdgcn_readfirstlane(x); }
template <int CTRL> DI float dpp_add(float v) {
  return v + __uint_as_float(__builtin_amdgcn_update_dpp(0u, __float_as_uint(v), CTRL, 0xf, 0xf, true));
}
DI float swap16_add(float v) { auto r = __builtin_amdgcn_permlane16_swap(__float_as_uint(v), __float_as_uint(v), false, false); return __uint_as_float(r[0]) + __uint_as_float(r[1]); }
DI float swap32_add(float v) { auto r = __builtin_amdgcn_permlane32_swap(__float_as_uint(v), __float_as_uint(v), false, false); return __uint_as_float(r[0]) + __uint_as_float(r[1]); }
DI float swap32_max(float v) { auto r = __builtin_amdgcn_permlane32_swap(__float_as_uint(v), __float_as_uint(v), false, false); return fmaxf(__uint_as_float(r[0]), __uint_as_float(r[1])); }
DI float wave_sum(float v) {
  v = dpp_add<0xB1>(v); v = dpp_add<0x4E>(v); v = dpp_add<0x141>(v); v = dpp_add<0x140>(v);
  v = swap16_add(v); v = swap32_add(v); return v;
}
DI float div_rn(float a, float d, float r) {
  float q = a * r; float e = __builtin_fmaf(-q, d, a); return __builtin_fmaf(e, r, q);
}
DI float gelu_tanh(float x) {
  const float u = 0.7978845608028654f * (x + 0.044715f * x * x * x);
  return x * __builtin_amdgcn_rcpf(1.f + __builtin_amdgcn_exp2f(-2.885390081777927f * u));
}
DI void ln_consts(float mean, float var, float& m_bf, float& d_bf, float& r) {
  m_bf = rb(mean); d_bf = rb(__builtin_sqrtf(rb(rb(var) + 1e-5f))); r = __builtin_amdgcn_rcpf(d_bf);
}
DI float ln_apply(float x, float m_bf, float d_bf, float r) { return rb(div_rn(rb(x - m_bf), d_bf, r)); }

#define MFMA16(a, b, c) __builtin_amdgcn_mfma_f32_16x16x32_bf16((a), (b), (c), 0, 0, 0)
#define MFMA32(a, b, c) __builtin_amdgcn_mfma_f32_32x32x16_bf16((a), (b), (c), 0, 0, 0)

DI void dma16(unsigned lds_dst, unsigned voff, rsrc_t r, unsigned soff) {
  asm volatile("s_mov_b32 m0, %0\n\ts_nop 0\n\tbuffer_load_dwordx4 %1, %2, %3 offen lds" ::"s"(lds_dst), "v"(voff), "s"(r), "s"(soff) : "memory");
}

__global__ __launch_bounds__(512) void k0_mod_ln(const u16* __restrict__ x, const u16* __restrict__ c, const u16* __restrict__ wmod,
                                                 u16* __restrict__ modb, u16* __restrict__ y) {
  __shared__ float red[8][64];
  const int tid = threadIdx.x, lane = tid & 63, wid = tid >> 6;
  if (blockIdx.x < 48) {
    const int n0 = blockIdx.x * 64, cg = tid & 7, kq = tid >> 3;
    u32x4 wv[8]; float sv[8];
#pragma unroll
    for (int i = 0; i < 8; ++i) wv[i] = *(const u32x4*)(wmod + (size_t)(kq + 64 * i) * 3072 + n0 + cg * 8);
#pragma unroll
    for (int i = 0; i < 8; ++i) { float cv = bflo(c[kq + 64 * i]); sv[i] = rb(cv / (1.f + __expf(-cv))); }
    float acc[8] = {};
#pragma unroll
    for (int i = 0; i < 8; ++i)
#pragma unroll
      for (int e = 0; e < 4; ++e) { acc[2 * e] += sv[i] * bflo(wv[i][e]); acc[2 * e + 1] += sv[i] * bfhi(wv[i][e]); }
#pragma unroll
    for (int e = 0; e < 8; ++e) { float v = acc[e]; v += __shfl_xor(v, 8); v = swap16_add(v); v = swap32_add(v); acc[e] = v; }
    if (lane < 8)
#pragma unroll
      for (int e = 0; e < 8; ++e) red[wid][lane * 8 + e] = acc[e];
    __syncthreads();
    if (tid < 64) {
      float s = 0;
#pragma unroll
      for (int w = 0; w < 8; ++w) s += red[w][tid];
      s = rb(s);
      const int n = n0 + tid, seg = n >> 9;
      if (seg == 1 || seg == 4) s = rb(1.f + s);
      modb[n] = (u16)(__float_as_uint(s) >> 16);
    }
  } else {
    const int row = (blockIdx.x - 48) * 8 + wid;
    const u32x4 xv = *(const u32x4*)(x + (size_t)row * D_ + lane * 8);
    float f[8];
#pragma unroll
    for (int e = 0; e < 4; ++e) { f[2 * e] = bflo(xv[e]); f[2 * e + 1] = bfhi(xv[e]); }
    float s = 0;
#pragma unroll
    for (int e = 0; e < 8; ++e) s += f[e];
    const float mean = wave_sum(s) * (1.f / 512.f);
    float q = 0;
#pragma unroll
    for (int e = 0; e < 8; ++e) { float d = f[e] - mean; q += d * d; }
    const float var = wave_sum(q) * (1.f / 512.f);
    float mb, db, r; ln_consts(mean, var, mb, db, r);
    u32x4 o;
#pragma unroll
    for (int e = 0; e < 4; ++e) o[e] = pk2(div_rn(rb(f[2 * e] - mb), db, r), div_rn(rb(f[2 * e + 1] - mb), db, r));
    *(u32x4*)(y + (size_t)row * D_ + lane * 8) = o;
  }
}

template <int NI> struct WStage { unsigned d[8][NI / 2]; };
template <int NI> DI void w_load(WStage<NI>& st, rsrc_t r, unsigned voff, unsigned soff, unsigned ldw_bytes) {
#pragma unroll
  for (int j = 0; j < 8; ++j) {
    if constexpr (NI == 4) { u32x2 v = __builtin_amdgcn_raw_buffer_load_b64(r, voff, soff + j * ldw_bytes, 0); st.d[j][0] = v[0]; st.d[j][1] = v[1]; }
    else                   { st.d[j][0] = __builtin_amdgcn_raw_buffer_load_b32(r, voff, soff + j * ldw_bytes, 0); }
  }
}
template <int NI> DI void w_frags(const WStage<NI>& st, bf16x8 (&A)[NI]) {
#pragma unroll
  for (int i = 0; i < NI; ++i) {
    u32x4 t;
#pragma unroll
    for (int p = 0; p < 4; ++p) t[p] = __builtin_amdgcn_perm(st.d[2 * p + 1][i >> 1], st.d[2 * p][i >> 1], (i & 1) ? 0x07060302u : 0x05040100u);
    A[i] = __builtin_bit_cast(bf16x8, t);
  }
}
template <int NI, int NQT, int KS, int DEPTH, int BSTRIDE>
DI void proj_gemm(f32x4 (&acc)[NI][NQT], WStage<NI> (&ring)[DEPTH], rsrc_t r, unsigned voff, unsigned ldw_bytes, const char* bl  ) {
  bf16x8 B[2][NQT];
#pragma unroll
  for (int qt = 0; qt < NQT; ++qt) B[0][qt] = *(const bf16x8*)(bl + qt * 16 * BSTRIDE);
#pragma unroll
  for (int s = 0; s < KS; ++s) {
    bf16x8 A[NI];
    w_frags<NI>(ring[s % DEPTH], A);
    if (s + DEPTH < KS) w_load<NI>(ring[s % DEPTH], r, voff, (unsigned)(32 * (s + DEPTH)) * ldw_bytes, ldw_bytes);
    if (s + 1 < KS) {
#pragma unroll
      for (int qt = 0; qt < NQT; ++qt) B[(s + 1) & 1][qt] = *(const bf16x8*)(bl + qt * 16 * BSTRIDE + 64 * (s + 1));
    }
#pragma unroll
    for (int i = 0; i < NI; ++i)
#pragma unroll
      for (int qt = 0; qt < NQT; ++qt) acc[i][qt] = MFMA16(A[i], B[s & 1][qt], acc[i][qt]);
  }
}

constexpr int HT_STRIDE = 1056, QX_STRIDE = 272;
constexpr int L1_HT = 0, L1_K = 69632, L1_V = L1_K + 3 * 16384, L1_END = L1_V + 2 * 16384;
constexpr float SM_C = 0.12751743082459868f;
constexpr float SM_LIMIT = 1099511627776.f;

struct Attn {
  f32x16 o[4]; bf16x8 qf[8]; float l0, l1, negmc;
};
DI void sm_half(const f32x16& S, float negmc, float& l0, float& l1, u32x4& pa, u32x4& pb) {
#pragma unroll
  for (int p = 0; p < 4; ++p) {
    const float e0 = __builtin_amdgcn_exp2f(__builtin_fmaf(S[2 * p], SM_C, negmc)), e1 = __builtin_amdgcn_exp2f(__builtin_fmaf(S[2 * p + 1], SM_C, negmc));
    l0 += e0; l1 += e1; pa[p] = pk2(e0, e1);
  }
#pragma unroll
  for (int p = 0; p < 4; ++p) {
    const float e0 = __builtin_amdgcn_exp2f(__builtin_fmaf(S[8 + 2 * p], SM_C, negmc)), e1 = __builtin_amdgcn_exp2f(__builtin_fmaf(S[8 + 2 * p + 1], SM_C, negmc));
    l0 += e0; l1 += e1; pb[p] = pk2(e0, e1);
  }
}
DI float max16(const f32x16& S) {
  float m = fmaxf(S[0], S[1]);
#pragma unroll
  for (int r = 2; r < 16; ++r) m = fmaxf(m, S[r]);
  return m;
}
DI unsigned scale_pk(unsigned p, float f) { return pk2(bflo(p) * f, bfhi(p) * f); }

__global__ __launch_bounds__(512, 2) void k1_attn(const u16* __restrict__ y, const u16* __restrict__ modb, const u16* __restrict__ kc, const u16* __restrict__ vc,
                                                  const u16* __restrict__ wq, const u16* __restrict__ wo, u16* __restrict__ part) {
  extern __shared__ __attribute__((aligned(16))) char lds[];
  const int tid = threadIdx.x, lane = tid & 63;
  const int wid = (int)rfl((unsigned)tid >> 6);
  const int g = blockIdx.x & 7, rbk = blockIdx.x >> 3;
  const int a = lane & 15, g4 = lane >> 4, r32 = lane & 31, hh = lane >> 5;
  const unsigned ldsb = (unsigned)(size_t)lds;

  u32x4 yv[8];
#pragma unroll
  for (int i = 0; i < 8; ++i) yv[i] = *(const u32x4*)(y + (size_t)(rbk * 64 + wid * 8 + i) * D_ + lane * 8);
  const u32x4 sav = *(const u32x4*)(modb + lane * 8), cav = *(const u32x4*)(modb + 512 + lane * 8);

  const rsrc_t rK = mk_rsrc(kc + ((size_t)g * SK + (SK - WIN)) * DH, WIN * DH * 2), rV = mk_rsrc(vc + ((size_t)g * SK + (SK - WIN)) * DH, WIN * DH * 2);
  const unsigned drow = wid * 8 + ((lane >> 2) & 7);
  const unsigned kvoff = drow * 256 + (lane >> 5) * 64 + (((lane & 3) ^ ((drow >> 2) & 3)) * 16);
  const unsigned vvoff = drow * 256 + (lane >> 5) * 64 + (lane & 3) * 16;
  const unsigned kdst = ldsb + L1_K + wid * 2048, vdst = ldsb + L1_V + wid * 2048;
  auto dmaK = [&](int t) { dma16(kdst + (t % 3) * 16384, kvoff, rK, t * 16384); dma16(kdst + (t % 3) * 16384 + 1024, kvoff, rK, t * 16384 + 128); };
  auto dmaV = [&](int t) { dma16(vdst + (t & 1) * 16384, vvoff, rV, t * 16384); dma16(vdst + (t & 1) * 16384 + 1024, vvoff, rV, t * 16384 + 128); };
  dmaK(0); dmaK(1); dmaV(0); dmaK(2); dmaV(1);

  constexpr int DEPTH = 3;
  WStage<4> ring[DEPTH];
  const rsrc_t rWq = mk_rsrc(wq, D_ * HQ * DH * 2);
  const unsigned wqoff = (unsigned)(8 * g4) * 8192u + (unsigned)(g * 512 + wid * 64 + 4 * a) * 2u;
#pragma unroll
  for (int s = 0; s < DEPTH; ++s) w_load<4>(ring[s], rWq, wqoff, (unsigned)(32 * s) * 8192u, 8192u);

  {
    float ca[8], sa[8];
#pragma unroll
    for (int e = 0; e < 4; ++e) { ca[2 * e] = bflo(cav[e]); ca[2 * e + 1] = bfhi(cav[e]); sa[2 * e] = bflo(sav[e]); sa[2 * e + 1] = bfhi(sav[e]); }
#pragma unroll
    for (int i = 0; i < 8; ++i) {
      u32x4 o;
#pragma unroll
      for (int e = 0; e < 4; ++e) {
        const unsigned t = pk2(bflo(yv[i][e]) * ca[2 * e], bfhi(yv[i][e]) * ca[2 * e + 1]);
        o[e] = pk2(bflo(t) + sa[2 * e], bfhi(t) + sa[2 * e + 1]);
      }
      *(u32x4*)(lds + L1_HT + (wid * 8 + i) * HT_STRIDE + lane * 16) = o;
    }
  }
  __syncthreads();

  const char* bl = lds + L1_HT + a * HT_STRIDE + g4 * 16;
  Attn A;
  {
    f32x4 acc[4][4] = {};
    proj_gemm<4, 4, 16, DEPTH, HT_STRIDE>(acc, ring, rWq, wqoff, 8192u, bl);
    __syncthreads();
#pragma unroll
    for (int qt = 0; qt < 4; ++qt) {
      u32x4 w0, w1;
#pragma unroll
      for (int rg = 0; rg < 2; ++rg) {
        w0[2 * rg] = pk2(acc[0][qt][rg], acc[1][qt][rg]); w0[2 * rg + 1] = pk2(acc[2][qt][rg], acc[3][qt][rg]);
        w1[2 * rg] = pk2(acc[0][qt][2 + rg], acc[1][qt][2 + rg]); w1[2 * rg + 1] = pk2(acc[2][qt][2 + rg], acc[3][qt][2 + rg]);
      }
      char* dst = lds + L1_HT + ((wid >> 1) * 64 + qt * 16 + a) * QX_STRIDE + ((wid & 1) * 64 + g4 * 16) * 2;
      *(u32x4*)dst = w0; *(u32x4*)(dst + 16) = w1;
    }
    asm volatile("s_waitcnt vmcnt(0)" ::: "memory");
    __syncthreads();
#pragma unroll
    for (int s = 0; s < 8; ++s) A.qf[s] = *(const bf16x8*)(lds + L1_HT + ((wid >> 1) * 64 + (wid & 1) * 32 + r32) * QX_STRIDE + 32 * s + 16 * hh);
  }

  const unsigned kx = (r32 >> 2) & 3;
  const char* kb0 = lds + L1_K + 2048 * (r32 >> 3) + 64 * (r32 & 7) + 16 * ((0 + hh) ^ kx);
  const char* kb1 = lds + L1_K + 2048 * (r32 >> 3) + 64 * (r32 & 7) + 16 * ((2 + hh) ^ kx);
  const char* vb = lds + L1_V + (4 * hh + ((lane >> 2) & 3)) * 64 + ((lane >> 4) & 1) * 32 + (lane & 3) * 8;
  auto kfrag = [&](int t, int kb, int s) { return *(const bf16x8*)(((s & 1) ? kb1 : kb0) + (t % 3) * 16384 + 512 * (s >> 1) + 8192 * kb); };
  auto vfrag = [&](int t, int db, int ks) {
    const char* p = vb + (t & 1) * 16384 + ks * 4096 + db * 512;
    bf16x4 lo = __builtin_amdgcn_ds_read_tr16_b64_v4bf16(LDSP(bf16x4, p)), hi = __builtin_amdgcn_ds_read_tr16_b64_v4bf16(LDSP(bf16x4, p + 2048));
    return __builtin_shufflevector(lo, hi, 0, 1, 2, 3, 4, 5, 6, 7);
  };
  auto qk = [&](int t, f32x16& S0, f32x16& S1) {
    S0 = MFMA32(kfrag(t, 0, 0), A.qf[0], (f32x16){});
#pragma unroll
    for (int s = 1; s < 8; ++s) S0 = MFMA32(kfrag(t, 0, s), A.qf[s], S0);
    S1 = MFMA32(kfrag(t, 1, 0), A.qf[0], (f32x16){});
#pragma unroll
    for (int s = 1; s < 8; ++s) S1 = MFMA32(kfrag(t, 1, s), A.qf[s], S1);
  };
  auto fixup = [&](const f32x16& S, float l0p, float l1p, u32x4& pa, u32x4& pb, u32x4* pend0, u32x4* pend1) {
    const float mx = swap32_max(__builtin_fmaf(max16(S), SM_C, A.negmc));
    const float dm = fmaxf(__builtin_ceilf(mx), 0.f);
    const float f = __builtin_amdgcn_exp2f(-dm);
    A.negmc -= dm; A.l0 = l0p * f; A.l1 = l1p * f;
#pragma unroll
    for (int db = 0; db < 4; ++db) A.o[db] *= f;
    if (pend0) {
#pragma unroll
      for (int p = 0; p < 4; ++p) { (*pend0)[p] = scale_pk((*pend0)[p], f); (*pend1)[p] = scale_pk((*pend1)[p], f); }
    }
    sm_half(S, A.negmc, A.l0, A.l1, pa, pb);
  };

  f32x16 Sa0, Sa1, Sb0, Sb1;
  u32x4 P[4], Pn[2];
#pragma unroll
  for (int db = 0; db < 4; ++db) A.o[db] = (f32x16){};
  qk(0, Sa0, Sa1);
  A.negmc = -swap32_max(fmaxf(max16(Sa0), max16(Sa1))) * SM_C;
  A.l0 = 0.f; A.l1 = 0.f;
  sm_half(Sa0, A.negmc, A.l0, A.l1, P[0], P[1]);

  const rsrc_t rWo = mk_rsrc(wo + (size_t)g * 512 * D_, 512 * D_ * 2);
  const unsigned wooff = (unsigned)(8 * g4) * 1024u + (unsigned)(wid * 64 + 4 * a) * 2u;

  auto iter = [&](const int t, f32x16& C1, f32x16& N0, f32x16& N1) {
    {
      const float l0p = A.l0, l1p = A.l1;
      if (t + 1 < 8) qk(t + 1, N0, N1);
      sm_half(C1, A.negmc, A.l0, A.l1, P[2], P[3]);
      if (__builtin_expect(__any(!(A.l0 + A.l1 <= SM_LIMIT)), 0)) fixup(C1, l0p, l1p, P[2], P[3], &P[0], &P[1]);
    }
    if (t == 7) {
#pragma unroll
      for (int s = 0; s < DEPTH; ++s) w_load<4>(ring[s], rWo, wooff, (unsigned)(32 * s) * 1024u, 1024u);
    }
    {
      const float l0p = A.l0, l1p = A.l1;
#pragma unroll
      for (int ks = 0; ks < 3; ++ks)
#pragma unroll
        for (int db = 0; db < 4; ++db) A.o[db] = MFMA32(vfrag(t, db, ks), __builtin_bit_cast(bf16x8, P[ks]), A.o[db]);
      bf16x8 vl[4];
#pragma unroll
      for (int db = 0; db < 4; ++db) vl[db] = vfrag(t, db, 3);
      if (t + 1 < 8) sm_half(N0, A.negmc, A.l0, A.l1, Pn[0], Pn[1]);
      if (t < 7) {
        asm volatile("s_waitcnt vmcnt(0) lgkmcnt(0)" : "+v"(vl[0]), "+v"(vl[1]), "+v"(vl[2]), "+v"(vl[3])::"memory");
        __builtin_amdgcn_s_barrier();
        asm volatile("" ::: "memory");
        if (t + 3 < 8) dmaK(t + 3);
        if (t + 2 < 8) dmaV(t + 2);
      }
#pragma unroll
      for (int db = 0; db < 4; ++db) A.o[db] = MFMA32(vl[db], __builtin_bit_cast(bf16x8, P[3]), A.o[db]);
      if (t + 1 < 8) {
        if (__builtin_expect(__any(!(A.l0 + A.l1 <= SM_LIMIT)), 0)) fixup(N0, l0p, l1p, Pn[0], Pn[1], nullptr, nullptr);
        P[0] = Pn[0]; P[1] = Pn[1];
      }
    }
  };
#pragma unroll
  for (int t = 0; t < 8; t += 2) { iter(t, Sa1, Sb0, Sb1); iter(t + 1, Sb1, Sa0, Sa1); }

  {
    const float inv = 1.f / swap32_add(A.l0 + A.l1);
    char* dst = lds + L1_HT + ((wid & 1) * 32 + r32) * HT_STRIDE + ((wid >> 1) * 128 + 4 * hh) * 2;
#pragma unroll
    for (int db = 0; db < 4; ++db)
#pragma unroll
      for (int gq = 0; gq < 4; ++gq) {
        u32x2 w = {pk2(A.o[db][4 * gq] * inv, A.o[db][4 * gq + 1] * inv), pk2(A.o[db][4 * gq + 2] * inv, A.o[db][4 * gq + 3] * inv)};
        *(u32x2*)(dst + (32 * db + 8 * gq) * 2) = w;
      }
  }
  __syncthreads();
  {
    f32x4 acc[4][4] = {};
    proj_gemm<4, 4, 16, DEPTH, HT_STRIDE>(acc, ring, rWo, wooff, 1024u, bl);
    char* dst = (char*)part + ((size_t)((rbk * 8 + g) * 8 + wid) * 8) * 1024 + lane * 16;
#pragma unroll
    for (int qt = 0; qt < 4; ++qt) {
      u32x4 w0, w1;
#pragma unroll
      for (int rg = 0; rg < 2; ++rg) {
        w0[2 * rg] = pk2(acc[0][qt][rg], acc[1][qt][rg]); w0[2 * rg + 1] = pk2(acc[2][qt][rg], acc[3][qt][rg]);
        w1[2 * rg] = pk2(acc[0][qt][2 + rg], acc[1][qt][2 + rg]); w1[2 * rg + 1] = pk2(acc[2][qt][2 + rg], acc[3][qt][2 + rg]);
      }
      *(u32x4*)(dst + (qt * 2) * 1024) = w0; *(u32x4*)(dst + (qt * 2 + 1) * 1024) = w1;
    }
  }
}

__global__ __launch_bounds__(512, 2) void k2_red_g1(const u16* __restrict__ x, const u16* __restrict__ modb, const u16* __restrict__ part, const u16* __restrict__ w1,
                                                    u16* __restrict__ x2o, u16* __restrict__ hbo, u16* __restrict__ h1o) {
  __shared__ __attribute__((aligned(16))) char lds[16 * HT_STRIDE + 2 * 8 * 16 * 8];
  f32x2* red = (f32x2*)(lds + 16 * HT_STRIDE);
  const int tid = threadIdx.x, lane = tid & 63, a = lane & 15, g4 = lane >> 4;
  const int wid = (int)rfl((unsigned)tid >> 6);
  const int b = blockIdx.x, rt = (b & 7) + 8 * (b >> 4), nh = (b >> 3) & 1;
  const int rbk = rt >> 2, qt = rt & 3, row = rt * 16 + a, col = wid * 64 + g4 * 16;

  u32x4 pv[8][2];
#pragma unroll
  for (int g = 0; g < 8; ++g)
#pragma unroll
    for (int h = 0; h < 2; ++h) pv[g][h] = *(const u32x4*)((const char*)part + ((size_t)(((rbk * 8 + g) * 8 + wid) * 4 + qt) * 2 + h) * 1024 + lane * 16);
  u32x4 xv[2], gav[2], cbv[2], sbv[2];
#pragma unroll
  for (int h = 0; h < 2; ++h) {
    xv[h] = *(const u32x4*)(x + (size_t)row * D_ + col + 8 * h);
    gav[h] = *(const u32x4*)(modb + 1024 + col + 8 * h); sbv[h] = *(const u32x4*)(modb + 1536 + col + 8 * h); cbv[h] = *(const u32x4*)(modb + 2048 + col + 8 * h);
  }
  constexpr int DEPTH = 8;
  WStage<2> ring[DEPTH];
  const rsrc_t rW = mk_rsrc(w1, D_ * D_ * 2);
  const unsigned woff = (unsigned)(8 * g4) * 1024u + (unsigned)(nh * 256 + wid * 32 + 2 * a) * 2u;
#pragma unroll
  for (int s = 0; s < DEPTH; ++s) w_load<2>(ring[s], rW, woff, (unsigned)(32 * s) * 1024u, 1024u);

  float v[16];
#pragma unroll
  for (int h = 0; h < 2; ++h)
#pragma unroll
    for (int e = 0; e < 4; ++e) {
      float lo = 0, hi = 0;
#pragma unroll
      for (int g = 0; g < 8; ++g) { lo += bflo(pv[g][h][e]); hi += bfhi(pv[g][h][e]); }
      const unsigned ar = pk2(lo, hi);
      const unsigned t = pk2(bflo(gav[h][e]) * bflo(ar), bfhi(gav[h][e]) * bfhi(ar));
      const unsigned x2 = pk2(bflo(xv[h][e]) + bflo(t), bfhi(xv[h][e]) + bfhi(t));
      xv[h][e] = x2; v[8 * h + 2 * e] = bflo(x2); v[8 * h + 2 * e + 1] = bfhi(x2);
    }
  if (nh == 0) { *(u32x4*)(x2o + (size_t)row * D_ + col) = xv[0]; *(u32x4*)(x2o + (size_t)row * D_ + col + 8) = xv[1]; }
  auto row_stats = [&](f32x2* rd, float& mean, float& var) {
    float s = 0, q = 0;
#pragma unroll
    for (int e = 0; e < 16; ++e) { s += v[e]; q += v[e] * v[e]; }
    s = swap32_add(swap16_add(s)); q = swap32_add(swap16_add(q));
    if (g4 == 0) rd[wid * 16 + a] = (f32x2){s, q};
    __syncthreads();
    s = 0; q = 0;
#pragma unroll
    for (int w = 0; w < 8; ++w) { f32x2 t = rd[w * 16 + a]; s += t[0]; q += t[1]; }
    mean = s * (1.f / 512.f); var = fmaxf(q * (1.f / 512.f) - mean * mean, 0.f);
  };
  float mean, var, mb, db, r;
  row_stats(red, mean, var); ln_consts(mean, var, mb, db, r);
  u32x4 hv[2];
#pragma unroll
  for (int h = 0; h < 2; ++h)
#pragma unroll
    for (int e = 0; e < 4; ++e) {
      const float y0 = ln_apply(v[8 * h + 2 * e], mb, db, r), y1 = ln_apply(v[8 * h + 2 * e + 1], mb, db, r);
      const unsigned t = pk2(y0 * bflo(cbv[h][e]), y1 * bfhi(cbv[h][e]));
      const unsigned hb = pk2(bflo(t) + bflo(sbv[h][e]), bfhi(t) + bfhi(sbv[h][e]));
      hv[h][e] = hb; v[8 * h + 2 * e] = bflo(hb); v[8 * h + 2 * e + 1] = bfhi(hb);
    }
  if (nh == 1) { *(u32x4*)(hbo + (size_t)row * D_ + col) = hv[0]; *(u32x4*)(hbo + (size_t)row * D_ + col + 8) = hv[1]; }
  row_stats(red + 128, mean, var); ln_consts(mean, var, mb, db, r);
#pragma unroll
  for (int h = 0; h < 2; ++h) {
    u32x4 o;
#pragma unroll
    for (int e = 0; e < 4; ++e) o[e] = pk2(div_rn(rb(v[8 * h + 2 * e] - mb), db, r), div_rn(rb(v[8 * h + 2 * e + 1] - mb), db, r));
    *(u32x4*)(lds + a * HT_STRIDE + (col + 8 * h) * 2) = o;
  }
  __syncthreads();
  f32x4 acc[2][1] = {};
  proj_gemm<2, 1, 16, DEPTH, HT_STRIDE>(acc, ring, rW, woff, 1024u, lds + a * HT_STRIDE + g4 * 16);
  u32x4 o;
#pragma unroll
  for (int rg = 0; rg < 4; ++rg) o[rg] = pk2(gelu_tanh(rb(acc[0][0][rg])), gelu_tanh(rb(acc[1][0][rg])));
  *(u32x4*)(h1o + (size_t)row * D_ + nh * 256 + wid * 32 + g4 * 8) = o;
}

__global__ __launch_bounds__(256, 2) void k3_g2(const u16* __restrict__ h1, const u16* __restrict__ hb, const u16* __restrict__ w2, u16* __restrict__ x1o) {
  __shared__ __attribute__((aligned(16))) char lds[32 * HT_STRIDE];
  const int tid = threadIdx.x, lane = tid & 63, a = lane & 15, g4 = lane >> 4;
  const int wid = (int)rfl((unsigned)tid >> 6);
  const int b = blockIdx.x, rt = (b & 7) + 8 * (b >> 5), nq = (b >> 3) & 3;
  const rsrc_t rH = mk_rsrc(h1, S_ * D_ * 2);
  const int sh = (g4 == 0) ? -1 : (g4 == 1) ? 1 : (g4 == 2) ? -2 : 2;
  u32x4 hv[8];
#pragma unroll
  for (int i = 0; i < 8; ++i) hv[i] = __builtin_amdgcn_raw_buffer_load_b128(rH, (unsigned)((rt * 32 + wid * 8 + i + sh) * 1024 + lane * 16), 0, 0);
  constexpr int DEPTH = 8;
  WStage<2> ring[DEPTH];
  const rsrc_t rW = mk_rsrc(w2, D_ * D_ * 2);
  const unsigned woff = (unsigned)(8 * g4) * 1024u + (unsigned)(nq * 128 + wid * 32 + 2 * a) * 2u;
#pragma unroll
  for (int s = 0; s < DEPTH; ++s) w_load<2>(ring[s], rW, woff, (unsigned)(32 * s) * 1024u, 1024u);
  u32x4 hbv[2];
#pragma unroll
  for (int qt = 0; qt < 2; ++qt) hbv[qt] = *(const u32x4*)(hb + (size_t)(rt * 32 + qt * 16 + a) * D_ + nq * 128 + wid * 32 + g4 * 8);
#pragma unroll
  for (int i = 0; i < 8; ++i) *(u32x4*)(lds + (wid * 8 + i) * HT_STRIDE + lane * 16) = hv[i];
  __syncthreads();
  f32x4 acc[2][2] = {};
  proj_gemm<2, 2, 16, DEPTH, HT_STRIDE>(acc, ring, rW, woff, 1024u, lds + a * HT_STRIDE + g4 * 16);
#pragma unroll
  for (int qt = 0; qt < 2; ++qt) {
    u32x4 o;
#pragma unroll
    for (int rg = 0; rg < 4; ++rg) o[rg] = pk2(bflo(hbv[qt][rg]) + rb(acc[0][qt][rg]), bfhi(hbv[qt][rg]) + rb(acc[1][qt][rg]));
    *(u32x4*)(x1o + (size_t)(rt * 32 + qt * 16 + a) * D_ + nq * 128 + wid * 32 + g4 * 8) = o;
  }
}

__global__ __launch_bounds__(512, 2) void k4_g3(const u16* __restrict__ x1, const u16* __restrict__ w3, u16* __restrict__ uo) {
  extern __shared__ __attribute__((aligned(16))) char lds[];
  const int tid = threadIdx.x, lane = tid & 63, a = lane & 15, g4 = lane >> 4;
  const int wid = (int)rfl((unsigned)tid >> 6);
  const int ns = blockIdx.x & 7, rbk = blockIdx.x >> 3;
  u32x4 xv[8];
#pragma unroll
  for (int i = 0; i < 8; ++i) xv[i] = *(const u32x4*)(x1 + (size_t)(rbk * 64 + wid * 8 + i) * D_ + lane * 8);
  constexpr int DEPTH = 6;
  WStage<2> ring[DEPTH];
  const rsrc_t rW = mk_rsrc(w3, D_ * F_ * 2);
  const int wn = wid < 7 ? wid : 6;
  const unsigned woff = (unsigned)(8 * g4) * (F_ * 2u) + (unsigned)(ns * 224 + wn * 32 + 2 * a) * 2u;
  if (wid < 7) {
#pragma unroll
    for (int s = 0; s < DEPTH; ++s) w_load<2>(ring[s], rW, woff, (unsigned)(32 * s) * (F_ * 2u), F_ * 2u);
  }
#pragma unroll
  for (int i = 0; i < 8; ++i) {
    float f[8];
#pragma unroll
    for (int e = 0; e < 4; ++e) { f[2 * e] = bflo(xv[i][e]); f[2 * e + 1] = bfhi(xv[i][e]); }
    float s = 0;
#pragma unroll
    for (int e = 0; e < 8; ++e) s += f[e];
    const float mean = wave_sum(s) * (1.f / 512.f);
    float q = 0;
#pragma unroll
    for (int e = 0; e < 8; ++e) { float d = f[e] - mean; q += d * d; }
    const float var = wave_sum(q) * (1.f / 512.f);
    float mb, db, r; ln_consts(mean, var, mb, db, r);
    u32x4 o;
#pragma unroll
    for (int e = 0; e < 4; ++e) o[e] = pk2(div_rn(rb(f[2 * e] - mb), db, r), div_rn(rb(f[2 * e + 1] - mb), db, r));
    *(u32x4*)(lds + (wid * 8 + i) * HT_STRIDE + lane * 16) = o;
  }
  __syncthreads();
  if (wid < 7) {
    f32x4 acc[2][4] = {};
    proj_gemm<2, 4, 16, DEPTH, HT_STRIDE>(acc, ring, rW, woff, F_ * 2u, lds + a * HT_STRIDE + g4 * 16);
#pragma unroll
    for (int qt = 0; qt < 4; ++qt) {
      u32x4 o;
#pragma unroll
      for (int rg = 0; rg < 4; ++rg) o[rg] = pk2(gelu_tanh(rb(acc[0][qt][rg])), gelu_tanh(rb(acc[1][qt][rg])));
      *(u32x4*)(uo + (size_t)(rbk * 64 + qt * 16 + a) * F_ + ns * 224 + wid * 32 + g4 * 8) = o;
    }
  }
}

__global__ __launch_bounds__(512, 2) void k5_g4(const u16* __restrict__ u, const u16* __restrict__ w4, const u16* __restrict__ x1, const u16* __restrict__ x2,
                                                const u16* __restrict__ modb, u16* __restrict__ out) {
  extern __shared__ __attribute__((aligned(16))) char lds[];
  const int tid = threadIdx.x, lane = tid & 63, a = lane & 15, g4 = lane >> 4;
  const int wid = (int)rfl((unsigned)tid >> 6);
  const int ns = blockIdx.x & 7, rbk = blockIdx.x >> 3;
  const rsrc_t rW = mk_rsrc(w4, F_ * D_ * 2), rU = mk_rsrc(u, S_ * F_ * 2);
  const unsigned woff = (unsigned)(wid * 224 + 8 * g4) * 1024u + (unsigned)(ns * 64 + 4 * a) * 2u;
  const unsigned uoff = (unsigned)(rbk * 64 + a) * (F_ * 2u) + (unsigned)(wid * 224 + 8 * g4) * 2u;
  WStage<4> ws[7]; u32x4 ub[7][4];
#pragma unroll
  for (int s = 0; s < 7; ++s) {
    w_load<4>(ws[s], rW, woff, (unsigned)(32 * s) * 1024u, 1024u);
#pragma unroll
    for (int qt = 0; qt < 4; ++qt) ub[s][qt] = __builtin_amdgcn_raw_buffer_load_b128(rU, uoff, (unsigned)(qt * 16 * F_ * 2 + 64 * s), 0);
  }
  const int oqt = wid >> 1, orp = wid & 1, orow = rbk * 64 + oqt * 16 + a, ocol = ns * 64 + g4 * 16 + orp * 8;
  const u32x4 x1v = *(const u32x4*)(x1 + (size_t)orow * D_ + ocol), x2v = *(const u32x4*)(x2 + (size_t)orow * D_ + ocol), gbv = *(const u32x4*)(modb + 2560 + ocol);
  f32x4 acc[4][4] = {};
#pragma unroll
  for (int s = 0; s < 7; ++s) {
    bf16x8 A[4]; w_frags<4>(ws[s], A);
#pragma unroll
    for (int i = 0; i < 4; ++i)
#pragma unroll
      for (int qt = 0; qt < 4; ++qt) acc[i][qt] = MFMA16(A[i], __builtin_bit_cast(bf16x8, ub[s][qt]), acc[i][qt]);
  }
#pragma unroll
  for (int qt = 0; qt < 4; ++qt)
#pragma unroll
    for (int rg = 0; rg < 4; ++rg)
      *(f32x4*)(lds + ((wid * 4 + qt) * 4 + rg) * 1024 + lane * 16) = (f32x4){acc[0][qt][rg], acc[1][qt][rg], acc[2][qt][rg], acc[3][qt][rg]};
  __syncthreads();
  f32x4 s0 = {}, s1 = {};
#pragma unroll
  for (int w = 0; w < 8; ++w) {
    s0 += *(const f32x4*)(lds + ((w * 4 + oqt) * 4 + 2 * orp) * 1024 + lane * 16);
    s1 += *(const f32x4*)(lds + ((w * 4 + oqt) * 4 + 2 * orp + 1) * 1024 + lane * 16);
  }
  float h2[8] = {s0[0], s0[1], s0[2], s0[3], s1[0], s1[1], s1[2], s1[3]};
  u32x4 o;
#pragma unroll
  for (int e = 0; e < 4; ++e) {
    const unsigned hr = pk2(h2[2 * e], h2[2 * e + 1]);
    const unsigned t = pk2(bflo(x1v[e]) + bflo(hr), bfhi(x1v[e]) + bfhi(hr));
    const unsigned t2 = pk2(bflo(gbv[e]) * bflo(t), bfhi(gbv[e]) * bfhi(t));
    o[e] = pk2(bflo(x2v[e]) + bflo(t2), bfhi(x2v[e]) + bfhi(t2));
  }
  *(u32x4*)(out + (size_t)orow * D_ + ocol) = o;
}

extern "C" void kernel_launch(void* const* d_in, const int* in_sizes, int n_in, void* d_out, int out_size, void* d_ws, size_t ws_size, hipStream_t stream) {
  const u16* x = (const u16*)d_in[0]; const u16* kc = (const u16*)d_in[1]; const u16* vc = (const u16*)d_in[2];
  const u16* wq = (const u16*)d_in[3]; const u16* wo = (const u16*)d_in[4];
  const u16* w1 = (const u16*)d_in[6]; const u16* w2 = (const u16*)d_in[7]; const u16* w3 = (const u16*)d_in[8]; const u16* w4 = (const u16*)d_in[9];
  const u16* c = (const u16*)d_in[10]; const u16* wmod = (const u16*)d_in[11];
  char* ws = (char*)d_ws;
  u16* modb = (u16*)(ws + WS_MOD); u16* y = (u16*)(ws + WS_Y); u16* x2 = (u16*)(ws + WS_X2); u16* hb = (u16*)(ws + WS_HB);
  u16* h1 = (u16*)(ws + WS_H1); u16* x1 = (u16*)(ws + WS_X1); u16* u = (u16*)(ws + WS_U); u16* part = (u16*)(ws + WS_PART);
  hipFuncSetAttribute((const void*)k1_attn, hipFuncAttributeMaxDynamicSharedMemorySize, L1_END);
  hipFuncSetAttribute((const void*)k4_g3, hipFuncAttributeMaxDynamicSharedMemorySize, 64 * HT_STRIDE);
  hipFuncSetAttribute((const void*)k5_g4, hipFuncAttributeMaxDynamicSharedMemorySize, 131072);
  k0_mod_ln<<<304, 512, 0, stream>>>(x, c, wmod, modb, y);
  k1_attn<<<256, 512, L1_END, stream>>>(y, modb, kc, vc, wq, wo, part);
  k2_red_g1<<<256, 512, 0, stream>>>(x, modb, part, w1, x2, hb, h1);
  k3_g2<<<256, 256, 0, stream>>>(h1, hb, w2, x1);
  k4_g3<<<256, 512, 64 * HT_STRIDE, stream>>>(x1, w3, u);
  k5_g4<<<256, 512, 131072, stream>>>(u, w4, x1, x2, modb, (u16*)d_out);
}
```

```cpp
#include <hip/hip_runtime.h>
#include <hip/hip_bf16.h>
#include <type_traits>
#include <utility>

typedef unsigned short u16;
typedef __bf16 bf16x8 __attribute__((ext_vector_type(8)));
typedef __bf16 bf16x4 __attribute__((ext_vector_type(4)));
typedef __bf16 bf16x2 __attribute__((ext_vector_type(2)));
typedef float f32x16 __attribute__((ext_vector_type(16)));
typedef float f32x4 __attribute__((ext_vector_type(4)));
typedef float f32x2 __attribute__((ext_vector_type(2)));
typedef unsigned u32x2 __attribute__((ext_vector_type(2)));
typedef unsigned u32x4 __attribute__((ext_vector_type(4)));
typedef __amdgpu_buffer_rsrc_t rsrc_t;
#define DI __device__ __forceinline__
#define AI __attribute__((always_inline))
#define LDSP(T, p) ((__attribute__((address_space(3))) T*)(p))
#define SCHED_FENCE() __builtin_amdgcn_sched_barrier(0)
#define PIN(x) asm volatile("" : "+v"(x))
#define MEM_FENCE() asm volatile("" ::: "memory")
#define WAIT_VM0() __builtin_amdgcn_s_waitcnt(0x0F70)
#define STAMP(i)
#define STAMPK(k, i)
#define RT(k, i)

constexpr int S_ = 2048, D_ = 512, HQ = 32, HKV = 8, DH = 128, SK = 2048, WIN = 512, F_ = 1792;

constexpr size_t NXB = (size_t)S_ * D_ * 2;
constexpr size_t WS_MOD = 0;
constexpr size_t WS_Y   = 8192;
constexpr size_t WS_X2  = WS_Y  + NXB;
constexpr size_t WS_HB  = WS_X2 + NXB;
constexpr size_t WS_H1  = WS_HB + NXB;
constexpr size_t WS_X1  = WS_H1 + NXB;
constexpr size_t WS_U   = WS_X1 + NXB;
constexpr size_t WS_PART = WS_U + (size_t)S_ * F_ * 2;
constexpr size_t WS_WQT = WS_PART + 8 * NXB;
constexpr size_t WS_WOT = WS_WQT + (size_t)D_ * 4096 * 2;
constexpr size_t WS_W1T = WS_WOT + (size_t)D_ * 4096 * 2;
constexpr size_t WS_W2T = WS_W1T + (size_t)D_ * D_ * 2;
constexpr size_t WS_W3T = WS_W2T + (size_t)D_ * D_ * 2;
constexpr size_t WS_W4T = WS_W3T + (size_t)D_ * F_ * 2;
constexpr size_t WS_ST  = WS_W4T + (size_t)D_ * F_ * 2;
constexpr size_t WS_FLAG = WS_ST + (size_t)S_ * 32;
constexpr int FL_W = 0, FL_P = 8, FL_H1 = 40, FL_X1 = 168, FL_U = 200, FL_N = 232;
constexpr size_t WS_END = WS_FLAG + FL_N * 128;

DI float bflo(unsigned u) { return __uint_as_float(u << 16); }
DI float bfhi(unsigned u) { return __uint_as_float(u & 0xffff0000u); }
DI unsigned pk2(float lo, float hi) { f32x2 v = {lo, hi}; return __builtin_bit_cast(unsigned, __builtin_convertvector(v, bf16x2)); }
DI float rb(float x) { return __uint_as_float(pk2(0.f, x)); }
DI rsrc_t mk_rsrc(const void* p, unsigned bytes) { return __builtin_amdgcn_make_buffer_rsrc((void*)p, (short)0, (int)bytes, 0x00020000); }
DI unsigned rfl(unsigned x) { return __builtin_amdgcn_readfirstlane(x); }
DI bf16x8 ldfrag(rsrc_t r, unsigned voff, unsigned soff) { return __builtin_bit_cast(bf16x8, __builtin_amdgcn_raw_buffer_load_b128(r, voff, soff, 0)); }
template <int CTRL> DI float dpp_add(float v) {
  return v + __uint_as_float(__builtin_amdgcn_update_dpp(0u, __float_as_uint(v), CTRL, 0xf, 0xf, true));
}
DI float swap16_add(float v) { auto r = __builtin_amdgcn_permlane16_swap(__float_as_uint(v), __float_as_uint(v), false, false); return __uint_as_float(r[0]) + __uint_as_float(r[1]); }
DI float swap32_add(float v) { auto r = __builtin_amdgcn_permlane32_swap(__float_as_uint(v), __float_as_uint(v), false, false); return __uint_as_float(r[0]) + __uint_as_float(r[1]); }
DI float swap32_max(float v) { auto r = __builtin_amdgcn_permlane32_swap(__float_as_uint(v), __float_as_uint(v), false, false); return fmaxf(__uint_as_float(r[0]), __uint_as_float(r[1])); }
DI float wave_sum(float v) {
  v = dpp_add<0xB1>(v); v = dpp_add<0x4E>(v); v = dpp_add<0x141>(v); v = dpp_add<0x140>(v);
  v = swap16_add(v); v = swap32_add(v); return v;
}
DI float div_rn(float a, float d, float r) {
  float q = a * r; float e = __builtin_fmaf(-q, d, a); return __builtin_fmaf(e, r, q);
}
DI float gelu_tanh(float x) {
  const float u = 0.7978845608028654f * (x + 0.044715f * x * x * x);
  return x * __builtin_amdgcn_rcpf(1.f + __builtin_amdgcn_exp2f(-2.885390081777927f * u));
}
DI void ln_consts(float mean, float var, float& m_bf, float& d_bf, float& r) {
  m_bf = rb(mean); d_bf = rb(__builtin_sqrtf(rb(rb(var) + 1e-5f))); r = __builtin_amdgcn_rcpf(d_bf);
}
DI float ln_apply(float x, float m_bf, float d_bf, float r) { return rb(div_rn(rb(x - m_bf), d_bf, r)); }
DI u32x4 ln_row(const u32x4 xv) {
  float f[8];
#pragma unroll
  for (int e = 0; e < 4; ++e) { f[2 * e] = bflo(xv[e]); f[2 * e + 1] = bfhi(xv[e]); }
  float s = 0;
#pragma unroll
  for (int e = 0; e < 8; ++e) s += f[e];
  const float mean = wave_sum(s) * (1.f / 512.f);
  float q = 0;
#pragma unroll
  for (int e = 0; e < 8; ++e) { float d = f[e] - mean; q += d * d; }
  const float var = wave_sum(q) * (1.f / 512.f);
  float mb, db, r; ln_consts(mean, var, mb, db, r);
  u32x4 o;
#pragma unroll
  for (int e = 0; e < 4; ++e) o[e] = pk2(div_rn(rb(f[2 * e] - mb), db, r), div_rn(rb(f[2 * e + 1] - mb), db, r));
  return o;
}

#define MFMA16(a, b, c) __builtin_amdgcn_mfma_f32_16x16x32_bf16((a), (b), (c), 0, 0, 0)
#define MFMA32(a, b, c) __builtin_amdgcn_mfma_f32_32x32x16_bf16((a), (b), (c), 0, 0, 0)

DI void dma16(unsigned lds_dst, unsigned voff, rsrc_t r, unsigned soff) {
  asm volatile("s_mov_b32 m0, %0\n\ts_nop 0\n\tbuffer_load_dwordx4 %1, %2, %3 offen lds" ::"s"(lds_dst), "v"(voff), "s"(r), "s"(soff) : "memory");
}

struct Retile { u32x2 d[8]; };
DI void retile_load(Retile& t, const u16* __restrict__ W, int ldw, int k0, bool perm, int n0, int lane) {
  const int a = lane & 15, g4 = lane >> 4;
  const u16* p = W + (size_t)(k0 + (perm ? 16 : 8) * g4) * ldw + n0 + 4 * a;
  const int s2 = (perm ? 4 : 2) * ldw;
#pragma unroll
  for (int j = 0; j < 8; ++j) t.d[j] = *(const u32x2*)(p + (j >> 1) * s2 + (j & 1) * ldw);
}
DI u32x4 retile_frag(const Retile& t, int i) {
  u32x4 f;
#pragma unroll
  for (int p = 0; p < 4; ++p) f[p] = __builtin_amdgcn_perm(t.d[2 * p + 1][i >> 1], t.d[2 * p][i >> 1], (i & 1) ? 0x07060302u : 0x05040100u);
  return f;
}

struct K0Args { const u16 *x, *c, *wmod, *wq, *w1; char* ws; };
constexpr int K0_BLOCKS = 304 + 128 + 16;
__global__ __launch_bounds__(512) void k0_prep(const K0Args p) {
  __shared__ float red[8][64];
  const int tid = threadIdx.x, lane = tid & 63, wid = tid >> 6;
  int b = blockIdx.x;
#define WSP p.ws
  RT(0, 0);
  if (b < 48) {
    const int n0 = b * 64, cg = tid & 7, kq = tid >> 3;
    u32x4 wv[8]; float sv[8];
#pragma unroll
    for (int i = 0; i < 8; ++i) wv[i] = *(const u32x4*)(p.wmod + (size_t)(kq + 64 * i) * 3072 + n0 + cg * 8);
#pragma unroll
    for (int i = 0; i < 8; ++i) { float cv = bflo(p.c[kq + 64 * i]); sv[i] = rb(cv / (1.f + __expf(-cv))); }
    float acc[8] = {};
#pragma unroll
    for (int i = 0; i < 8; ++i)
#pragma unroll
      for (int e = 0; e < 4; ++e) { acc[2 * e] += sv[i] * bflo(wv[i][e]); acc[2 * e + 1] += sv[i] * bfhi(wv[i][e]); }
#pragma unroll
    for (int e = 0; e < 8; ++e) { float v = acc[e]; v += __shfl_xor(v, 8); v = swap16_add(v); v = swap32_add(v); acc[e] = v; }
    if (lane < 8)
#pragma unroll
      for (int e = 0; e < 8; ++e) red[wid][lane * 8 + e] = acc[e];
    __syncthreads();
    if (tid < 64) {
      float s = 0;
#pragma unroll
      for (int w = 0; w < 8; ++w) s += red[w][tid];
      s = rb(s);
      const int n = n0 + tid, seg = n >> 9;
      if (seg == 1 || seg == 4) s = rb(1.f + s);
      ((u16*)(p.ws + WS_MOD))[n] = (u16)(__float_as_uint(s) >> 16);
    }
    RT(0, 1);
    return;
  }
  b -= 48;
  if (b < 256) {
    const int row = b * 8 + wid;
    const u32x4 xv = *(const u32x4*)(p.x + (size_t)row * D_ + lane * 8);
    u32x4 wv[8];
#pragma unroll
    for (int i = 0; i < 8; ++i) wv[i] = *(const u32x4*)(p.x + (size_t)((b >> 3) * 64 + wid * 8 + i) * D_ + lane * 8);
    float f[8];
#pragma unroll
    for (int e = 0; e < 4; ++e) { f[2 * e] = bflo(xv[e]); f[2 * e + 1] = bfhi(xv[e]); }
    float s = 0;
#pragma unroll
    for (int e = 0; e < 8; ++e) s += f[e];
    const float mean = wave_sum(s) * (1.f / 512.f);
    float q = 0;
#pragma unroll
    for (int e = 0; e < 8; ++e) { float d = f[e] - mean; q += d * d; }
    const float var = wave_sum(q) * (1.f / 512.f);
    float mb, db, r; ln_consts(mean, var, mb, db, r);
    if (lane == 0) *(f32x2*)(p.ws + WS_Y + (size_t)row * 8) = (f32x2){mb, db};
#pragma unroll
    for (int i = 0; i < 8; ++i) asm volatile("" ::"v"(wv[i]));
    RT(0, 1);
    return;
  }
  b -= 256;
  Retile t; char* dst;
  if (b < 128) { const int g = b & 7, s = b >> 3, ng = g * 8 + wid; retile_load(t, p.wq, 4096, 32 * s, false, 64 * ng, lane); dst = p.ws + WS_WQT + (size_t)(ng * 16 + s) * 4096; }
  else {
    const int s = b - 128; retile_load(t, p.w1, 512, 32 * s, false, 64 * wid, lane); dst = p.ws + WS_W1T + (size_t)(wid * 16 + s) * 4096;
    if (s == 0 && tid < FL_N) *(unsigned*)(p.ws + WS_FLAG + tid * 128) = 0u;
  }
#pragma unroll
  for (int i = 0; i < 4; ++i) *(u32x4*)(dst + i * 1024 + lane * 16) = retile_frag(t, i);
  RT(0, 1);
#undef WSP
#define WSP ws
}

template <int V> using IC = std::integral_constant<int, V>;
template <int N, class F, int... Is> DI void sfor_(F&& f, std::integer_sequence<int, Is...>) { (f(IC<Is>{}), ...); }
template <int N, class F> DI void sfor(F&& f) { sfor_<N>(f, std::make_integer_sequence<int, N>{}); }
template <int NI, int NQT, int NSTEP, int DEPTH, int NSET, int BD = 1, class ALoad, class BRead, class Hook>
DI void gemm_steps(f32x4 (&acc)[NSET][NI][NQT], bf16x8 (&ring)[DEPTH][NI], ALoad aload, BRead bread, Hook hook) {
  constexpr int NM = NI * NQT, PER = NSTEP / NSET;
  bf16x8 B[BD + 1][NQT];
#pragma unroll
  for (int s = 0; s < BD; ++s)
#pragma unroll
    for (int qt = 0; qt < NQT; ++qt) B[s][qt] = bread(s, qt);
  SCHED_FENCE();
  sfor<NSTEP * NM>([&](auto X) AI {
    constexpr int s = X.value / NM, m = X.value % NM, i = m / NQT, qt = m % NQT;
    if constexpr (s % PER == 0) acc[s / PER][i][qt] = MFMA16(ring[s % DEPTH][i], B[s % (BD + 1)][qt], (f32x4){});
    else                        acc[s / PER][i][qt] = MFMA16(ring[s % DEPTH][i], B[s % (BD + 1)][qt], acc[s / PER][i][qt]);
    if constexpr (m < NQT && s + BD < NSTEP) B[(s + BD) % (BD + 1)][m] = bread(s + BD, m);
    if constexpr (qt == NQT - 1 && s + DEPTH < NSTEP) ring[s % DEPTH][i] = aload(s + DEPTH, i);
    hook(IC<s>{}, IC<m>{});
    SCHED_FENCE();
  });
}

constexpr int HT_STRIDE = 1056, QX_STRIDE = 272;
constexpr int L1_HT = 0, L1_K = 69632, L1_V = L1_K + 3 * 16384, L1_END = L1_V + 2 * 16384;
#define PRIO_Q 11
#define PRIO_O 21
#define PRIO_A 0
#define GBAR 1
#define WO_SC1 1
#define OPASS 2
#define PF_ON 1
#define RT_AUX 16
#define PART_AUX 16
constexpr float SM_C = 0.12751743082459868f;
constexpr float SM_LIMIT = 1099511627776.f;

DI float max16(const f32x16& S) {
  float m = fmaxf(S[0], S[1]);
#pragma unroll
  for (int r = 2; r < 16; ++r) m = fmaxf(m, S[r]);
  return m;
}
DI unsigned scale_pk(unsigned p, float f) { return pk2(bflo(p) * f, bfhi(p) * f); }
DI void sm_half(const f32x16& S, float negmc, float& l0, float& l1, u32x4& pa, u32x4& pb) {
#pragma unroll
  for (int p = 0; p < 4; ++p) {
    const float e0 = __builtin_amdgcn_exp2f(__builtin_fmaf(S[2 * p], SM_C, negmc)), e1 = __builtin_amdgcn_exp2f(__builtin_fmaf(S[2 * p + 1], SM_C, negmc));
    l0 += e0; l1 += e1; pa[p] = pk2(e0, e1);
  }
#pragma unroll
  for (int p = 0; p < 4; ++p) {
    const float e0 = __builtin_amdgcn_exp2f(__builtin_fmaf(S[8 + 2 * p], SM_C, negmc)), e1 = __builtin_amdgcn_exp2f(__builtin_fmaf(S[8 + 2 * p + 1], SM_C, negmc));
    l0 += e0; l1 += e1; pb[p] = pk2(e0, e1);
  }
}
struct SmPipe { float x[16], e[16]; };
DI void sm_gap(const int m, SmPipe& sp, const f32x16& S, float negmc, float& l0, float& l1, u32x4& pa, u32x4& pb) {
#pragma unroll
  for (int e = 0; e < 16; ++e) {
    const int f = e * 13 / 16;
    if (f + 2 == m) {
      if (e & 1) {
        l1 += sp.e[e]; PIN(l1);
        unsigned w = pk2(sp.e[e - 1], sp.e[e]); PIN(w);
        if (e < 8) pa[e >> 1] = w; else pb[(e - 8) >> 1] = w;
      } else { l0 += sp.e[e]; PIN(l0); }
    }
  }
#pragma unroll
  for (int e = 0; e < 16; ++e) if (e * 13 / 16 + 1 == m) sp.e[e] = __builtin_amdgcn_exp2f(sp.x[e]);
#pragma unroll
  for (int e = 0; e < 16; ++e) if (e * 13 / 16 == m) sp.x[e] = __builtin_fmaf(S[e], SM_C, negmc);
}
DI void pack_acc16(const f32x4 (&acc)[4][4], int qt, u32x4& w0, u32x4& w1) {
#pragma unroll
  for (int rg = 0; rg < 2; ++rg) {
    w0[2 * rg] = pk2(acc[0][qt][rg], acc[1][qt][rg]); w0[2 * rg + 1] = pk2(acc[2][qt][rg], acc[3][qt][rg]);
    w1[2 * rg] = pk2(acc[0][qt][2 + rg], acc[1][qt][2 + rg]); w1[2 * rg + 1] = pk2(acc[2][qt][2 + rg], acc[3][qt][2 + rg]);
  }
}

typedef __attribute__((address_space(1))) unsigned gu32;
typedef __attribute__((address_space(1))) unsigned long long gu64;
constexpr unsigned SPIN_CAP = 1u << 20;
struct K1Args { const u16 *x, *kc, *vc, *wo, *w2, *w3, *w4; const float* lnst; char* ws; u16* out; };

__global__ __launch_bounds__(512, 2) __attribute__((amdgpu_waves_per_eu(2, 2)))
void k1_attn(const K1Args p) {
  extern __shared__ __attribute__((aligned(16))) char lds[];
  char* __restrict__ ws = p.ws;
  const int tid = threadIdx.x, lane = tid & 63;
  const int wid = (int)rfl((unsigned)tid >> 6);
  const int g = blockIdx.x & 7, rbk = blockIdx.x >> 3;
  const int a = lane & 15, g4 = lane >> 4, r32 = lane & 31, hh = lane >> 5;
  const unsigned ldsb = (unsigned)(size_t)lds;
  const unsigned fo = lane * 16;
  RT(1, 0);
  STAMP(0);
  const rsrc_t rK = mk_rsrc(p.kc + ((size_t)g * SK + (SK - WIN)) * DH, WIN * DH * 2), rV = mk_rsrc(p.vc + ((size_t)g * SK + (SK - WIN)) * DH, WIN * DH * 2);
  const unsigned drow = wid * 8 + ((lane >> 2) & 7);
  const unsigned kvoff = drow * 256 + (lane >> 5) * 64 + (((lane & 3) ^ ((drow >> 2) & 3)) * 16);
  const unsigned vvoff = drow * 256 + (lane >> 5) * 64 + (lane & 3) * 16;
  const unsigned kdst = ldsb + L1_K + wid * 2048, vdst = ldsb + L1_V + wid * 2048;
  auto dmaK = [&](int t) AI { dma16(kdst + (t % 3) * 16384, kvoff, rK, t * 16384); dma16(kdst + (t % 3) * 16384 + 1024, kvoff, rK, t * 16384 + 128); };
  auto dmaV = [&](int t) AI { dma16(vdst + (t & 1) * 16384, vvoff, rV, t * 16384); dma16(vdst + (t & 1) * 16384 + 1024, vvoff, rV, t * 16384 + 128); };
  constexpr int DEPTH = 6;
  bf16x8 ring[DEPTH][4];
  const rsrc_t rWq = mk_rsrc(ws + WS_WQT + (size_t)(g * 8 + wid) * 65536, 65536);
  {
    const u32x4 sav = *(const u32x4*)(ws + WS_MOD + fo), cav = *(const u32x4*)(ws + WS_MOD + 1024 + fo);
    u32x4 xv[8];
#pragma unroll
    for (int i = 0; i < 8; ++i) xv[i] = *(const u32x4*)((const char*)p.x + (size_t)(rbk * 64 + wid * 8 + i) * 1024 + fo);
    float st[16];
#pragma unroll
    for (int i = 0; i < 16; ++i) st[i] = p.lnst[(rbk * 64 + wid * 8) * 2 + i];
    SCHED_FENCE();
    float ca[8], sa[8];
#pragma unroll
    for (int e = 0; e < 4; ++e) { ca[2 * e] = bflo(cav[e]); ca[2 * e + 1] = bfhi(cav[e]); sa[2 * e] = bflo(sav[e]); sa[2 * e + 1] = bfhi(sav[e]); }
#pragma unroll
    for (int hf = 0; hf < 2; ++hf) {
      float lo[16], hi[16], rr[4]; unsigned t[16];
#define H_STAGE(body) _Pragma("unroll") for (int j = 0; j < 16; ++j) { const int i = hf * 4 + (j >> 2), d = j & 3; (void)i; (void)d; body; } SCHED_FENCE()
#pragma unroll
      for (int i = 0; i < 4; ++i) rr[i] = __builtin_amdgcn_rcpf(st[2 * (hf * 4 + i) + 1]);
      H_STAGE(lo[j] = bflo(xv[i][d]); hi[j] = bfhi(xv[i][d]));
      H_STAGE(lo[j] -= st[2 * i]; hi[j] -= st[2 * i]);
      H_STAGE(lo[j] = rb(lo[j]); hi[j] = rb(hi[j]));
      H_STAGE(lo[j] *= rr[j >> 2]; hi[j] *= rr[j >> 2]);
      H_STAGE(lo[j] = rb(lo[j]); hi[j] = rb(hi[j]));
#pragma unroll
      for (int i = 0; i < 4; ++i) ring[hf][i] = ldfrag(rWq, fo, (hf * 4 + i) * 1024);
      H_STAGE(lo[j] *= ca[2 * d]; hi[j] *= ca[2 * d + 1]);
      H_STAGE(lo[j] = rb(lo[j]); hi[j] = rb(hi[j]));
      H_STAGE(lo[j] += sa[2 * d]; hi[j] += sa[2 * d + 1]);
      H_STAGE(t[j] = pk2(lo[j], hi[j]));
#pragma unroll
      for (int i = 0; i < 4; ++i) *(u32x4*)(lds + L1_HT + (wid * 8 + hf * 4 + i) * HT_STRIDE + fo) = (u32x4){t[4 * i], t[4 * i + 1], t[4 * i + 2], t[4 * i + 3]};
#pragma unroll
      for (int i = 0; i < 2; ++i) ring[2][hf * 2 + i] = ldfrag(rWq, fo, (8 + hf * 2 + i) * 1024);
      SCHED_FENCE();
    }
  }

  Retile rtl; const u16* rsrc_w; int rld, rk0, rn0; bool rperm; size_t rdst; unsigned rhalf = 2048;
  if (wid < 4) { const int id = rbk * 4 + wid, s = id >> 3, ng = id & 7; rsrc_w = p.wo; rld = 512; rk0 = g * 512 + 32 * s; rn0 = 64 * ng; rperm = false; rdst = WS_WOT + (size_t)(g * 8 + ng) * 65536 + s * 2048; rhalf = 32768; }
  else {
    int id = blockIdx.x * 4 + wid - 4, s, ng; rperm = true;
    if (id < 128)      { ng = id >> 4; s = id & 15; rsrc_w = p.w2; rld = 512; rdst = WS_W2T + (size_t)id * 4096; }
    else if (id < 576) { id -= 128; ng = id >> 4; s = id & 15; rsrc_w = p.w3; rld = F_; rdst = WS_W3T + (size_t)id * 4096; }
    else               { id -= 576; ng = id & 7; s = id >> 3; rsrc_w = p.w4; rld = 512; rdst = WS_W4T + (size_t)(ng * 56 + s) * 4096; }
    rk0 = 64 * (s >> 1) + 2 * (s & 1); rn0 = 64 * ng;
  }
  gu32* flags = (gu32*)(ws + WS_FLAG);
  gu32* flag = flags + (FL_W + g) * 32;
  gu32* flagw = flags + (FL_W + (lane & 7)) * 32;
  STAMP(1);
  __syncthreads();
  STAMP(2);
  if (wid >= 4 && PRIO_Q > 0) __builtin_amdgcn_s_setprio(1);

  const char* bl = lds + L1_HT + a * HT_STRIDE + g4 * 16;
  auto bread = [&](int s, int qt) AI { return *(const bf16x8*)(bl + qt * 16 * HT_STRIDE + 64 * (s & 15)); };
  f32x16 o[4]; bf16x8 qf[8]; float l0, l1, negmc;
  {
    f32x4 acc[1][4][4];
    gemm_steps<4, 4, 16, DEPTH, 1>(acc, ring, [&](int s, int i) AI { return ldfrag(rWq, fo, (s * 4 + i) * 1024); }, bread, [&](auto S_, auto M_) AI {
      constexpr int s = S_.value, m = M_.value;
      if constexpr (s < 3 && (m & 3) == 1) ring[3 + s][m >> 2] = ldfrag(rWq, fo, ((3 + s) * 4 + (m >> 2)) * 1024);
      if constexpr (s == PRIO_Q && m == 0) __builtin_amdgcn_s_setprio(0);
      if constexpr (s == 11 && m == 8) { WAIT_VM0(); dmaK(0); }
      if constexpr (s == 12 && m == 0) dmaK(1);
      if constexpr (s == 12 && m == 8) dmaV(0);
      if constexpr (s == 13 && m == 0) dmaK(2);
      if constexpr (s == 13 && m == 8) dmaV(1);
    });
    STAMP(3);
    __syncthreads();
#pragma unroll
    for (int qt = 0; qt < 4; ++qt) {
      u32x4 w0, w1; pack_acc16(acc[0], qt, w0, w1);
      char* dst = lds + L1_HT + ((wid >> 1) * 64 + qt * 16 + a) * QX_STRIDE + ((wid & 1) * 64 + g4 * 16) * 2;
      *(u32x4*)dst = w0; *(u32x4*)(dst + 16) = w1;
    }
    asm volatile("s_waitcnt vmcnt(0)" ::: "memory");
    __syncthreads();
#pragma unroll
    for (int s = 0; s < 8; ++s) qf[s] = *(const bf16x8*)(lds + L1_HT + ((wid >> 1) * 64 + (wid & 1) * 32 + r32) * QX_STRIDE + 32 * s + 16 * hh);
  }
  STAMP(4);

  const unsigned kx = (r32 >> 2) & 3;
  const char* kb0 = lds + L1_K + 2048 * (r32 >> 3) + 64 * (r32 & 7) + 16 * ((0 + hh) ^ kx);
  const char* kb1 = lds + L1_K + 2048 * (r32 >> 3) + 64 * (r32 & 7) + 16 * ((2 + hh) ^ kx);
  const char* vb = lds + L1_V + (4 * hh + ((lane >> 2) & 3)) * 64 + ((lane >> 4) & 1) * 32 + (lane & 3) * 8;
  auto kfrag = [&](int t, int m) AI { return *(const bf16x8*)(((m & 1) ? kb1 : kb0) + (t % 3) * 16384 + 512 * ((m & 7) >> 1) + 8192 * (m >> 3)); };
  auto vfrag = [&](int t, int m) AI {
    const char* q = vb + (t & 1) * 16384 + (m >> 2) * 4096 + (m & 3) * 512;
    bf16x4 lo = __builtin_amdgcn_ds_read_tr16_b64_v4bf16(LDSP(bf16x4, q)), hi = __builtin_amdgcn_ds_read_tr16_b64_v4bf16(LDSP(bf16x4, q + 2048));
    return __builtin_shufflevector(lo, hi, 0, 1, 2, 3, 4, 5, 6, 7);
  };
  auto fixup = [&](const f32x16& S, float l0p, float l1p, u32x4& pa, u32x4& pb, bool pend, u32x4& pend0, u32x4& pend1) AI {
    const float mx = swap32_max(__builtin_fmaf(max16(S), SM_C, negmc));
    const float dm = fmaxf(__builtin_ceilf(mx), 0.f);
    const float f = __builtin_amdgcn_exp2f(-dm);
    negmc -= dm; l0 = l0p * f; l1 = l1p * f;
#pragma unroll
    for (int db = 0; db < 4; ++db) o[db] *= f;
    if (pend) {
#pragma unroll
      for (int q = 0; q < 4; ++q) { pend0[q] = scale_pk(pend0[q], f); pend1[q] = scale_pk(pend1[q], f); }
    }
    sm_half(S, negmc, l0, l1, pa, pb);
  };

  f32x16 Sa0, Sa1, Sb0, Sb1;
  u32x4 Pa0, Pa1, Pb0, Pb1, P2, P3;
  bf16x8 kf[4], vf[4];
  {
#pragma unroll
    for (int m = 0; m < 16; ++m) {
      const bf16x8 k = kfrag(0, m);
      if (m < 8) Sa0 = MFMA32(k, qf[m & 7], m == 0 ? (f32x16){} : Sa0); else Sa1 = MFMA32(k, qf[m & 7], m == 8 ? (f32x16){} : Sa1);
    }
  }
#pragma unroll
  for (int m = 0; m < 3; ++m) kf[m] = kfrag(1, m);
  negmc = -swap32_max(fmaxf(max16(Sa0), max16(Sa1))) * SM_C;
  l0 = 0.f; l1 = 0.f;
  sm_half(Sa0, negmc, l0, l1, Pa0, Pa1);
#pragma unroll
  for (int db = 0; db < 4; ++db) o[db] = (f32x16){};
  SCHED_FENCE();

  const rsrc_t rWo = mk_rsrc(ws + WS_WOT + (size_t)(g * 8 + wid) * 65536, 65536);
  const rsrc_t rRt = mk_rsrc(ws + rdst, 36864);
  bf16x8 ring2[12][2];
  auto wo_load = [&](int sg, int i) AI { return __builtin_bit_cast(bf16x8, __builtin_amdgcn_raw_buffer_load_b128(rWo, fo, OPASS == 2 ? (sg * 2 + i) * 1024 : ((i >> 1) * 32 + sg * 2 + (i & 1)) * 1024, WO_SC1 ? 16 : 0)); };
  unsigned flagv = 0;

  auto tile = [&](auto T_, const f32x16& C1, f32x16& N0, f32x16& N1, u32x4& Pc0, u32x4& Pc1, u32x4& Pn0, u32x4& Pn1) AI {
    constexpr int T = T_.value;
    if (T == 4) STAMP(12);
    if (PRIO_A && wid >= 4) __builtin_amdgcn_s_setprio(1);
    {
      const float l0p = l0, l1p = l1; SmPipe sp;
#pragma unroll
      for (int m = 0; m < 16; ++m) {
        if (T < 7) {
          if (m < 8) N0 = MFMA32(kf[m & 3], qf[m & 7], m == 0 ? (f32x16){} : N0); else N1 = MFMA32(kf[m & 3], qf[m & 7], m == 8 ? (f32x16){} : N1);
          if (m + 3 < 16) kf[(m + 3) & 3] = kfrag(T + 1, m + 3);
        }
        if (m >= 13) vf[m - 13] = vfrag(T, m - 13);
        sm_gap(m, sp, C1, negmc, l0, l1, P2, P3);
        if (T == 7) ring2[m >> 1][m & 1] = wo_load(m >> 1, m & 1);
        if (T < 7) SCHED_FENCE();
      }
      MEM_FENCE();
      if (__builtin_expect(__any(!(l0 + l1 <= SM_LIMIT)), 0)) fixup(C1, l0p, l1p, P2, P3, true, Pc0, Pc1);
    }
    if (T == 4) STAMP(13);
    if (PRIO_A == 1) __builtin_amdgcn_s_setprio(0);
    {
      const float l0p = l0, l1p = l1; SmPipe sp;
#pragma unroll
      for (int m = 0; m < 16; ++m) {
        const u32x4 pp = (m < 4) ? Pc0 : (m < 8) ? Pc1 : (m < 12) ? P2 : P3;
        o[m & 3] = MFMA32(vf[m & 3], __builtin_bit_cast(bf16x8, pp), o[m & 3]);
        if (m + 3 < 16) vf[(m + 3) & 3] = vfrag(T, m + 3);
        if (T < 7) sm_gap(m, sp, N0, negmc, l0, l1, Pn0, Pn1);
        if (T == 7 && m < 8) ring2[8 + (m >> 1)][m & 1] = wo_load(8 + (m >> 1), m & 1);
        if (PRIO_A == 2 && m == 4) __builtin_amdgcn_s_setprio(0);
        if (T < 7 && m == 13) {
          if (T == 6 && wid == 0) {
            for (unsigned spins = 0; !__all(flagv == 32u) && spins < SPIN_CAP; ++spins) { __builtin_amdgcn_s_sleep(2); flagv = __hip_atomic_load(flagw, __ATOMIC_RELAXED, __HIP_MEMORY_SCOPE_AGENT); }
          }
          if (T == 4) STAMP(14);
          if (T == 2) WAIT_VM0();
          if (T == 0)      asm volatile("s_waitcnt lgkmcnt(0)" : "+v"(vf[2]), "+v"(vf[3])::"memory");
          else if (T == 1) asm volatile("s_waitcnt vmcnt(8) lgkmcnt(0)" : "+v"(vf[2]), "+v"(vf[3])::"memory");
          else if (T == 3) asm volatile("s_waitcnt vmcnt(4) lgkmcnt(0)" : "+v"(vf[2]), "+v"(vf[3])::"memory");
          else             asm volatile("s_waitcnt vmcnt(0) lgkmcnt(0)" : "+v"(vf[2]), "+v"(vf[3])::"memory");
          __builtin_amdgcn_s_barrier();
          MEM_FENCE();
          if (T == 4) STAMP(15);
          if (T == 4 && tid == 0) __hip_atomic_fetch_add(flag, 1u, __ATOMIC_RELAXED, __HIP_MEMORY_SCOPE_AGENT);
          if (T == 5 && wid == 0) flagv = __hip_atomic_load(flagw, __ATOMIC_RELAXED, __HIP_MEMORY_SCOPE_AGENT);
        }
        if (T < 6 && m == 13) { kf[0] = kfrag(T + 2, 0); kf[1] = kfrag(T + 2, 1); }
        if (T < 6 && m == 14) kf[2] = kfrag(T + 2, 2);
        if (T < 5 && m == 14) dmaK(T + 3);
        if (T < 6 && m == 15) dmaV(T + 2);
        if (T == 0 && m == 15) { retile_load(rtl, rsrc_w, rld, rk0, rperm, rn0, lane); MEM_FENCE(); }
        if (T == 2 && m == 15) {
#pragma unroll
          for (int i = 0; i < 4; ++i) __builtin_amdgcn_raw_buffer_store_b128(retile_frag(rtl, i), rRt, fo + (i >> 1) * rhalf + (i & 1) * 1024, 0, 16);
        }
        SCHED_FENCE();
      }
      MEM_FENCE();
      if (T < 7) {
        if (__builtin_expect(__any(!(l0 + l1 <= SM_LIMIT)), 0)) fixup(N0, l0p, l1p, Pn0, Pn1, false, Pc0, Pc1);
      }
    }
  };
  STAMP(5);
  tile(IC<0>{}, Sa1, Sb0, Sb1, Pa0, Pa1, Pb0, Pb1);
  tile(IC<1>{}, Sb1, Sa0, Sa1, Pb0, Pb1, Pa0, Pa1);
  tile(IC<2>{}, Sa1, Sb0, Sb1, Pa0, Pa1, Pb0, Pb1);
  tile(IC<3>{}, Sb1, Sa0, Sa1, Pb0, Pb1, Pa0, Pa1);
  tile(IC<4>{}, Sa1, Sb0, Sb1, Pa0, Pa1, Pb0, Pb1);
  tile(IC<5>{}, Sb1, Sa0, Sa1, Pb0, Pb1, Pa0, Pa1);
  STAMP(6);
  tile(IC<6>{}, Sa1, Sb0, Sb1, Pa0, Pa1, Pb0, Pb1);
  tile(IC<7>{}, Sb1, Sa0, Sa1, Pb0, Pb1, Pa0, Pa1);
  STAMP(7);

  {
    const float inv = 1.f / swap32_add(l0 + l1);
    char* dst = lds + L1_HT + ((wid & 1) * 32 + r32) * HT_STRIDE + ((wid >> 1) * 128 + 4 * hh) * 2;
#pragma unroll
    for (int db = 0; db < 4; ++db)
#pragma unroll
      for (int gq = 0; gq < 4; ++gq) {
        u32x2 w = {pk2(o[db][4 * gq] * inv, o[db][4 * gq + 1] * inv), pk2(o[db][4 * gq + 2] * inv, o[db][4 * gq + 3] * inv)};
        *(u32x2*)(dst + (32 * db + 8 * gq) * 2) = w;
      }
  }
  __syncthreads();
  STAMP(8);
  if (wid >= 4 && PRIO_O > 0) __builtin_amdgcn_s_setprio(1);
  {
    f32x4 acc[2][2][4];
    const rsrc_t rP = mk_rsrc(ws + WS_PART + (size_t)((rbk * 8 + g) * 8 + wid) * 8192, 8192);
    auto pstore = [&](int ps, int qt) AI {
      u32x4 w;
#pragma unroll
      for (int r = 0; r < 4; ++r) w[r] = pk2(acc[ps][0][qt][r], acc[ps][1][qt][r]);
      __builtin_amdgcn_raw_buffer_store_b128(w, rP, fo + (qt * 2 + ps) * 1024, 0, PART_AUX);
    };
    gemm_steps<2, 4, 32, 12, 2, 2>(acc, ring2, wo_load, bread, [&](auto S_, auto M_) AI {
      if constexpr (S_.value == PRIO_O && M_.value == 0) __builtin_amdgcn_s_setprio(0);
      if constexpr (S_.value >= 20 && S_.value < 22 && (M_.value & 3) == 1) pstore(0, (S_.value - 20) * 2 + (M_.value >> 2)); });
    STAMP(9);
#pragma unroll
    for (int qt = 0; qt < 4; ++qt) pstore(1, qt);
  }
  STAMP(10);

  const rsrc_t rWs = mk_rsrc(ws, (unsigned)WS_END);
  auto ld1 = [&](size_t off, unsigned soff) AI { return __builtin_amdgcn_raw_buffer_load_b128(rWs, (unsigned)off, soff, 16); };
  auto ldf1 = [&](size_t off, unsigned soff) AI { return __builtin_bit_cast(bf16x8, __builtin_amdgcn_raw_buffer_load_b128(rWs, (unsigned)off, soff, 16)); };
  auto st1 = [&](size_t off, u32x4 v) AI { __builtin_amdgcn_raw_buffer_store_b128(v, rWs, (unsigned)off, 0, 16); };
  auto publish = [&](int idx) AI {
    asm volatile("s_waitcnt vmcnt(0) lgkmcnt(0)" ::: "memory"); __builtin_amdgcn_s_barrier(); MEM_FENCE();
    if (tid == 0) __hip_atomic_fetch_add(flags + idx * 32, 1u, __ATOMIC_RELAXED, __HIP_MEMORY_SCOPE_AGENT);
  };
  auto await = [&](int pw, int idx, unsigned want) AI {
    if (wid == pw) {
      unsigned spins = 0;
      while (!__all(__hip_atomic_load(flags + idx * 32, __ATOMIC_RELAXED, __HIP_MEMORY_SCOPE_AGENT) >= want) && ++spins < SPIN_CAP) __builtin_amdgcn_s_sleep(1);
    }
    MEM_FENCE(); __builtin_amdgcn_s_barrier(); MEM_FENCE();
  };
  const int b = blockIdx.x;
  const u16* modb = (const u16*)(ws + WS_MOD);
  publish(FL_P + rbk);
  STAMP(11);
  RT(1, 1);

  RT(2, 0);
  STAMPK(2, 0);
  {
    f32x2* red = (f32x2*)(lds + 16 * HT_STRIDE);
    const int rt = (b & 7) * 16 + (b >> 4), nh = (b >> 3) & 1;
    const int prb = rt >> 2, qt = rt & 3, row = rt * 16 + a, col = wid * 64 + g4 * 16;
    u32x4 xv[2], gav[2], cbv[2], sbv[2];
#pragma unroll
    for (int h = 0; h < 2; ++h) {
      xv[h] = *(const u32x4*)(p.x + (size_t)row * D_ + col + 8 * h);
      gav[h] = *(const u32x4*)(modb + 1024 + col + 8 * h); sbv[h] = *(const u32x4*)(modb + 1536 + col + 8 * h); cbv[h] = *(const u32x4*)(modb + 2048 + col + 8 * h);
    }
    constexpr int D1 = 12;
    bf16x8 A[D1][2];
    const int G = nh * 4 + (wid >> 1), ps = wid & 1;
    const rsrc_t rW = mk_rsrc(ws + WS_W1T + (size_t)G * 65536 + ps * 2048, 65536);
    auto aload = [&](int s, int i) AI { return ldfrag(rW, fo, (s * 4 + i) * 1024); };
#pragma unroll
    for (int s = 0; s < D1; ++s) { A[s][0] = aload(s, 0); A[s][1] = aload(s, 1); }
    SCHED_FENCE();
    await(0, FL_P + prb, 8);
    STAMPK(2, 1);
    u32x4 pv[8][2];
#pragma unroll
    for (int gg = 0; gg < 8; ++gg)
#pragma unroll
      for (int h = 0; h < 2; ++h) pv[gg][h] = ld1(WS_PART + (size_t)((prb * 64 + wid) * 8 + qt * 2) * 1024 + fo, (gg * 64 + h) * 1024);
    SCHED_FENCE();
    float v[16];
#pragma unroll
    for (int d = 0; d < 8; ++d) { v[2 * d] = bflo(pv[0][d & 1][d >> 1]); v[2 * d + 1] = bfhi(pv[0][d & 1][d >> 1]); }
#pragma unroll
    for (int gg = 1; gg < 8; ++gg) {
#pragma unroll
      for (int d = 0; d < 8; ++d) { v[2 * d] += bflo(pv[gg][d & 1][d >> 1]); v[2 * d + 1] += bfhi(pv[gg][d & 1][d >> 1]); }
      SCHED_FENCE();
    }
#pragma unroll
    for (int d = 0; d < 8; ++d) {
      const unsigned ga = gav[d >> 2][d & 3], xx = xv[d >> 2][d & 3];
      const unsigned x2 = pk2(__builtin_fmaf(bflo(ga), v[2 * d], bflo(xx)), __builtin_fmaf(bfhi(ga), v[2 * d + 1], bfhi(xx)));
      xv[d >> 2][d & 3] = x2; v[2 * d] = bflo(x2); v[2 * d + 1] = bfhi(x2);
    }
    if (nh == 0) { st1(WS_X2 + (size_t)row * 1024 + col * 2, xv[0]); st1(WS_X2 + (size_t)row * 1024 + col * 2 + 16, xv[1]); }
    STAMPK(2, 2);
    auto row_stats = [&](f32x2* rd, float& mean, float& rstd) AI {
      float s0 = 0, s1 = 0, q0 = 0, q1 = 0;
#pragma unroll
      for (int e = 0; e < 8; ++e) { s0 += v[2 * e]; s1 += v[2 * e + 1]; q0 = __builtin_fmaf(v[2 * e], v[2 * e], q0); q1 = __builtin_fmaf(v[2 * e + 1], v[2 * e + 1], q1); }
      float s = swap32_add(swap16_add(s0 + s1)), q = swap32_add(swap16_add(q0 + q1));
      if (g4 == 0) rd[wid * 16 + a] = (f32x2){s, q};
      asm volatile("s_waitcnt lgkmcnt(0)" ::: "memory"); __builtin_amdgcn_s_barrier(); MEM_FENCE();
      f32x2 t = rd[a];
#pragma unroll
      for (int w = 1; w < 8; ++w) t += rd[w * 16 + a];
      mean = t[0] * (1.f / 512.f);
      rstd = __builtin_amdgcn_rsqf(fmaxf(t[1] * (1.f / 512.f) - mean * mean, 0.f) + 1e-5f);
    };
    float mean, rstd;
    row_stats(red, mean, rstd);
    u32x4 hv[2];
#pragma unroll
    for (int d = 0; d < 8; ++d) {
      const unsigned cb = cbv[d >> 2][d & 3], sb = sbv[d >> 2][d & 3];
      const unsigned hb = pk2((v[2 * d] - mean) * rstd * bflo(cb) + bflo(sb), (v[2 * d + 1] - mean) * rstd * bfhi(cb) + bfhi(sb));
      hv[d & 1][d >> 1] = hb; v[2 * d] = bflo(hb); v[2 * d + 1] = bfhi(hb);
    }
    if (nh == 1) { st1(WS_HB + (size_t)row * 1024 + wid * 128 + g4 * 16, hv[0]); st1(WS_HB + (size_t)row * 1024 + wid * 128 + 64 + g4 * 16, hv[1]); }
    row_stats(red + 128, mean, rstd);
    const float nm = -mean * rstd;
#pragma unroll
    for (int h = 0; h < 2; ++h) {
      u32x4 o;
#pragma unroll
      for (int e = 0; e < 4; ++e) o[e] = pk2(__builtin_fmaf(v[8 * h + 2 * e], rstd, nm), __builtin_fmaf(v[8 * h + 2 * e + 1], rstd, nm));
      *(u32x4*)(lds + a * HT_STRIDE + (col + 8 * h) * 2) = o;
    }
    STAMPK(2, 3);
    asm volatile("s_waitcnt lgkmcnt(0)" ::: "memory"); __builtin_amdgcn_s_barrier(); MEM_FENCE();
    STAMPK(2, 4);
    f32x4 acc[1][2][1];
    gemm_steps<2, 1, 16, D1, 1, 4>(acc, A, aload, [&](int s, int) AI { return *(const bf16x8*)(bl + 64 * s); }, [](auto, auto) AI {});
    STAMPK(2, 5);
    u32x4 ov;
#pragma unroll
    for (int rg = 0; rg < 4; ++rg) ov[rg] = pk2(gelu_tanh(acc[0][0][0][rg]), gelu_tanh(acc[0][1][0][rg]));
    st1(WS_H1 + (size_t)(rt * 16 + 2 * G + ps) * 1024 + fo, ov);
    STAMPK(2, 6);
    publish(FL_H1 + rt);
    RT(2, 1);
    STAMPK(2, 7);
  }

  const int rb2 = (b & 7) * 4 + (b >> 6), ns = (b >> 3) & 7;
  const int ng = ns * 7 + (wid < 7 ? wid : 6);
  const size_t w3off = WS_W3T + (size_t)(ng >> 1) * 65536 + (ng & 1) * 2048 + fo;
  bf16x8 A[16][2];
  RT(3, 0);
  STAMPK(3, 0);
  {
    const int rt2 = (b & 7) * 8 + (b >> 5), nq = (b >> 3) & 3;
    const int G = nq * 2 + ((wid & 3) >> 1), ps = wid & 1;
    const size_t aoff = (wid < 4) ? WS_W2T + (size_t)G * 65536 + ps * 2048 + fo : w3off;
#pragma unroll
    for (int s = 0; s < 16; ++s) { A[s][0] = ldf1(aoff, (s * 4) * 1024); A[s][1] = ldf1(aoff, (s * 4 + 1) * 1024); }
    SCHED_FENCE();
    { const int t = 2 * rt2 - 1 + (lane & 3); await(7, FL_H1 + (t < 0 ? 0 : t > 127 ? 127 : t), 2); }
    STAMPK(3, 1);
    const rsrc_t rH = mk_rsrc(ws + WS_H1, (unsigned)NXB);
    const int sh = (wid < 2) ? -1 : (wid < 4) ? 1 : (wid < 6) ? -2 : 2;
    u32x4 hf[2][2], hbv[2];
#pragma unroll
    for (int qt = 0; qt < 2; ++qt) {
      const int r = rt2 * 32 + qt * 16 + a + sh;
      const unsigned ho = (unsigned)((r >> 4) * 16384 + ((r & 15) + 16 * g4) * 16);
      hf[qt][0] = __builtin_amdgcn_raw_buffer_load_b128(rH, ho, (wid * 2) * 1024, 16); hf[qt][1] = __builtin_amdgcn_raw_buffer_load_b128(rH, ho, (wid * 2 + 1) * 1024, 16);
    }
    const size_t xoff = (size_t)(rt2 * 32 + a) * 1024 + G * 128 + ps * 64 + g4 * 16;
    if (wid < 4) { hbv[0] = ld1(WS_HB + xoff, 0); hbv[1] = ld1(WS_HB + xoff, 16384); }
#pragma unroll
    for (int qt = 0; qt < 2; ++qt) { *(u32x4*)(lds + (qt * 16 + wid * 2) * 1024 + fo) = hf[qt][0]; *(u32x4*)(lds + (qt * 16 + wid * 2 + 1) * 1024 + fo) = hf[qt][1]; }
    asm volatile("s_waitcnt lgkmcnt(0)" ::: "memory"); __builtin_amdgcn_s_barrier(); MEM_FENCE();
    STAMPK(3, 2);
    f32x2* red = (f32x2*)(lds + 32768);
    if (wid < 4) {
      f32x4 acc[1][2][2];
      gemm_steps<2, 2, 16, 16, 1, 3>(acc, A, [&](int, int) AI { return bf16x8{}; }, [&](int s, int qt) AI { return *(const bf16x8*)(lds + (qt * 16 + s) * 1024 + fo); }, [](auto, auto) AI {});
#pragma unroll
      for (int qt = 0; qt < 2; ++qt) {
        u32x4 ov; float s = 0, q = 0;
#pragma unroll
        for (int rg = 0; rg < 4; ++rg) {
          ov[rg] = pk2(bflo(hbv[qt][rg]) + acc[0][0][qt][rg], bfhi(hbv[qt][rg]) + acc[0][1][qt][rg]);
          const float lo = bflo(ov[rg]), hi = bfhi(ov[rg]); s += lo + hi; q += lo * lo + hi * hi;
        }
        st1(WS_X1 + xoff + qt * 16384, ov);
        s = swap32_add(swap16_add(s)); q = swap32_add(swap16_add(q));
        if (g4 == 0) red[wid * 32 + qt * 16 + a] = (f32x2){s, q};
      }
    }
    asm volatile("s_waitcnt lgkmcnt(0)" ::: "memory"); __builtin_amdgcn_s_barrier(); MEM_FENCE();
    if (tid < 32) {
      const f32x2 t = red[tid] + red[32 + tid] + red[64 + tid] + red[96 + tid];
      __hip_atomic_store((gu64*)(ws + WS_ST + (size_t)(rt2 * 32 + tid) * 32 + nq * 8), __builtin_bit_cast(unsigned long long, t), __ATOMIC_RELAXED, __HIP_MEMORY_SCOPE_AGENT);
    }
    STAMPK(3, 3);
    publish(FL_X1 + (rt2 >> 1));
    RT(3, 1);
    STAMPK(3, 4);
  }

  RT(4, 0);
  STAMPK(4, 0);
  {
    if (wid < 4) {
#pragma unroll
      for (int s = 0; s < 16; ++s) { A[s][0] = ldf1(w3off, (s * 4) * 1024); A[s][1] = ldf1(w3off, (s * 4 + 1) * 1024); }
    }
    SCHED_FENCE();
    await(7, FL_X1 + rb2, 8);
    STAMPK(4, 1);
    u32x4 xv[8];
    const u32x4 stv = ld1(WS_ST + (size_t)(rb2 * 64 + wid * 8) * 32 + (lane & 15) * 16, 0);
#pragma unroll
    for (int i = 0; i < 8; ++i) xv[i] = ld1(WS_X1 + (size_t)(rb2 * 64 + wid * 8) * 1024 + fo, i * 1024);
    SCHED_FENCE();
    const float sm = dpp_add<0xB1>(__uint_as_float(stv[0]) + __uint_as_float(stv[2])) * (1.f / 512.f);
    const float sq = dpp_add<0xB1>(__uint_as_float(stv[1]) + __uint_as_float(stv[3])) * (1.f / 512.f);
    const float rsl = __builtin_amdgcn_rsqf(fmaxf(sq - sm * sm, 0.f) + 1e-5f), nml = -sm * rsl;
    float rs[8], nm[8];
#pragma unroll
    for (int i = 0; i < 8; ++i) { rs[i] = __uint_as_float(__builtin_amdgcn_readlane(__float_as_uint(rsl), 2 * i)); nm[i] = __uint_as_float(__builtin_amdgcn_readlane(__float_as_uint(nml), 2 * i)); }
    SCHED_FENCE();
#pragma unroll
    for (int hf = 0; hf < 2; ++hf) {
      float lo[16], hi[16]; unsigned t[16];
      H_STAGE(lo[j] = bflo(xv[i][d]); hi[j] = bfhi(xv[i][d]));
      H_STAGE(lo[j] = __builtin_fmaf(lo[j], rs[i], nm[i]); hi[j] = __builtin_fmaf(hi[j], rs[i], nm[i]));
      H_STAGE(t[j] = pk2(lo[j], hi[j]));
#pragma unroll
      for (int i = 0; i < 4; ++i) *(u32x4*)(lds + (wid * 8 + hf * 4 + i) * HT_STRIDE + fo) = (u32x4){t[4 * i], t[4 * i + 1], t[4 * i + 2], t[4 * i + 3]};
      SCHED_FENCE();
    }
    STAMPK(4, 2);
    asm volatile("s_waitcnt lgkmcnt(0)" ::: "memory"); __builtin_amdgcn_s_barrier(); MEM_FENCE();
    STAMPK(4, 3);
    if (wid < 7) {
      f32x4 acc[2][2][2];
      const size_t dst = WS_U + (size_t)(rb2 * 4 * 56 + ng) * 1024 + fo;
      u32x4 ow;
      auto gpart = [&](int ps, int k) AI {
        unsigned w = pk2(gelu_tanh(acc[ps][0][k >> 2][k & 3]), gelu_tanh(acc[ps][1][k >> 2][k & 3])); PIN(w); ow[k & 3] = w;
        if ((k & 3) == 3) st1(dst + (size_t)(2 * ps + (k >> 2)) * 56 * 1024, ow);
      };
      gemm_steps<2, 2, 32, 16, 2, 3>(acc, A, [&](int s, int i) AI { return A[s & 15][i]; },
                                     [&](int s, int qt) AI { return *(const bf16x8*)(bl + ((s >> 4) * 2 + qt) * 16 * HT_STRIDE + 64 * (s & 15)); },
                                     [&](auto S_, auto M_) AI { if constexpr (S_.value >= 17 && (S_.value & 1) && M_.value == 1) gpart(0, (S_.value - 17) >> 1); });
      STAMPK(4, 4);
#pragma unroll
      for (int k = 0; k < 8; ++k) gpart(1, k);
    }
    STAMPK(4, 5);
    publish(FL_U + rb2);
    RT(4, 1);
    STAMPK(4, 6);
  }

  RT(5, 0);
  STAMPK(5, 0);
  {
    bf16x8 W[7][4], B[7][4];
    const size_t woff = WS_W4T + (size_t)(ns * 56 + wid * 7) * 4096 + fo, uoff = WS_U + (size_t)(rb2 * 4 * 56 + wid * 7) * 1024 + fo;
#pragma unroll
    for (int s = 0; s < 7; ++s)
#pragma unroll
      for (int i = 0; i < 4; ++i) W[s][i] = ldf1(woff, (s * 4 + i) * 1024);
    const int oqt = wid >> 1, orp = wid & 1, orow = rb2 * 64 + oqt * 16 + a, ocol = ns * 64 + g4 * 16 + orp * 8;
    const size_t x1o = WS_X1 + (size_t)orow * 1024 + ns * 128 + g4 * 16 + orp * 8;
    const u32x2 x1a = __builtin_amdgcn_raw_buffer_load_b64(rWs, (unsigned)x1o, 0, 16), x1b = __builtin_amdgcn_raw_buffer_load_b64(rWs, (unsigned)x1o, 64, 16);
    const u32x4 x2v = ld1(WS_X2 + (size_t)orow * 1024 + ocol * 2, 0), gbv = *(const u32x4*)(modb + 2560 + ocol);
    SCHED_FENCE();
    await(0, FL_U + rb2, 8);
#pragma unroll
    for (int s = 0; s < 4; ++s)
#pragma unroll
      for (int i = 0; i < 4; ++i) B[s][i] = ldf1(uoff, (i * 56 + s) * 1024);
    SCHED_FENCE();
    STAMPK(5, 1);
    f32x4 acc[4][4] = {};
#pragma unroll
    for (int s = 0; s < 7; ++s) {
#pragma unroll
      for (int i = 0; i < 4; ++i) {
#pragma unroll
        for (int qt = 0; qt < 4; ++qt) acc[i][qt] = MFMA16(W[s][i], B[s][qt], acc[i][qt]);
        if (s + 4 < 7) B[s + 4][i] = ldf1(uoff, (i * 56 + s + 4) * 1024);
        SCHED_FENCE();
      }
    }
    STAMPK(5, 2);
#pragma unroll
    for (int qt = 0; qt < 4; ++qt)
#pragma unroll
      for (int rg = 0; rg < 4; ++rg)
        *(f32x4*)(lds + ((wid * 4 + qt) * 4 + rg) * 1024 + fo) = (f32x4){acc[0][qt][rg], acc[1][qt][rg], acc[2][qt][rg], acc[3][qt][rg]};
    STAMPK(5, 3);
    __syncthreads();
    STAMPK(5, 4);
    f32x4 s0 = {}, s1 = {};
#pragma unroll
    for (int w = 0; w < 8; ++w) {
      s0 += *(const f32x4*)(lds + ((w * 4 + oqt) * 4 + 2 * orp) * 1024 + fo);
      s1 += *(const f32x4*)(lds + ((w * 4 + oqt) * 4 + 2 * orp + 1) * 1024 + fo);
    }
    const float h2[8] = {s0[0], s0[1], s0[2], s0[3], s1[0], s1[1], s1[2], s1[3]};
    const unsigned x1v[4] = {x1a[0], x1b[0], x1a[1], x1b[1]};
    u32x4 ov;
#pragma unroll
    for (int e = 0; e < 4; ++e)
      ov[e] = pk2(bflo(x2v[e]) + bflo(gbv[e]) * (bflo(x1v[e]) + h2[2 * e]), bfhi(x2v[e]) + bfhi(gbv[e]) * (bfhi(x1v[e]) + h2[2 * e + 1]));
    *(u32x4*)(p.out + (size_t)orow * D_ + ocol) = ov;
    STAMPK(5, 5);
    RT(5, 1);
    STAMPK(5, 6);
  }
}

extern "C" void kernel_launch(void* const* d_in, const int* in_sizes, int n_in, void* d_out, int out_size, void* d_ws, size_t ws_size, hipStream_t stream) {
  char* ws = (char*)d_ws;
  K0Args p0;
  p0.x = (const u16*)d_in[0]; p0.c = (const u16*)d_in[10]; p0.wmod = (const u16*)d_in[11]; p0.wq = (const u16*)d_in[3]; p0.w1 = (const u16*)d_in[6]; p0.ws = ws;
  K1Args p1;
  p1.x = p0.x; p1.kc = (const u16*)d_in[1]; p1.vc = (const u16*)d_in[2]; p1.wo = (const u16*)d_in[4];
  p1.w2 = (const u16*)d_in[7]; p1.w3 = (const u16*)d_in[8]; p1.w4 = (const u16*)d_in[9]; p1.lnst = (const float*)(ws + WS_Y); p1.ws = ws; p1.out = (u16*)d_out;
  (void)hipFuncSetAttribute((const void*)k1_attn, hipFuncAttributeMaxDynamicSharedMemorySize, L1_END);
  k0_prep<<<K0_BLOCKS, 512, 0, stream>>>(p0);
  k1_attn<<<256, 512, L1_END, stream>>>(p1);
}
```

```cpp
#include <hip/hip_runtime.h>
#include <hip/hip_bf16.h>
#include <type_traits>
#include <utility>

typedef unsigned short u16;
typedef __bf16 bf16x8 __attribute__((ext_vector_type(8)));
typedef __bf16 bf16x4 __attribute__((ext_vector_type(4)));
typedef __bf16 bf16x2 __attribute__((ext_vector_type(2)));
typedef float f32x16 __attribute__((ext_vector_type(16)));
typedef float f32x4 __attribute__((ext_vector_type(4)));
typedef float f32x2 __attribute__((ext_vector_type(2)));
typedef unsigned u32x2 __attribute__((ext_vector_type(2)));
typedef unsigned u32x4 __attribute__((ext_vector_type(4)));
typedef __amdgpu_buffer_rsrc_t rsrc_t;
#define DI __device__ __forceinline__
#define AI __attribute__((always_inline))
#define LDSP(T, p) ((__attribute__((address_space(3))) T*)(p))
#define SCHED_FENCE() __builtin_amdgcn_sched_barrier(0)
#define PIN(x) asm volatile("" : "+v"(x))
#define MEM_FENCE() asm volatile("" ::: "memory")
#define WAIT_VM0() __builtin_amdgcn_s_waitcnt(0x0F70)
#define STAMP(i)
#define STAMPK(k, i)
#define RT(k, i)

#define K0_WARM 1
constexpr int S_ = 2048, D_ = 512, HQ = 32, HKV = 8, DH = 128, SK = 2048, WIN = 512, F_ = 1792;

constexpr size_t NXB = (size_t)S_ * D_ * 2;
constexpr size_t WS_MOD = 0;
constexpr size_t WS_Y   = 8192;
constexpr size_t WS_X2  = WS_Y  + NXB;
constexpr size_t WS_HB  = WS_X2 + NXB;
constexpr size_t WS_H1  = WS_HB + NXB;
constexpr size_t WS_X1  = WS_H1 + NXB;
constexpr size_t WS_U   = WS_X1 + NXB;
constexpr size_t WS_PART = WS_U + (size_t)S_ * F_ * 2;
constexpr size_t WS_WQT = WS_PART + 8 * NXB;
constexpr size_t WS_WOT = WS_WQT + (size_t)D_ * 4096 * 2;
constexpr size_t WS_W1T = WS_WOT + (size_t)D_ * 4096 * 2;
constexpr size_t WS_W2T = WS_W1T + (size_t)D_ * D_ * 2;
constexpr size_t WS_W3T = WS_W2T + (size_t)D_ * D_ * 2;
constexpr size_t WS_W4T = WS_W3T + (size_t)D_ * F_ * 2;
constexpr size_t WS_ST  = WS_W4T + (size_t)D_ * F_ * 2;
constexpr size_t WS_FLAG = WS_ST + (size_t)S_ * 32;
constexpr int FL_W = 0, FL_P = 8, FL_H1 = 40, FL_X1 = 168, FL_U = 200, FL_P0 = 232, FL_N = 264;
constexpr size_t WS_END = WS_FLAG + FL_N * 128;

DI float bflo(unsigned u) { return __uint_as_float(u << 16); }
DI float bfhi(unsigned u) { return __uint_as_float(u & 0xffff0000u); }
DI unsigned pk2(float lo, float hi) { f32x2 v = {lo, hi}; return __builtin_bit_cast(unsigned, __builtin_convertvector(v, bf16x2)); }
DI float rb(float x) { return __uint_as_float(pk2(0.f, x)); }
DI rsrc_t mk_rsrc(const void* p, unsigned bytes) { return __builtin_amdgcn_make_buffer_rsrc((void*)p, (short)0, (int)bytes, 0x00020000); }
DI unsigned rfl(unsigned x) { return __builtin_amdgcn_readfirstlane(x); }
DI bf16x8 ldfrag(rsrc_t r, unsigned voff, unsigned soff) { return __builtin_bit_cast(bf16x8, __builtin_amdgcn_raw_buffer_load_b128(r, voff, soff, 0)); }
template <int CTRL> DI float dpp_add(float v) {
  return v + __uint_as_float(__builtin_amdgcn_update_dpp(0u, __float_as_uint(v), CTRL, 0xf, 0xf, true));
}
DI float swap16_add(float v) { auto r = __builtin_amdgcn_permlane16_swap(__float_as_uint(v), __float_as_uint(v), false, false); return __uint_as_float(r[0]) + __uint_as_float(r[1]); }
DI float swap32_add(float v) { auto r = __builtin_amdgcn_permlane32_swap(__float_as_uint(v), __float_as_uint(v), false, false); return __uint_as_float(r[0]) + __uint_as_float(r[1]); }
DI float swap32_max(float v) { auto r = __builtin_amdgcn_permlane32_swap(__float_as_uint(v), __float_as_uint(v), false, false); return fmaxf(__uint_as_float(r[0]), __uint_as_float(r[1])); }
DI float wave_sum(float v) {
  v = dpp_add<0xB1>(v); v = dpp_add<0x4E>(v); v = dpp_add<0x141>(v); v = dpp_add<0x140>(v);
  v = swap16_add(v); v = swap32_add(v); return v;
}
DI float div_rn(float a, float d, float r) {
  float q = a * r; float e = __builtin_fmaf(-q, d, a); return __builtin_fmaf(e, r, q);
}
DI float gelu_tanh(float x) {
  const float u = 0.7978845608028654f * (x + 0.044715f * x * x * x);
  return x * __builtin_amdgcn_rcpf(1.f + __builtin_amdgcn_exp2f(-2.885390081777927f * u));
}
DI void ln_consts(float mean, float var, float& m_bf, float& d_bf, float& r) {
  m_bf = rb(mean); d_bf = rb(__builtin_sqrtf(rb(rb(var) + 1e-5f))); r = __builtin_amdgcn_rcpf(d_bf);
}
DI float ln_apply(float x, float m_bf, float d_bf, float r) { return rb(div_rn(rb(x - m_bf), d_bf, r)); }
DI u32x4 ln_row(const u32x4 xv) {
  float f[8];
#pragma unroll
  for (int e = 0; e < 4; ++e) { f[2 * e] = bflo(xv[e]); f[2 * e + 1] = bfhi(xv[e]); }
  float s = 0;
#pragma unroll
  for (int e = 0; e < 8; ++e) s += f[e];
  const float mean = wave_sum(s) * (1.f / 512.f);
  float q = 0;
#pragma unroll
  for (int e = 0; e < 8; ++e) { float d = f[e] - mean; q += d * d; }
  const float var = wave_sum(q) * (1.f / 512.f);
  float mb, db, r; ln_consts(mean, var, mb, db, r);
  u32x4 o;
#pragma unroll
  for (int e = 0; e < 4; ++e) o[e] = pk2(div_rn(rb(f[2 * e] - mb), db, r), div_rn(rb(f[2 * e + 1] - mb), db, r));
  return o;
}

#define MFMA16(a, b, c) __builtin_amdgcn_mfma_f32_16x16x32_bf16((a), (b), (c), 0, 0, 0)
#define MFMA32(a, b, c) __builtin_amdgcn_mfma_f32_32x32x16_bf16((a), (b), (c), 0, 0, 0)

DI void dma16(unsigned lds_dst, unsigned voff, rsrc_t r, unsigned soff) {
  asm volatile("s_mov_b32 m0, %0\n\ts_nop 0\n\tbuffer_load_dwordx4 %1, %2, %3 offen lds" ::"s"(lds_dst), "v"(voff), "s"(r), "s"(soff) : "memory");
}

struct Retile { u32x2 d[8]; };
DI void retile_load(Retile& t, const u16* __restrict__ W, int ldw, int k0, bool perm, int n0, int lane) {
  const int a = lane & 15, g4 = lane >> 4;
  const u16* p = W + (size_t)(k0 + (perm ? 16 : 8) * g4) * ldw + n0 + 4 * a;
  const int s2 = (perm ? 4 : 2) * ldw;
#pragma unroll
  for (int j = 0; j < 8; ++j) t.d[j] = *(const u32x2*)(p + (j >> 1) * s2 + (j & 1) * ldw);
}
DI u32x4 retile_frag(const Retile& t, int i) {
  u32x4 f;
#pragma unroll
  for (int p = 0; p < 4; ++p) f[p] = __builtin_amdgcn_perm(t.d[2 * p + 1][i >> 1], t.d[2 * p][i >> 1], (i & 1) ? 0x07060302u : 0x05040100u);
  return f;
}

struct K0Args { const u16 *x, *c, *wmod, *wq, *w1, *kc, *vc; char* ws; };
constexpr int K0_BLOCKS = 304 + 128 + 16;
__global__ __launch_bounds__(512) void k0_prep(const K0Args p) {
  __shared__ float red[8][64];
  const int tid = threadIdx.x, lane = tid & 63, wid = tid >> 6;
  int b = blockIdx.x;
#define WSP p.ws
  RT(0, 0);
  if (b < 48) {
    const int n0 = b * 64, cg = tid & 7, kq = tid >> 3;
    u32x4 wv[8]; float sv[8];
#pragma unroll
    for (int i = 0; i < 8; ++i) wv[i] = *(const u32x4*)(p.wmod + (size_t)(kq + 64 * i) * 3072 + n0 + cg * 8);
#pragma unroll
    for (int i = 0; i < 8; ++i) { float cv = bflo(p.c[kq + 64 * i]); sv[i] = rb(cv / (1.f + __expf(-cv))); }
    float acc[8] = {};
#pragma unroll
    for (int i = 0; i < 8; ++i)
#pragma unroll
      for (int e = 0; e < 4; ++e) { acc[2 * e] += sv[i] * bflo(wv[i][e]); acc[2 * e + 1] += sv[i] * bfhi(wv[i][e]); }
#pragma unroll
    for (int e = 0; e < 8; ++e) { float v = acc[e]; v += __shfl_xor(v, 8); v = swap16_add(v); v = swap32_add(v); acc[e] = v; }
    if (lane < 8)
#pragma unroll
      for (int e = 0; e < 8; ++e) red[wid][lane * 8 + e] = acc[e];
    __syncthreads();
    if (tid < 64) {
      float s = 0;
#pragma unroll
      for (int w = 0; w < 8; ++w) s += red[w][tid];
      s = rb(s);
      const int n = n0 + tid, seg = n >> 9;
      if (seg == 1 || seg == 4) s = rb(1.f + s);
      ((u16*)(p.ws + WS_MOD))[n] = (u16)(__float_as_uint(s) >> 16);
    }
    RT(0, 1);
    return;
  }
  b -= 48;
  if (b < 256) {
    const int row = b * 8 + wid;
    const u32x4 xv = *(const u32x4*)(p.x + (size_t)row * D_ + lane * 8);
    u32x4 wv[8];
#pragma unroll
    for (int i = 0; i < 8; ++i) wv[i] = *(const u32x4*)(p.x + (size_t)((b >> 3) * 64 + wid * 8 + i) * D_ + lane * 8);
    const u32x4 kvw = *(const u32x4*)((wid < 4 ? p.kc : p.vc) + ((size_t)(b & 7) * SK + (SK - WIN)) * DH + ((b >> 3) * 4 + (wid & 3)) * 512 + lane * 8);
    float f[8];
#pragma unroll
    for (int e = 0; e < 4; ++e) { f[2 * e] = bflo(xv[e]); f[2 * e + 1] = bfhi(xv[e]); }
    float s = 0;
#pragma unroll
    for (int e = 0; e < 8; ++e) s += f[e];
    const float mean = wave_sum(s) * (1.f / 512.f);
    float q = 0;
#pragma unroll
    for (int e = 0; e < 8; ++e) { float d = f[e] - mean; q += d * d; }
    const float var = wave_sum(q) * (1.f / 512.f);
    float mb, db, r; ln_consts(mean, var, mb, db, r);
    asm volatile("" ::"v"(kvw));
    if (lane == 0) *(f32x2*)(p.ws + WS_Y + (size_t)row * 8) = (f32x2){mb, db};
#pragma unroll
    for (int i = 0; i < 8; ++i) asm volatile("" ::"v"(wv[i]));
    RT(0, 1);
    return;
  }
  b -= 256;
  Retile t; char* dst;
  if (b < 128) { const int g = b & 7, s = b >> 3, ng = g * 8 + wid; retile_load(t, p.wq, 4096, 32 * s, false, 64 * ng, lane); dst = p.ws + WS_WQT + (size_t)(ng * 16 + s) * 4096; }
  else {
    const int s = b - 128; retile_load(t, p.w1, 512, 32 * s, false, 64 * wid, lane); dst = p.ws + WS_W1T + (size_t)(wid * 16 + s) * 4096;
    if (s == 0 && tid < FL_N) *(unsigned*)(p.ws + WS_FLAG + tid * 128) = 0u;
  }
#pragma unroll
  for (int i = 0; i < 4; ++i) *(u32x4*)(dst + i * 1024 + lane * 16) = retile_frag(t, i);
  RT(0, 1);
#undef WSP
#define WSP ws
}

template <int V> using IC = std::integral_constant<int, V>;
template <int N, class F, int... Is> DI void sfor_(F&& f, std::integer_sequence<int, Is...>) { (f(IC<Is>{}), ...); }
template <int N, class F> DI void sfor(F&& f) { sfor_<N>(f, std::make_integer_sequence<int, N>{}); }
template <int NI, int NQT, int NSTEP, int DEPTH, int NSET, int BD = 1, class ALoad, class BRead, class Hook>
DI void gemm_steps(f32x4 (&acc)[NSET][NI][NQT], bf16x8 (&ring)[DEPTH][NI], ALoad aload, BRead bread, Hook hook) {
  constexpr int NM = NI * NQT, PER = NSTEP / NSET;
  bf16x8 B[BD + 1][NQT];
#pragma unroll
  for (int s = 0; s < BD; ++s)
#pragma unroll
    for (int qt = 0; qt < NQT; ++qt) B[s][qt] = bread(s, qt);
  SCHED_FENCE();
  sfor<NSTEP * NM>([&](auto X) AI {
    constexpr int s = X.value / NM, m = X.value % NM, i = m / NQT, qt = m % NQT;
    if constexpr (s % PER == 0) acc[s / PER][i][qt] = MFMA16(ring[s % DEPTH][i], B[s % (BD + 1)][qt], (f32x4){});
    else                        acc[s / PER][i][qt] = MFMA16(ring[s % DEPTH][i], B[s % (BD + 1)][qt], acc[s / PER][i][qt]);
    if constexpr (m < NQT && s + BD < NSTEP) B[(s + BD) % (BD + 1)][m] = bread(s + BD, m);
    if constexpr (qt == NQT - 1 && s + DEPTH < NSTEP) ring[s % DEPTH][i] = aload(s + DEPTH, i);
    hook(IC<s>{}, IC<m>{});
    SCHED_FENCE();
  });
}

constexpr int HT_STRIDE = 1056, QX_STRIDE = 272;
constexpr int L1_HT = 0, L1_K = 69632, L1_V = L1_K + 3 * 16384, L1_END = L1_V + 2 * 16384;
#define PRIO_G3 21
#define G4_AHEAD 2
#define ROT 1
#define P0_STEP 28
#define PRIO_P 1
#define PRIO_Q 11
#define PRIO_O 21
#define PRIO_A 0
#define GBAR 1
#define WO_SC1 1
#define OPASS 2
#define PF_ON 1
#define RT_AUX 16
#define PART_AUX 16
constexpr float SM_C = 0.12751743082459868f;
constexpr float SM_LIMIT = 1099511627776.f;

DI float max16(const f32x16& S) {
  float m = fmaxf(S[0], S[1]);
#pragma unroll
  for (int r = 2; r < 16; ++r) m = fmaxf(m, S[r]);
  return m;
}
DI unsigned scale_pk(unsigned p, float f) { return pk2(bflo(p) * f, bfhi(p) * f); }
DI void sm_half(const f32x16& S, float negmc, float& l0, float& l1, u32x4& pa, u32x4& pb) {
#pragma unroll
  for (int p = 0; p < 4; ++p) {
    const float e0 = __builtin_amdgcn_exp2f(__builtin_fmaf(S[2 * p], SM_C, negmc)), e1 = __builtin_amdgcn_exp2f(__builtin_fmaf(S[2 * p + 1], SM_C, negmc));
    l0 += e0; l1 += e1; pa[p] = pk2(e0, e1);
  }
#pragma unroll
  for (int p = 0; p < 4; ++p) {
    const float e0 = __builtin_amdgcn_exp2f(__builtin_fmaf(S[8 + 2 * p], SM_C, negmc)), e1 = __builtin_amdgcn_exp2f(__builtin_fmaf(S[8 + 2 * p + 1], SM_C, negmc));
    l0 += e0; l1 += e1; pb[p] = pk2(e0, e1);
  }
}
struct SmPipe { float x[16], e[16]; };
DI void sm_gap(const int m, SmPipe& sp, const f32x16& S, float negmc, float& l0, float& l1, u32x4& pa, u32x4& pb) {
#pragma unroll
  for (int e = 0; e < 16; ++e) {
    const int f = e * 13 / 16;
    if (f + 2 == m) {
      if (e & 1) {
        l1 += sp.e[e]; PIN(l1);
        unsigned w = pk2(sp.e[e - 1], sp.e[e]); PIN(w);
        if (e < 8) pa[e >> 1] = w; else pb[(e - 8) >> 1] = w;
      } else { l0 += sp.e[e]; PIN(l0); }
    }
  }
#pragma unroll
  for (int e = 0; e < 16; ++e) if (e * 13 / 16 + 1 == m) sp.e[e] = __builtin_amdgcn_exp2f(sp.x[e]);
#pragma unroll
  for (int e = 0; e < 16; ++e) if (e * 13 / 16 == m) sp.x[e] = __builtin_fmaf(S[e], SM_C, negmc);
}
DI void pack_acc16(const f32x4 (&acc)[4][4], int qt, u32x4& w0, u32x4& w1) {
#pragma unroll
  for (int rg = 0; rg < 2; ++rg) {
    w0[2 * rg] = pk2(acc[0][qt][rg], acc[1][qt][rg]); w0[2 * rg + 1] = pk2(acc[2][qt][rg], acc[3][qt][rg]);
    w1[2 * rg] = pk2(acc[0][qt][2 + rg], acc[1][qt][2 + rg]); w1[2 * rg + 1] = pk2(acc[2][qt][2 + rg], acc[3][qt][2 + rg]);
  }
}

typedef __attribute__((address_space(1))) unsigned gu32;
typedef __attribute__((address_space(1))) unsigned long long gu64;
constexpr unsigned SPIN_CAP = 1u << 20;
struct K1Args { const u16 *x, *kc, *vc, *wo, *w2, *w3, *w4; const float* lnst; char* ws; u16* out; };

__global__ __launch_bounds__(512, 2) __attribute__((amdgpu_waves_per_eu(2, 2)))
void k1_attn(const K1Args p) {
  extern __shared__ __attribute__((aligned(16))) char lds[];
  char* __restrict__ ws = p.ws;
  const int tid = threadIdx.x, lane = tid & 63;
  const int wid = (int)rfl((unsigned)tid >> 6);
  const int g = blockIdx.x & 7, rbk = blockIdx.x >> 3;
  const int a = lane & 15, g4 = lane >> 4, r32 = lane & 31, hh = lane >> 5;
  const unsigned ldsb = (unsigned)(size_t)lds;
  const unsigned fo = lane * 16;
  RT(1, 0);
  STAMP(0);
  const rsrc_t rK = mk_rsrc(p.kc + ((size_t)g * SK + (SK - WIN)) * DH, WIN * DH * 2), rV = mk_rsrc(p.vc + ((size_t)g * SK + (SK - WIN)) * DH, WIN * DH * 2);
  const unsigned drow = wid * 8 + ((lane >> 2) & 7);
  const unsigned kvoff = drow * 256 + (lane >> 5) * 64 + (((lane & 3) ^ ((drow >> 2) & 3)) * 16);
  const unsigned vvoff = drow * 256 + (lane >> 5) * 64 + (lane & 3) * 16;
  const unsigned kdst = ldsb + L1_K + wid * 2048, vdst = ldsb + L1_V + wid * 2048;
  auto dmaK = [&](int t) AI { dma16(kdst + (t % 3) * 16384, kvoff, rK, t * 16384); dma16(kdst + (t % 3) * 16384 + 1024, kvoff, rK, t * 16384 + 128); };
  auto dmaV = [&](int t) AI { dma16(vdst + (t & 1) * 16384, vvoff, rV, t * 16384); dma16(vdst + (t & 1) * 16384 + 1024, vvoff, rV, t * 16384 + 128); };
  constexpr int DEPTH = 6;
  bf16x8 ring[DEPTH][4];
  const rsrc_t rWq = mk_rsrc(ws + WS_WQT + (size_t)(g * 8 + wid) * 65536, 65536);
  const int rot = ROT ? (rbk & 15) : 0;
  auto wq_load = [&](int s, int i) AI { return ldfrag(rWq, fo + i * 1024, ((s + rot) & 15) * 4096); };
  {
    const u32x4 sav = *(const u32x4*)(ws + WS_MOD + fo), cav = *(const u32x4*)(ws + WS_MOD + 1024 + fo);
    u32x4 xv[8];
#pragma unroll
    for (int i = 0; i < 8; ++i) xv[i] = *(const u32x4*)((const char*)p.x + (size_t)(rbk * 64 + wid * 8 + i) * 1024 + fo);
    float st[16];
#pragma unroll
    for (int i = 0; i < 16; ++i) st[i] = p.lnst[(rbk * 64 + wid * 8) * 2 + i];
    SCHED_FENCE();
    float ca[8], sa[8];
#pragma unroll
    for (int e = 0; e < 4; ++e) { ca[2 * e] = bflo(cav[e]); ca[2 * e + 1] = bfhi(cav[e]); sa[2 * e] = bflo(sav[e]); sa[2 * e + 1] = bfhi(sav[e]); }
    if (PRIO_P && wid >= 4) __builtin_amdgcn_s_setprio(1);
#pragma unroll
    for (int hf = 0; hf < 2; ++hf) {
      float lo[16], hi[16], rr[4]; unsigned t[16];
#define H_STAGE(body) _Pragma("unroll") for (int j = 0; j < 16; ++j) { const int i = hf * 4 + (j >> 2), d = j & 3; (void)i; (void)d; body; } SCHED_FENCE()
#pragma unroll
      for (int i = 0; i < 4; ++i) rr[i] = __builtin_amdgcn_rcpf(st[2 * (hf * 4 + i) + 1]);
      H_STAGE(lo[j] = bflo(xv[i][d]); hi[j] = bfhi(xv[i][d]));
      H_STAGE(lo[j] -= st[2 * i]; hi[j] -= st[2 * i]);
      H_STAGE(lo[j] = rb(lo[j]); hi[j] = rb(hi[j]));
      if (PRIO_P && hf == 1) __builtin_amdgcn_s_setprio(0);
      H_STAGE(lo[j] *= rr[j >> 2]; hi[j] *= rr[j >> 2]);
      H_STAGE(lo[j] = rb(lo[j]); hi[j] = rb(hi[j]));
#pragma unroll
      for (int i = 0; i < 4; ++i) ring[hf][i] = wq_load(hf, i);
      H_STAGE(lo[j] *= ca[2 * d]; hi[j] *= ca[2 * d + 1]);
      H_STAGE(lo[j] = rb(lo[j]); hi[j] = rb(hi[j]));
      H_STAGE(lo[j] += sa[2 * d]; hi[j] += sa[2 * d + 1]);
      H_STAGE(t[j] = pk2(lo[j], hi[j]));
#pragma unroll
      for (int i = 0; i < 4; ++i) *(u32x4*)(lds + L1_HT + (wid * 8 + hf * 4 + i) * HT_STRIDE + fo) = (u32x4){t[4 * i], t[4 * i + 1], t[4 * i + 2], t[4 * i + 3]};
#pragma unroll
      for (int i = 0; i < 2; ++i) ring[2][hf * 2 + i] = wq_load(2, hf * 2 + i);
      SCHED_FENCE();
    }
  }

  Retile rtl; const u16* rsrc_w; int rld, rk0, rn0; bool rperm; size_t rdst; unsigned rhalf = 2048;
  if (wid < 4) { const int id = rbk * 4 + wid, s = id >> 3, ng = id & 7; rsrc_w = p.wo; rld = 512; rk0 = g * 512 + 32 * s; rn0 = 64 * ng; rperm = false; rdst = WS_WOT + (size_t)(g * 8 + ng) * 65536 + s * 2048; rhalf = 32768; }
  else {
    int id = blockIdx.x * 4 + wid - 4, s, ng; rperm = true;
    if (id < 128)      { ng = id >> 4; s = id & 15; rsrc_w = p.w2; rld = 512; rdst = WS_W2T + (size_t)id * 4096; }
    else if (id < 576) { id -= 128; ng = id >> 4; s = id & 15; rsrc_w = p.w3; rld = F_; rdst = WS_W3T + (size_t)id * 4096; }
    else               { id -= 576; ng = id & 7; s = id >> 3; rsrc_w = p.w4; rld = 512; rdst = WS_W4T + (size_t)(ng * 56 + s) * 4096; }
    rk0 = 64 * (s >> 1) + 2 * (s & 1); rn0 = 64 * ng;
  }
  gu32* flags = (gu32*)(ws + WS_FLAG);
  gu32* flag = flags + (FL_W + g) * 32;
  gu32* flagw = flags + (FL_W + (lane & 7)) * 32;
  STAMP(1);
  __syncthreads();
  STAMP(2);
  if (wid >= 4 && PRIO_Q > 0) __builtin_amdgcn_s_setprio(1);

  const char* bl = lds + L1_HT + a * HT_STRIDE + g4 * 16;
  auto bread = [&](int s, int qt) AI { return *(const bf16x8*)(bl + 64 * ((s + rot) & 15) + qt * 16 * HT_STRIDE); };
  f32x16 o[4]; bf16x8 qf[8]; float l0, l1, negmc;
  {
    f32x4 acc[1][4][4];
    gemm_steps<4, 4, 16, DEPTH, 1>(acc, ring, wq_load, bread, [&](auto S_, auto M_) AI {
      constexpr int s = S_.value, m = M_.value;
      if constexpr (s < 3 && (m & 3) == 1) ring[3 + s][m >> 2] = wq_load(3 + s, m >> 2);
      if constexpr (s == PRIO_Q && m == 0) __builtin_amdgcn_s_setprio(0);
      if constexpr (s == 11 && m == 8) { WAIT_VM0(); dmaK(0); }
      if constexpr (s == 12 && m == 0) dmaK(1);
      if constexpr (s == 12 && m == 8) dmaV(0);
      if constexpr (s == 13 && m == 0) dmaK(2);
      if constexpr (s == 13 && m == 8) dmaV(1);
    });
    STAMP(3);
    __syncthreads();
#pragma unroll
    for (int qt = 0; qt < 4; ++qt) {
      u32x4 w0, w1; pack_acc16(acc[0], qt, w0, w1);
      char* dst = lds + L1_HT + ((wid >> 1) * 64 + qt * 16 + a) * QX_STRIDE + ((wid & 1) * 64 + g4 * 16) * 2;
      *(u32x4*)dst = w0; *(u32x4*)(dst + 16) = w1;
    }
    asm volatile("s_waitcnt vmcnt(0)" ::: "memory");
    __syncthreads();
#pragma unroll
    for (int s = 0; s < 8; ++s) qf[s] = *(const bf16x8*)(lds + L1_HT + ((wid >> 1) * 64 + (wid & 1) * 32 + r32) * QX_STRIDE + 32 * s + 16 * hh);
  }
  STAMP(4);

  const unsigned kx = (r32 >> 2) & 3;
  const char* kb0 = lds + L1_K + 2048 * (r32 >> 3) + 64 * (r32 & 7) + 16 * ((0 + hh) ^ kx);
  const char* kb1 = lds + L1_K + 2048 * (r32 >> 3) + 64 * (r32 & 7) + 16 * ((2 + hh) ^ kx);
  const char* vb = lds + L1_V + (4 * hh + ((lane >> 2) & 3)) * 64 + ((lane >> 4) & 1) * 32 + (lane & 3) * 8;
  auto kfrag = [&](int t, int m) AI { return *(const bf16x8*)(((m & 1) ? kb1 : kb0) + (t % 3) * 16384 + 512 * ((m & 7) >> 1) + 8192 * (m >> 3)); };
  auto vfrag = [&](int t, int m) AI {
    const char* q = vb + (t & 1) * 16384 + (m >> 2) * 4096 + (m & 3) * 512;
    bf16x4 lo = __builtin_amdgcn_ds_read_tr16_b64_v4bf16(LDSP(bf16x4, q)), hi = __builtin_amdgcn_ds_read_tr16_b64_v4bf16(LDSP(bf16x4, q + 2048));
    return __builtin_shufflevector(lo, hi, 0, 1, 2, 3, 4, 5, 6, 7);
  };
  auto fixup = [&](const f32x16& S, float l0p, float l1p, u32x4& pa, u32x4& pb, bool pend, u32x4& pend0, u32x4& pend1) AI {
    const float mx = swap32_max(__builtin_fmaf(max16(S), SM_C, negmc));
    const float dm = fmaxf(__builtin_ceilf(mx), 0.f);
    const float f = __builtin_amdgcn_exp2f(-dm);
    negmc -= dm; l0 = l0p * f; l1 = l1p * f;
#pragma unroll
    for (int db = 0; db < 4; ++db) o[db] *= f;
    if (pend) {
#pragma unroll
      for (int q = 0; q < 4; ++q) { pend0[q] = scale_pk(pend0[q], f); pend1[q] = scale_pk(pend1[q], f); }
    }
    sm_half(S, negmc, l0, l1, pa, pb);
  };

  f32x16 Sa0, Sa1, Sb0, Sb1;
  u32x4 Pa0, Pa1, Pb0, Pb1, P2, P3;
  bf16x8 kf[4], vf[4];
  {
#pragma unroll
    for (int m = 0; m < 16; ++m) {
      const bf16x8 k = kfrag(0, m);
      if (m < 8) Sa0 = MFMA32(k, qf[m & 7], m == 0 ? (f32x16){} : Sa0); else Sa1 = MFMA32(k, qf[m & 7], m == 8 ? (f32x16){} : Sa1);
    }
  }
#pragma unroll
  for (int m = 0; m < 3; ++m) kf[m] = kfrag(1, m);
  negmc = -swap32_max(fmaxf(max16(Sa0), max16(Sa1))) * SM_C;
  l0 = 0.f; l1 = 0.f;
  sm_half(Sa0, negmc, l0, l1, Pa0, Pa1);
#pragma unroll
  for (int db = 0; db < 4; ++db) o[db] = (f32x16){};
  SCHED_FENCE();

  const rsrc_t rWo = mk_rsrc(ws + WS_WOT + (size_t)(g * 8 + wid) * 65536, 65536);
  const rsrc_t rRt = mk_rsrc(ws + rdst, 36864);
  bf16x8 ring2[12][2];
  auto wo_load = [&](int sg, int i) AI { return __builtin_bit_cast(bf16x8, __builtin_amdgcn_raw_buffer_load_b128(rWo, fo + i * 1024, (sg >> 4) * 32768 + ((sg + rot) & 15) * 2048, 16)); };
  unsigned flagv = 0;

  auto tile = [&](auto T_, const f32x16& C1, f32x16& N0, f32x16& N1, u32x4& Pc0, u32x4& Pc1, u32x4& Pn0, u32x4& Pn1) AI {
    constexpr int T = T_.value;
    if (T == 4) STAMP(12);
    if (PRIO_A && wid >= 4) __builtin_amdgcn_s_setprio(1);
    {
      const float l0p = l0, l1p = l1; SmPipe sp;
#pragma unroll
      for (int m = 0; m < 16; ++m) {
        if (T < 7) {
          if (m < 8) N0 = MFMA32(kf[m & 3], qf[m & 7], m == 0 ? (f32x16){} : N0); else N1 = MFMA32(kf[m & 3], qf[m & 7], m == 8 ? (f32x16){} : N1);
          if (m + 3 < 16) kf[(m + 3) & 3] = kfrag(T + 1, m + 3);
        }
        if (m >= 13) vf[m - 13] = vfrag(T, m - 13);
        sm_gap(m, sp, C1, negmc, l0, l1, P2, P3);
        if (T == 7) ring2[m >> 1][m & 1] = wo_load(m >> 1, m & 1);
        if (T < 7) SCHED_FENCE();
      }
      MEM_FENCE();
      if (__builtin_expect(__any(!(l0 + l1 <= SM_LIMIT)), 0)) fixup(C1, l0p, l1p, P2, P3, true, Pc0, Pc1);
    }
    if (T == 4) STAMP(13);
    if (PRIO_A == 1) __builtin_amdgcn_s_setprio(0);
    {
      const float l0p = l0, l1p = l1; SmPipe sp;
#pragma unroll
      for (int m = 0; m < 16; ++m) {
        const u32x4 pp = (m < 4) ? Pc0 : (m < 8) ? Pc1 : (m < 12) ? P2 : P3;
        o[m & 3] = MFMA32(vf[m & 3], __builtin_bit_cast(bf16x8, pp), o[m & 3]);
        if (m + 3 < 16) vf[(m + 3) & 3] = vfrag(T, m + 3);
        if (T < 7) sm_gap(m, sp, N0, negmc, l0, l1, Pn0, Pn1);
        if (T == 7 && m < 8) ring2[8 + (m >> 1)][m & 1] = wo_load(8 + (m >> 1), m & 1);
        if (PRIO_A == 2 && m == 4) __builtin_amdgcn_s_setprio(0);
        if (T < 7 && m == 13) {
          if (T == 6 && wid == 0) {
            for (unsigned spins = 0; !__all(flagv == 32u) && spins < SPIN_CAP; ++spins) { __builtin_amdgcn_s_sleep(2); flagv = __hip_atomic_load(flagw, __ATOMIC_RELAXED, __HIP_MEMORY_SCOPE_AGENT); }
          }
          if (T == 4) STAMP(14);
          if (T == 2) WAIT_VM0();
          if (T == 0)      asm volatile("s_waitcnt lgkmcnt(0)" : "+v"(vf[2]), "+v"(vf[3])::"memory");
          else if (T == 1) asm volatile("s_waitcnt vmcnt(8) lgkmcnt(0)" : "+v"(vf[2]), "+v"(vf[3])::"memory");
          else if (T == 3) asm volatile("s_waitcnt vmcnt(4) lgkmcnt(0)" : "+v"(vf[2]), "+v"(vf[3])::"memory");
          else             asm volatile("s_waitcnt vmcnt(0) lgkmcnt(0)" : "+v"(vf[2]), "+v"(vf[3])::"memory");
          __builtin_amdgcn_s_barrier();
          MEM_FENCE();
          if (T == 4) STAMP(15);
          if (T == 4 && tid == 0) __hip_atomic_fetch_add(flag, 1u, __ATOMIC_RELAXED, __HIP_MEMORY_SCOPE_AGENT);
          if (T == 5 && wid == 0) flagv = __hip_atomic_load(flagw, __ATOMIC_RELAXED, __HIP_MEMORY_SCOPE_AGENT);
        }
        if (T < 6 && m == 13) { kf[0] = kfrag(T + 2, 0); kf[1] = kfrag(T + 2, 1); }
        if (T < 6 && m == 14) kf[2] = kfrag(T + 2, 2);
        if (T < 5 && m == 14) dmaK(T + 3);
        if (T < 6 && m == 15) dmaV(T + 2);
        if (T == 0 && m == 15) { retile_load(rtl, rsrc_w, rld, rk0, rperm, rn0, lane); MEM_FENCE(); }
        if (T == 2 && m == 15) {
#pragma unroll
          for (int i = 0; i < 4; ++i) __builtin_amdgcn_raw_buffer_store_b128(retile_frag(rtl, i), rRt, fo + (i >> 1) * rhalf + (i & 1) * 1024, 0, 16);
        }
        SCHED_FENCE();
      }
      MEM_FENCE();
      if (T < 7) {
        if (__builtin_expect(__any(!(l0 + l1 <= SM_LIMIT)), 0)) fixup(N0, l0p, l1p, Pn0, Pn1, false, Pc0, Pc1);
      }
    }
  };
  STAMP(5);
  tile(IC<0>{}, Sa1, Sb0, Sb1, Pa0, Pa1, Pb0, Pb1);
  tile(IC<1>{}, Sb1, Sa0, Sa1, Pb0, Pb1, Pa0, Pa1);
  tile(IC<2>{}, Sa1, Sb0, Sb1, Pa0, Pa1, Pb0, Pb1);
  tile(IC<3>{}, Sb1, Sa0, Sa1, Pb0, Pb1, Pa0, Pa1);
  tile(IC<4>{}, Sa1, Sb0, Sb1, Pa0, Pa1, Pb0, Pb1);
  tile(IC<5>{}, Sb1, Sa0, Sa1, Pb0, Pb1, Pa0, Pa1);
  STAMP(6);
  tile(IC<6>{}, Sa1, Sb0, Sb1, Pa0, Pa1, Pb0, Pb1);
  tile(IC<7>{}, Sb1, Sa0, Sa1, Pb0, Pb1, Pa0, Pa1);
  STAMP(7);

  {
    const float inv = 1.f / swap32_add(l0 + l1);
    char* dst = lds + L1_HT + ((wid & 1) * 32 + r32) * HT_STRIDE + ((wid >> 1) * 128 + 4 * hh) * 2;
#pragma unroll
    for (int db = 0; db < 4; ++db)
#pragma unroll
      for (int gq = 0; gq < 4; ++gq) {
        u32x2 w = {pk2(o[db][4 * gq] * inv, o[db][4 * gq + 1] * inv), pk2(o[db][4 * gq + 2] * inv, o[db][4 * gq + 3] * inv)};
        *(u32x2*)(dst + (32 * db + 8 * gq) * 2) = w;
      }
  }
  __syncthreads();
  STAMP(8);
  if (wid >= 4 && PRIO_O > 0) __builtin_amdgcn_s_setprio(1);
  {
    f32x4 acc[2][2][4];
    const rsrc_t rP = mk_rsrc(ws + WS_PART + (size_t)((rbk * 8 + g) * 8 + wid) * 8192, 8192);
    auto pstore = [&](int ps, int qt) AI {
      u32x4 w;
#pragma unroll
      for (int r = 0; r < 4; ++r) w[r] = pk2(acc[ps][0][qt][r], acc[ps][1][qt][r]);
      __builtin_amdgcn_raw_buffer_store_b128(w, rP, fo + (qt * 2 + ps) * 1024, 0, PART_AUX);
    };
    gemm_steps<2, 4, 32, 12, 2, 2>(acc, ring2, wo_load, bread, [&](auto S_, auto M_) AI {
      if constexpr (S_.value == PRIO_O && M_.value == 0) __builtin_amdgcn_s_setprio(0);
      if constexpr (S_.value >= 20 && S_.value < 22 && (M_.value & 3) == 1) pstore(0, (S_.value - 20) * 2 + (M_.value >> 2));
      if constexpr (S_.value == P0_STEP && M_.value == 7) {
        WAIT_VM0(); asm volatile("s_waitcnt vmcnt(0)" ::: "memory"); __builtin_amdgcn_s_barrier(); MEM_FENCE();
        if (tid == 0) __hip_atomic_fetch_add(flags + (FL_P0 + rbk) * 32, 1u, __ATOMIC_RELAXED, __HIP_MEMORY_SCOPE_AGENT);
      } });
    STAMP(9);
#pragma unroll
    for (int qt = 0; qt < 4; ++qt) pstore(1, qt);
  }
  STAMP(10);

  const rsrc_t rWs = mk_rsrc(ws, (unsigned)WS_END);
  auto ld1 = [&](size_t off, unsigned soff) AI { return __builtin_amdgcn_raw_buffer_load_b128(rWs, (unsigned)off, soff, 16); };
  auto ldf1 = [&](size_t off, unsigned soff) AI { return __builtin_bit_cast(bf16x8, __builtin_amdgcn_raw_buffer_load_b128(rWs, (unsigned)off, soff, 16)); };
  auto st1 = [&](size_t off, u32x4 v) AI { __builtin_amdgcn_raw_buffer_store_b128(v, rWs, (unsigned)off, 0, 16); };
  auto publish = [&](int idx) AI {
    asm volatile("s_waitcnt vmcnt(0) lgkmcnt(0)" ::: "memory"); __builtin_amdgcn_s_barrier(); MEM_FENCE();
    if (tid == 0) __hip_atomic_fetch_add(flags + idx * 32, 1u, __ATOMIC_RELAXED, __HIP_MEMORY_SCOPE_AGENT);
  };
  auto await = [&](int pw, int idx, unsigned want) AI {
    if (wid == pw) {
      unsigned spins = 0;
      while (!__all(__hip_atomic_load(flags + idx * 32, __ATOMIC_RELAXED, __HIP_MEMORY_SCOPE_AGENT) >= want) && ++spins < SPIN_CAP) __builtin_amdgcn_s_sleep(1);
    }
    MEM_FENCE(); __builtin_amdgcn_s_barrier(); MEM_FENCE();
  };
  const int b = blockIdx.x;
  const u16* modb = (const u16*)(ws + WS_MOD);
  publish(FL_P + rbk);
  STAMP(11);
  RT(1, 1);

  RT(2, 0);
  STAMPK(2, 0);
  {
    f32x2* red = (f32x2*)(lds + 16 * HT_STRIDE);
    const int rt = (b & 7) * 16 + (b >> 4), nh = (b >> 3) & 1;
    const int prb = rt >> 2, qt = rt & 3, row = rt * 16 + a, col = wid * 64 + g4 * 16;
    constexpr int D1 = 12, D1E = 12;
    bf16x8 A[D1][2];
    const int G = nh * 4 + (wid >> 1), ps = wid & 1;
    const rsrc_t rW = mk_rsrc(ws + WS_W1T + (size_t)G * 65536 + ps * 2048, 65536);
    auto aload = [&](int s, int i) AI { return ldfrag(rW, fo, (s * 4 + i) * 1024); };
    u32x4 pv[8][2];
    const size_t poff = WS_PART + (size_t)((prb * 64 + wid) * 8 + qt * 2) * 1024 + fo;
    const int prot = 0;
    await(0, FL_P0 + prb, 8);
    if (wid != 7) {
#pragma unroll
      for (int gg = 0; gg < 8; ++gg) pv[gg][0] = ld1(poff, (((gg + prot) & 7) * 64) * 1024);
    }
    SCHED_FENCE();
    await(7, FL_P + prb, 8);
    STAMPK(2, 1);
#pragma unroll
    for (int gg = 0; gg < 8; ++gg) pv[gg][1] = ld1(poff, (((gg + prot) & 7) * 64 + 1) * 1024);
    if (wid == 7) {
#pragma unroll
      for (int gg = 0; gg < 8; ++gg) pv[gg][0] = ld1(poff, (((gg + prot) & 7) * 64) * 1024);
    }
    u32x4 xv[2], gav[2], cbv[2], sbv[2];
#pragma unroll
    for (int h = 0; h < 2; ++h) {
      xv[h] = *(const u32x4*)(p.x + (size_t)row * D_ + col + 8 * h);
      gav[h] = *(const u32x4*)(modb + 1024 + col + 8 * h); sbv[h] = *(const u32x4*)(modb + 1536 + col + 8 * h); cbv[h] = *(const u32x4*)(modb + 2048 + col + 8 * h);
    }
    SCHED_FENCE();
    __builtin_amdgcn_s_barrier();
#pragma unroll
    for (int s = 0; s < D1E; ++s) { A[s][0] = aload(s, 0); A[s][1] = aload(s, 1); }
    SCHED_FENCE();
    float v[16];
#pragma unroll
    for (int d = 0; d < 8; ++d) { v[2 * d] = bflo(pv[0][d & 1][d >> 1]); v[2 * d + 1] = bfhi(pv[0][d & 1][d >> 1]); }
#pragma unroll
    for (int gg = 1; gg < 8; ++gg) {
#pragma unroll
      for (int d = 0; d < 8; ++d) { v[2 * d] += bflo(pv[gg][d & 1][d >> 1]); v[2 * d + 1] += bfhi(pv[gg][d & 1][d >> 1]); }
      SCHED_FENCE();
    }
#pragma unroll
    for (int d = 0; d < 8; ++d) {
      const unsigned ga = gav[d >> 2][d & 3], xx = xv[d >> 2][d & 3];
      const unsigned x2 = pk2(__builtin_fmaf(bflo(ga), v[2 * d], bflo(xx)), __builtin_fmaf(bfhi(ga), v[2 * d + 1], bfhi(xx)));
      xv[d >> 2][d & 3] = x2; v[2 * d] = bflo(x2); v[2 * d + 1] = bfhi(x2);
    }
    if (nh == 0) { st1(WS_X2 + (size_t)row * 1024 + col * 2, xv[0]); st1(WS_X2 + (size_t)row * 1024 + col * 2 + 16, xv[1]); }
    STAMPK(2, 2);
    auto row_stats = [&](f32x2* rd, float& mean, float& rstd) AI {
      float s0 = 0, s1 = 0, q0 = 0, q1 = 0;
#pragma unroll
      for (int e = 0; e < 8; ++e) { s0 += v[2 * e]; s1 += v[2 * e + 1]; q0 = __builtin_fmaf(v[2 * e], v[2 * e], q0); q1 = __builtin_fmaf(v[2 * e + 1], v[2 * e + 1], q1); }
      float s = swap32_add(swap16_add(s0 + s1)), q = swap32_add(swap16_add(q0 + q1));
      if (g4 == 0) rd[wid * 16 + a] = (f32x2){s, q};
      asm volatile("s_waitcnt lgkmcnt(0)" ::: "memory"); __builtin_amdgcn_s_barrier(); MEM_FENCE();
      f32x2 t = rd[a];
#pragma unroll
      for (int w = 1; w < 8; ++w) t += rd[w * 16 + a];
      mean = t[0] * (1.f / 512.f);
      rstd = __builtin_amdgcn_rsqf(fmaxf(t[1] * (1.f / 512.f) - mean * mean, 0.f) + 1e-5f);
    };
    float mean, rstd;
#pragma unroll
    for (int s = D1E; s < D1; ++s) { A[s][0] = aload(s, 0); A[s][1] = aload(s, 1); }
    SCHED_FENCE();
    row_stats(red, mean, rstd);
    u32x4 hv[2];
#pragma unroll
    for (int d = 0; d < 8; ++d) {
      const unsigned cb = cbv[d >> 2][d & 3], sb = sbv[d >> 2][d & 3];
      const unsigned hb = pk2((v[2 * d] - mean) * rstd * bflo(cb) + bflo(sb), (v[2 * d + 1] - mean) * rstd * bfhi(cb) + bfhi(sb));
      hv[d & 1][d >> 1] = hb; v[2 * d] = bflo(hb); v[2 * d + 1] = bfhi(hb);
    }
    if (nh == 1) { st1(WS_HB + (size_t)row * 1024 + wid * 128 + g4 * 16, hv[0]); st1(WS_HB + (size_t)row * 1024 + wid * 128 + 64 + g4 * 16, hv[1]); }
    row_stats(red + 128, mean, rstd);
    const float nm = -mean * rstd;
#pragma unroll
    for (int h = 0; h < 2; ++h) {
      u32x4 o;
#pragma unroll
      for (int e = 0; e < 4; ++e) o[e] = pk2(__builtin_fmaf(v[8 * h + 2 * e], rstd, nm), __builtin_fmaf(v[8 * h + 2 * e + 1], rstd, nm));
      *(u32x4*)(lds + a * HT_STRIDE + (col + 8 * h) * 2) = o;
    }
    STAMPK(2, 3);
    asm volatile("s_waitcnt lgkmcnt(0)" ::: "memory"); __builtin_amdgcn_s_barrier(); MEM_FENCE();
    STAMPK(2, 4);
    f32x4 acc[1][2][1];
    gemm_steps<2, 1, 16, D1, 1, 4>(acc, A, aload, [&](int s, int) AI { return *(const bf16x8*)(bl + 64 * s); }, [](auto, auto) AI {});
    STAMPK(2, 5);
    u32x4 ov;
#pragma unroll
    for (int rg = 0; rg < 4; ++rg) ov[rg] = pk2(gelu_tanh(acc[0][0][0][rg]), gelu_tanh(acc[0][1][0][rg]));
    st1(WS_H1 + (size_t)(rt * 16 + 2 * G + ps) * 1024 + fo, ov);
    STAMPK(2, 6);
    publish(FL_H1 + rt);
    RT(2, 1);
    STAMPK(2, 7);
  }

  const int rb2 = (b & 7) * 4 + (b >> 6), ns = (b >> 3) & 7;
  const int ng = ns * 7 + (wid < 7 ? wid : 6);
  const size_t w3off = WS_W3T + (size_t)(ng >> 1) * 65536 + (ng & 1) * 2048 + fo;
  bf16x8 A[16][2];
  RT(3, 0);
  STAMPK(3, 0);
  {
    const int rt2 = (b & 7) * 8 + (b >> 5), nq = (b >> 3) & 3;
    const int G = nq * 2 + ((wid & 3) >> 1), ps = wid & 1;
    if (wid < 4) {
      const size_t aoff = WS_W2T + (size_t)G * 65536 + ps * 2048 + fo;
#pragma unroll
      for (int s = 0; s < 16; ++s) { A[s][0] = ldf1(aoff, (s * 4) * 1024); A[s][1] = ldf1(aoff, (s * 4 + 1) * 1024); }
    }
    SCHED_FENCE();
    { const int t = 2 * rt2 - 1 + (lane & 3); await(7, FL_H1 + (t < 0 ? 0 : t > 127 ? 127 : t), 2); }
    STAMPK(3, 1);
    const rsrc_t rH = mk_rsrc(ws + WS_H1, (unsigned)NXB);
    const int sh = (wid < 2) ? -1 : (wid < 4) ? 1 : (wid < 6) ? -2 : 2;
    u32x4 hf[2][2], hbv[2];
#pragma unroll
    for (int qt = 0; qt < 2; ++qt) {
      const int r = rt2 * 32 + qt * 16 + a + sh;
      const unsigned ho = (unsigned)((r >> 4) * 16384 + ((r & 15) + 16 * g4) * 16);
      hf[qt][0] = __builtin_amdgcn_raw_buffer_load_b128(rH, ho, (wid * 2) * 1024, 16); hf[qt][1] = __builtin_amdgcn_raw_buffer_load_b128(rH, ho, (wid * 2 + 1) * 1024, 16);
    }
    const size_t xoff = (size_t)(rt2 * 32 + a) * 1024 + G * 128 + ps * 64 + g4 * 16;
    if (wid < 4) { hbv[0] = ld1(WS_HB + xoff, 0); hbv[1] = ld1(WS_HB + xoff, 16384); }
#pragma unroll
    for (int qt = 0; qt < 2; ++qt) { *(u32x4*)(lds + (qt * 16 + wid * 2) * 1024 + fo) = hf[qt][0]; *(u32x4*)(lds + (qt * 16 + wid * 2 + 1) * 1024 + fo) = hf[qt][1]; }
    asm volatile("s_waitcnt lgkmcnt(0)" ::: "memory"); __builtin_amdgcn_s_barrier(); MEM_FENCE();
    STAMPK(3, 2);
    f32x2* red = (f32x2*)(lds + 32768);
    if (wid < 4) {
      f32x4 acc[1][2][2];
      gemm_steps<2, 2, 16, 16, 1, 3>(acc, A, [&](int, int) AI { return bf16x8{}; }, [&](int s, int qt) AI { return *(const bf16x8*)(lds + (qt * 16 + s) * 1024 + fo); }, [](auto, auto) AI {});
#pragma unroll
      for (int qt = 0; qt < 2; ++qt) {
        u32x4 ov; float s = 0, q = 0;
#pragma unroll
        for (int rg = 0; rg < 4; ++rg) {
          ov[rg] = pk2(bflo(hbv[qt][rg]) + acc[0][0][qt][rg], bfhi(hbv[qt][rg]) + acc[0][1][qt][rg]);
          const float lo = bflo(ov[rg]), hi = bfhi(ov[rg]); s += lo + hi; q += lo * lo + hi * hi;
        }
        st1(WS_X1 + xoff + qt * 16384, ov);
        s = swap32_add(swap16_add(s)); q = swap32_add(swap16_add(q));
        if (g4 == 0) red[wid * 32 + qt * 16 + a] = (f32x2){s, q};
      }
    } else if (wid < 7) {
#pragma unroll
      for (int s = 0; s < 16; ++s) { A[s][0] = ldf1(w3off, (s * 4) * 1024); A[s][1] = ldf1(w3off, (s * 4 + 1) * 1024); }
    }
    asm volatile("s_waitcnt lgkmcnt(0)" ::: "memory"); __builtin_amdgcn_s_barrier(); MEM_FENCE();
    if (tid < 32) {
      const f32x2 t = red[tid] + red[32 + tid] + red[64 + tid] + red[96 + tid];
      __hip_atomic_store((gu64*)(ws + WS_ST + (size_t)(rt2 * 32 + tid) * 32 + nq * 8), __builtin_bit_cast(unsigned long long, t), __ATOMIC_RELAXED, __HIP_MEMORY_SCOPE_AGENT);
    }
    STAMPK(3, 3);
    publish(FL_X1 + (rt2 >> 1));
    RT(3, 1);
    STAMPK(3, 4);
  }

  bf16x8 W[7][4];
  const size_t woff = WS_W4T + (size_t)(ns * 56 + wid * 7) * 4096 + fo;
  auto rot7 = [&](int s) AI { const int t = s + (ROT ? (ns < 7 ? ns : 0) : 0); return t >= 7 ? t - 7 : t; };
  RT(4, 0);
  STAMPK(4, 0);
  {
    if (wid < 4) {
#pragma unroll
      for (int s = 0; s < 16; ++s) { A[s][0] = ldf1(w3off, (s * 4) * 1024); A[s][1] = ldf1(w3off, (s * 4 + 1) * 1024); }
    }
    SCHED_FENCE();
    await(7, FL_X1 + rb2, 8);
    STAMPK(4, 1);
    u32x4 xv[8];
    const u32x4 stv = ld1(WS_ST + (size_t)(rb2 * 64 + wid * 8) * 32 + (lane & 15) * 16, 0);
#pragma unroll
    for (int i = 0; i < 8; ++i) xv[i] = ld1(WS_X1 + (size_t)(rb2 * 64 + wid * 8) * 1024 + fo, i * 1024);
    SCHED_FENCE();
    const float sm = dpp_add<0xB1>(__uint_as_float(stv[0]) + __uint_as_float(stv[2])) * (1.f / 512.f);
    const float sq = dpp_add<0xB1>(__uint_as_float(stv[1]) + __uint_as_float(stv[3])) * (1.f / 512.f);
    const float rsl = __builtin_amdgcn_rsqf(fmaxf(sq - sm * sm, 0.f) + 1e-5f), nml = -sm * rsl;
    float rs[8], nm[8];
#pragma unroll
    for (int i = 0; i < 8; ++i) { rs[i] = __uint_as_float(__builtin_amdgcn_readlane(__float_as_uint(rsl), 2 * i)); nm[i] = __uint_as_float(__builtin_amdgcn_readlane(__float_as_uint(nml), 2 * i)); }
    SCHED_FENCE();
#pragma unroll
    for (int hf = 0; hf < 2; ++hf) {
      float lo[16], hi[16]; unsigned t[16];
      H_STAGE(lo[j] = bflo(xv[i][d]); hi[j] = bfhi(xv[i][d]));
      H_STAGE(lo[j] = __builtin_fmaf(lo[j], rs[i], nm[i]); hi[j] = __builtin_fmaf(hi[j], rs[i], nm[i]));
      H_STAGE(t[j] = pk2(lo[j], hi[j]));
#pragma unroll
      for (int i = 0; i < 4; ++i) *(u32x4*)(lds + (wid * 8 + hf * 4 + i) * HT_STRIDE + fo) = (u32x4){t[4 * i], t[4 * i + 1], t[4 * i + 2], t[4 * i + 3]};
      SCHED_FENCE();
    }
    STAMPK(4, 2);
    asm volatile("s_waitcnt lgkmcnt(0)" ::: "memory"); __builtin_amdgcn_s_barrier(); MEM_FENCE();
    STAMPK(4, 3);
    if (wid < 7) {
      f32x4 acc[2][2][2];
      if (wid >= 4) __builtin_amdgcn_s_setprio(1);
      const size_t dst = WS_U + (size_t)(rb2 * 4 * 56 + ng) * 1024 + fo;
      u32x4 ow;
      auto gpart = [&](int ps, int k) AI {
        unsigned w = pk2(gelu_tanh(acc[ps][0][k >> 2][k & 3]), gelu_tanh(acc[ps][1][k >> 2][k & 3])); PIN(w); ow[k & 3] = w;
        if ((k & 3) == 3) st1(dst + (size_t)(2 * ps + (k >> 2)) * 56 * 1024, ow);
      };
      gemm_steps<2, 2, 32, 16, 2, 3>(acc, A, [&](int s, int i) AI { return A[s & 15][i]; },
                                     [&](int s, int qt) AI { return *(const bf16x8*)(bl + ((s >> 4) * 2 + qt) * 16 * HT_STRIDE + 64 * (s & 15)); },
                                     [&](auto S_, auto M_) AI {
                                       constexpr int s = S_.value, m = M_.value;
                                       if constexpr (s == PRIO_G3 && m == 0) __builtin_amdgcn_s_setprio(0);
                                       if constexpr (s >= 17 && (s & 1) && m == 1) gpart(0, (s - 17) >> 1);
                                       if constexpr (s >= 17 && s < 31 && m >= 2) { constexpr int k = (s - 17) * 2 + m - 2; W[k >> 2][k & 3] = ldf1(woff + (k & 3) * 1024, rot7(k >> 2) * 4096); }
                                     });
      STAMPK(4, 4);
#pragma unroll
      for (int k = 0; k < 8; ++k) gpart(1, k);
    } else {
#pragma unroll
      for (int k = 0; k < 28; ++k) W[k >> 2][k & 3] = ldf1(woff + (k & 3) * 1024, rot7(k >> 2) * 4096);
    }
    STAMPK(4, 5);
    publish(FL_U + rb2);
    RT(4, 1);
    STAMPK(4, 6);
  }

  RT(5, 0);
  STAMPK(5, 0);
  {
    bf16x8 B[7][4];
    const size_t uoff = WS_U + (size_t)(rb2 * 4 * 56 + wid * 7) * 1024 + fo;
    const int oqt = wid >> 1, orp = wid & 1, orow = rb2 * 64 + oqt * 16 + a, ocol = ns * 64 + g4 * 16 + orp * 8;
    u32x2 x1a, x1b; u32x4 x2v, gbv;
    SCHED_FENCE();
    await(0, FL_U + rb2, 8);
#pragma unroll
    for (int s = 0; s < G4_AHEAD; ++s)
#pragma unroll
      for (int i = 0; i < 4; ++i) B[s][i] = ldf1(uoff, (i * 56 + rot7(s)) * 1024);
    SCHED_FENCE();
    STAMPK(5, 1);
    f32x4 acc[4][4] = {};
#pragma unroll
    for (int s = 0; s < 7; ++s) {
#pragma unroll
      for (int i = 0; i < 4; ++i) {
#pragma unroll
        for (int qt = 0; qt < 4; ++qt) acc[i][qt] = MFMA16(W[s][i], B[s][qt], acc[i][qt]);
        if (s + G4_AHEAD < 7) B[s + G4_AHEAD][i] = ldf1(uoff, (i * 56 + rot7(s + G4_AHEAD)) * 1024);
        SCHED_FENCE();
      }
      if (s == 5) {
        const size_t x1o = WS_X1 + (size_t)orow * 1024 + ns * 128 + g4 * 16 + orp * 8;
        x1a = __builtin_amdgcn_raw_buffer_load_b64(rWs, (unsigned)x1o, 0, 16); x1b = __builtin_amdgcn_raw_buffer_load_b64(rWs, (unsigned)x1o, 64, 16);
        x2v = ld1(WS_X2 + (size_t)orow * 1024 + ocol * 2, 0); gbv = *(const u32x4*)(modb + 2560 + ocol);
        SCHED_FENCE();
      }
    }
    STAMPK(5, 2);
#pragma unroll
    for (int qt = 0; qt < 4; ++qt)
#pragma unroll
      for (int rg = 0; rg < 4; ++rg)
        *(f32x4*)(lds + ((wid * 4 + qt) * 4 + rg) * 1024 + fo) = (f32x4){acc[0][qt][rg], acc[1][qt][rg], acc[2][qt][rg], acc[3][qt][rg]};
    STAMPK(5, 3);
    __syncthreads();
    STAMPK(5, 4);
    f32x4 s0 = {}, s1 = {};
#pragma unroll
    for (int w = 0; w < 8; ++w) {
      s0 += *(const f32x4*)(lds + ((w * 4 + oqt) * 4 + 2 * orp) * 1024 + fo);
      s1 += *(const f32x4*)(lds + ((w * 4 + oqt) * 4 + 2 * orp + 1) * 1024 + fo);
    }
    const float h2[8] = {s0[0], s0[1], s0[2], s0[3], s1[0], s1[1], s1[2], s1[3]};
    const unsigned x1v[4] = {x1a[0], x1b[0], x1a[1], x1b[1]};
    u32x4 ov;
#pragma unroll
    for (int e = 0; e < 4; ++e)
      ov[e] = pk2(bflo(x2v[e]) + bflo(gbv[e]) * (bflo(x1v[e]) + h2[2 * e]), bfhi(x2v[e]) + bfhi(gbv[e]) * (bfhi(x1v[e]) + h2[2 * e + 1]));
    *(u32x4*)(p.out + (size_t)orow * D_ + ocol) = ov;
    STAMPK(5, 5);
    RT(5, 1);
    STAMPK(5, 6);
  }
}

extern "C" void kernel_launch(void* const* d_in, const int* in_sizes, int n_in, void* d_out, int out_size, void* d_ws, size_t ws_size, hipStream_t stream) {
  char* ws = (char*)d_ws;
  K0Args p0;
  p0.x = (const u16*)d_in[0]; p0.c = (const u16*)d_in[10]; p0.wmod = (const u16*)d_in[11]; p0.wq = (const u16*)d_in[3]; p0.w1 = (const u16*)d_in[6]; p0.kc = (const u16*)d_in[1]; p0.vc = (const u16*)d_in[2]; p0.ws = ws;
  K1Args p1;
  p1.x = p0.x; p1.kc = (const u16*)d_in[1]; p1.vc = (const u16*)d_in[2]; p1.wo = (const u16*)d_in[4];
  p1.w2 = (const u16*)d_in[7]; p1.w3 = (const u16*)d_in[8]; p1.w4 = (const u16*)d_in[9]; p1.lnst = (const float*)(ws + WS_Y); p1.ws = ws; p1.out = (u16*)d_out;
  (void)hipFuncSetAttribute((const void*)k1_attn, hipFuncAttributeMaxDynamicSharedMemorySize, L1_END);
  k0_prep<<<K0_BLOCKS, 512, 0, stream>>>(p0);
  k1_attn<<<256, 512, L1_END, stream>>>(p1);
}
```

```cpp
#include <hip/hip_runtime.h>
#include <hip/hip_bf16.h>
#include <type_traits>
#include <utility>

typedef unsigned short u16;
typedef __bf16 bf16x8 __attribute__((ext_vector_type(8)));
typedef __bf16 bf16x4 __attribute__((ext_vector_type(4)));
typedef __bf16 bf16x2 __attribute__((ext_vector_type(2)));
typedef float f32x16 __attribute__((ext_vector_type(16)));
typedef float f32x4 __attribute__((ext_vector_type(4)));
typedef float f32x2 __attribute__((ext_vector_type(2)));
typedef unsigned u32x2 __attribute__((ext_vector_type(2)));
typedef unsigned u32x4 __attribute__((ext_vector_type(4)));
typedef __amdgpu_buffer_rsrc_t rsrc_t;
#define DI __device__ __forceinline__
#define AI __attribute__((always_inline))
#define LDSP(T, p) ((__attribute__((address_space(3))) T*)(p))
#define SCHED_FENCE() __builtin_amdgcn_sched_barrier(0)
#define PIN(x) asm volatile("" : "+v"(x))
#define MEM_FENCE() asm volatile("" ::: "memory")
#define WAIT_VM0() __builtin_amdgcn_s_waitcnt(0x0F70)
#define STAMP(i)
#define STAMPK(k, i)
#define RT(k, i)

#define NT_SRC 1
#define K0_WARM 3
constexpr int S_ = 2048, D_ = 512, HQ = 32, HKV = 8, DH = 128, SK = 2048, WIN = 512, F_ = 1792;

constexpr size_t NXB = (size_t)S_ * D_ * 2;
constexpr size_t WS_MOD = 0;
constexpr size_t WS_Y   = 8192;
constexpr size_t WS_X2  = WS_Y  + NXB;
constexpr size_t WS_HB  = WS_X2 + NXB;
constexpr size_t WS_H1  = WS_HB + NXB;
constexpr size_t WS_X1  = WS_H1 + NXB;
constexpr size_t WS_U   = WS_X1 + NXB;
constexpr size_t WS_PART = WS_U + (size_t)S_ * F_ * 2;
constexpr size_t WS_WQT = WS_PART + 8 * NXB;
constexpr size_t WS_WOT = WS_WQT + (size_t)D_ * 4096 * 2;
constexpr size_t WS_W1T = WS_WOT + (size_t)D_ * 4096 * 2;
constexpr size_t WS_W2T = WS_W1T + (size_t)D_ * D_ * 2;
constexpr size_t WS_W3T = WS_W2T + (size_t)D_ * D_ * 2;
constexpr size_t WS_W4T = WS_W3T + (size_t)D_ * F_ * 2;
constexpr size_t WS_ST  = WS_W4T + (size_t)D_ * F_ * 2;
constexpr size_t WS_FLAG = WS_ST + (size_t)S_ * 32;
constexpr int FL_W = 0, FL_P = 8, FL_H1 = 40, FL_X1 = 168, FL_U = 200, FL_P0 = 232, FL_N = 264;
constexpr size_t WS_END = WS_FLAG + FL_N * 128;

DI float bflo(unsigned u) { return __uint_as_float(u << 16); }
DI float bfhi(unsigned u) { return __uint_as_float(u & 0xffff0000u); }
DI unsigned pk2(float lo, float hi) { f32x2 v = {lo, hi}; return __builtin_bit_cast(unsigned, __builtin_convertvector(v, bf16x2)); }
DI float rb(float x) { return __uint_as_float(pk2(0.f, x)); }
DI rsrc_t mk_rsrc(const void* p, unsigned bytes) { return __builtin_amdgcn_make_buffer_rsrc((void*)p, (short)0, (int)bytes, 0x00020000); }
DI unsigned rfl(unsigned x) { return __builtin_amdgcn_readfirstlane(x); }
DI bf16x8 ldfrag(rsrc_t r, unsigned voff, unsigned soff) { return __builtin_bit_cast(bf16x8, __builtin_amdgcn_raw_buffer_load_b128(r, voff, soff, 0)); }
template <int CTRL> DI float dpp_add(float v) {
  return v + __uint_as_float(__builtin_amdgcn_update_dpp(0u, __float_as_uint(v), CTRL, 0xf, 0xf, true));
}
DI float swap16_add(float v) { auto r = __builtin_amdgcn_permlane16_swap(__float_as_uint(v), __float_as_uint(v), false, false); return __uint_as_float(r[0]) + __uint_as_float(r[1]); }
DI float swap32_add(float v) { auto r = __builtin_amdgcn_permlane32_swap(__float_as_uint(v), __float_as_uint(v), false, false); return __uint_as_float(r[0]) + __uint_as_float(r[1]); }
DI float swap32_max(float v) { auto r = __builtin_amdgcn_permlane32_swap(__float_as_uint(v), __float_as_uint(v), false, false); return fmaxf(__uint_as_float(r[0]), __uint_as_float(r[1])); }
DI float wave_sum(float v) {
  v = dpp_add<0xB1>(v); v = dpp_add<0x4E>(v); v = dpp_add<0x141>(v); v = dpp_add<0x140>(v);
  v = swap16_add(v); v = swap32_add(v); return v;
}
DI float div_rn(float a, float d, float r) {
  float q = a * r; float e = __builtin_fmaf(-q, d, a); return __builtin_fmaf(e, r, q);
}
DI float gelu_tanh(float x) {
  const float u = 0.7978845608028654f * (x + 0.044715f * x * x * x);
  return x * __builtin_amdgcn_rcpf(1.f + __builtin_amdgcn_exp2f(-2.885390081777927f * u));
}
DI void ln_consts(float mean, float var, float& m_bf, float& d_bf, float& r) {
  m_bf = rb(mean); d_bf = rb(__builtin_sqrtf(rb(rb(var) + 1e-5f))); r = __builtin_amdgcn_rcpf(d_bf);
}
DI float ln_apply(float x, float m_bf, float d_bf, float r) { return rb(div_rn(rb(x - m_bf), d_bf, r)); }
DI u32x4 ln_row(const u32x4 xv) {
  float f[8];
#pragma unroll
  for (int e = 0; e < 4; ++e) { f[2 * e] = bflo(xv[e]); f[2 * e + 1] = bfhi(xv[e]); }
  float s = 0;
#pragma unroll
  for (int e = 0; e < 8; ++e) s += f[e];
  const float mean = wave_sum(s) * (1.f / 512.f);
  float q = 0;
#pragma unroll
  for (int e = 0; e < 8; ++e) { float d = f[e] - mean; q += d * d; }
  const float var = wave_sum(q) * (1.f / 512.f);
  float mb, db, r; ln_consts(mean, var, mb, db, r);
  u32x4 o;
#pragma unroll
  for (int e = 0; e < 4; ++e) o[e] = pk2(div_rn(rb(f[2 * e] - mb), db, r), div_rn(rb(f[2 * e + 1] - mb), db, r));
  return o;
}

#define MFMA16(a, b, c) __builtin_amdgcn_mfma_f32_16x16x32_bf16((a), (b), (c), 0, 0, 0)
#define MFMA32(a, b, c) __builtin_amdgcn_mfma_f32_32x32x16_bf16((a), (b), (c), 0, 0, 0)

DI void dma16(unsigned lds_dst, unsigned voff, rsrc_t r, unsigned soff) {
  asm volatile("s_mov_b32 m0, %0\n\ts_nop 0\n\tbuffer_load_dwordx4 %1, %2, %3 offen lds" ::"s"(lds_dst), "v"(voff), "s"(r), "s"(soff) : "memory");
}

struct Retile { u32x2 d[8]; };
DI void retile_load(Retile& t, const u16* __restrict__ W, int ldw, int k0, bool perm, int n0, int lane) {
  const int a = lane & 15, g4 = lane >> 4;
  const u16* p = W + (size_t)(k0 + (perm ? 16 : 8) * g4) * ldw + n0 + 4 * a;
  const int s2 = (perm ? 4 : 2) * ldw;
#pragma unroll
  for (int j = 0; j < 8; ++j) {
    const u32x2* q = (const u32x2*)(p + (j >> 1) * s2 + (j & 1) * ldw);
    t.d[j] = NT_SRC ? __builtin_nontemporal_load(q) : *q;
  }
}
DI u32x4 retile_frag(const Retile& t, int i) {
  u32x4 f;
#pragma unroll
  for (int p = 0; p < 4; ++p) f[p] = __builtin_amdgcn_perm(t.d[2 * p + 1][i >> 1], t.d[2 * p][i >> 1], (i & 1) ? 0x07060302u : 0x05040100u);
  return f;
}

struct K0Args { const u16 *x, *c, *wmod, *wq, *w1, *kc, *vc; char* ws; };
constexpr int K0_BLOCKS = 304 + 128 + 16;
__global__ __launch_bounds__(512) void k0_prep(const K0Args p) {
  __shared__ float red[8][64];
  const int tid = threadIdx.x, lane = tid & 63, wid = tid >> 6;
  int b = blockIdx.x;
#define WSP p.ws
  RT(0, 0);
  if (b < 48) {
    const int n0 = b * 64, cg = tid & 7, kq = tid >> 3;
    u32x4 wv[8]; float sv[8];
#pragma unroll
    for (int i = 0; i < 8; ++i) { const u32x4* q = (const u32x4*)(p.wmod + (size_t)(kq + 64 * i) * 3072 + n0 + cg * 8); wv[i] = NT_SRC ? __builtin_nontemporal_load(q) : *q; }
#pragma unroll
    for (int i = 0; i < 8; ++i) { float cv = bflo(p.c[kq + 64 * i]); sv[i] = rb(cv / (1.f + __expf(-cv))); }
    float acc[8] = {};
#pragma unroll
    for (int i = 0; i < 8; ++i)
#pragma unroll
      for (int e = 0; e < 4; ++e) { acc[2 * e] += sv[i] * bflo(wv[i][e]); acc[2 * e + 1] += sv[i] * bfhi(wv[i][e]); }
#pragma unroll
    for (int e = 0; e < 8; ++e) { float v = acc[e]; v += __shfl_xor(v, 8); v = swap16_add(v); v = swap32_add(v); acc[e] = v; }
    if (lane < 8)
#pragma unroll
      for (int e = 0; e < 8; ++e) red[wid][lane * 8 + e] = acc[e];
    __syncthreads();
    if (tid < 64) {
      float s = 0;
#pragma unroll
      for (int w = 0; w < 8; ++w) s += red[w][tid];
      s = rb(s);
      const int n = n0 + tid, seg = n >> 9;
      if (seg == 1 || seg == 4) s = rb(1.f + s);
      ((u16*)(p.ws + WS_MOD))[n] = (u16)(__float_as_uint(s) >> 16);
    }
    RT(0, 1);
    return;
  }
  b -= 48;
  if (b < 256) {
    const int row = b * 8 + wid;
    const u32x4 xv = *(const u32x4*)(p.x + (size_t)row * D_ + lane * 8);
    u32x4 wv[8];
#pragma unroll
    for (int i = 0; i < 8; ++i) wv[i] = *(const u32x4*)(p.x + (size_t)((b >> 3) * 64 + wid * 8 + i) * D_ + lane * 8);
    float f[8];
#pragma unroll
    for (int e = 0; e < 4; ++e) { f[2 * e] = bflo(xv[e]); f[2 * e + 1] = bfhi(xv[e]); }
    float s = 0;
#pragma unroll
    for (int e = 0; e < 8; ++e) s += f[e];
    const float mean = wave_sum(s) * (1.f / 512.f);
    float q = 0;
#pragma unroll
    for (int e = 0; e < 8; ++e) { float d = f[e] - mean; q += d * d; }
    const float var = wave_sum(q) * (1.f / 512.f);
    float mb, db, r; ln_consts(mean, var, mb, db, r);
    if (lane == 0) *(f32x2*)(p.ws + WS_Y + (size_t)row * 8) = (f32x2){mb, db};
#pragma unroll
    for (int i = 0; i < 8; ++i) asm volatile("" ::"v"(wv[i]));
    RT(0, 1);
    return;
  }
  b -= 256;
  Retile t; char* dst;
  if (b < 128) { const int g = b & 7, s = b >> 3, ng = g * 8 + wid; retile_load(t, p.wq, 4096, 32 * s, false, 64 * ng, lane); dst = p.ws + WS_WQT + (size_t)(ng * 16 + s) * 4096; }
  else {
    const int s = b - 128; retile_load(t, p.w1, 512, 32 * s, false, 64 * wid, lane); dst = p.ws + WS_W1T + (size_t)(wid * 16 + s) * 4096;
    if (s == 0 && tid < FL_N) *(unsigned*)(p.ws + WS_FLAG + tid * 128) = 0u;
  }
#pragma unroll
  for (int i = 0; i < 4; ++i) *(u32x4*)(dst + i * 1024 + lane * 16) = retile_frag(t, i);
  RT(0, 1);
#undef WSP
#define WSP ws
}

template <int V> using IC = std::integral_constant<int, V>;
template <int N, class F, int... Is> DI void sfor_(F&& f, std::integer_sequence<int, Is...>) { (f(IC<Is>{}), ...); }
template <int N, class F> DI void sfor(F&& f) { sfor_<N>(f, std::make_integer_sequence<int, N>{}); }
template <int NI, int NQT, int NSTEP, int DEPTH, int NSET, int BD = 1, class ALoad, class BRead, class Hook>
DI void gemm_steps(f32x4 (&acc)[NSET][NI][NQT], bf16x8 (&ring)[DEPTH][NI], ALoad aload, BRead bread, Hook hook) {
  constexpr int NM = NI * NQT, PER = NSTEP / NSET;
  bf16x8 B[BD + 1][NQT];
#pragma unroll
  for (int s = 0; s < BD; ++s)
#pragma unroll
    for (int qt = 0; qt < NQT; ++qt) B[s][qt] = bread(s, qt);
  SCHED_FENCE();
  sfor<NSTEP * NM>([&](auto X) AI {
    constexpr int s = X.value / NM, m = X.value % NM, i = m / NQT, qt = m % NQT;
    if constexpr (s % PER == 0) acc[s / PER][i][qt] = MFMA16(ring[s % DEPTH][i], B[s % (BD + 1)][qt], (f32x4){});
    else                        acc[s / PER][i][qt] = MFMA16(ring[s % DEPTH][i], B[s % (BD + 1)][qt], acc[s / PER][i][qt]);
    if constexpr (m < NQT && s + BD < NSTEP) B[(s + BD) % (BD + 1)][m] = bread(s + BD, m);
    if constexpr (qt == NQT - 1 && s + DEPTH < NSTEP) ring[s % DEPTH][i] = aload(s + DEPTH, i);
    hook(IC<s>{}, IC<m>{});
    SCHED_FENCE();
  });
}

constexpr int HT_STRIDE = 1056, QX_STRIDE = 272;
constexpr int L1_HT = 0, L1_K = 69632, L1_V = L1_K + 3 * 16384, L1_END = L1_V + 2 * 16384;
#define PRIO_G3 21
#define G4_AHEAD 2
#define ROT 1
#define P0_STEP 28
#define PRIO_P 1
#define PRIO_Q 11
#define PRIO_O 21
#define PRIO_A 0
#define GBAR 1
#define WO_SC1 1
#define OPASS 2
#define PF_ON 1
#define RT_AUX 16
#define PART_AUX 16
constexpr float SM_C = 0.12751743082459868f;
constexpr float SM_LIMIT = 1099511627776.f;

DI float max16(const f32x16& S) {
  float m = fmaxf(S[0], S[1]);
#pragma unroll
  for (int r = 2; r < 16; ++r) m = fmaxf(m, S[r]);
  return m;
}
DI unsigned scale_pk(unsigned p, float f) { return pk2(bflo(p) * f, bfhi(p) * f); }
DI void sm_half(const f32x16& S, float negmc, float& l0, float& l1, u32x4& pa, u32x4& pb) {
#pragma unroll
  for (int p = 0; p < 4; ++p) {
    const float e0 = __builtin_amdgcn_exp2f(__builtin_fmaf(S[2 * p], SM_C, negmc)), e1 = __builtin_amdgcn_exp2f(__builtin_fmaf(S[2 * p + 1], SM_C, negmc));
    l0 += e0; l1 += e1; pa[p] = pk2(e0, e1);
  }
#pragma unroll
  for (int p = 0; p < 4; ++p) {
    const float e0 = __builtin_amdgcn_exp2f(__builtin_fmaf(S[8 + 2 * p], SM_C, negmc)), e1 = __builtin_amdgcn_exp2f(__builtin_fmaf(S[8 + 2 * p + 1], SM_C, negmc));
    l0 += e0; l1 += e1; pb[p] = pk2(e0, e1);
  }
}
struct SmPipe { float x[16], e[16]; };
DI void sm_gap(const int m, SmPipe& sp, const f32x16& S, float negmc, float& l0, float& l1, u32x4& pa, u32x4& pb) {
#pragma unroll
  for (int e = 0; e < 16; ++e) {
    const int f = e * 13 / 16;
    if (f + 2 == m) {
      if (e & 1) {
        l1 += sp.e[e]; PIN(l1);
        unsigned w = pk2(sp.e[e - 1], sp.e[e]); PIN(w);
        if (e < 8) pa[e >> 1] = w; else pb[(e - 8) >> 1] = w;
      } else { l0 += sp.e[e]; PIN(l0); }
    }
  }
#pragma unroll
  for (int e = 0; e < 16; ++e) if (e * 13 / 16 + 1 == m) sp.e[e] = __builtin_amdgcn_exp2f(sp.x[e]);
#pragma unroll
  for (int e = 0; e < 16; ++e) if (e * 13 / 16 == m) sp.x[e] = __builtin_fmaf(S[e], SM_C, negmc);
}
DI void pack_acc16(const f32x4 (&acc)[4][4], int qt, u32x4& w0, u32x4& w1) {
#pragma unroll
  for (int rg = 0; rg < 2; ++rg) {
    w0[2 * rg] = pk2(acc[0][qt][rg], acc[1][qt][rg]); w0[2 * rg + 1] = pk2(acc[2][qt][rg], acc[3][qt][rg]);
    w1[2 * rg] = pk2(acc[0][qt][2 + rg], acc[1][qt][2 + rg]); w1[2 * rg + 1] = pk2(acc[2][qt][2 + rg], acc[3][qt][2 + rg]);
  }
}

typedef __attribute__((address_space(1))) unsigned gu32;
typedef __attribute__((address_space(1))) unsigned long long gu64;
constexpr unsigned SPIN_CAP = 1u << 20;
struct K1Args { const u16 *x, *kc, *vc, *wo, *w2, *w3, *w4; const float* lnst; char* ws; u16* out; };

__global__ __launch_bounds__(512, 2) __attribute__((amdgpu_waves_per_eu(2, 2)))
void k1_attn(const K1Args p) {
  extern __shared__ __attribute__((aligned(16))) char lds[];
  char* __restrict__ ws = p.ws;
  const int tid = threadIdx.x, lane = tid & 63;
  const int wid = (int)rfl((unsigned)tid >> 6);
  const int g = blockIdx.x & 7, rbk = blockIdx.x >> 3;
  const int a = lane & 15, g4 = lane >> 4, r32 = lane & 31, hh = lane >> 5;
  const unsigned ldsb = (unsigned)(size_t)lds;
  const unsigned fo = lane * 16;
  RT(1, 0);
  STAMP(0);
  const rsrc_t rK = mk_rsrc(p.kc + ((size_t)g * SK + (SK - WIN)) * DH, WIN * DH * 2), rV = mk_rsrc(p.vc + ((size_t)g * SK + (SK - WIN)) * DH, WIN * DH * 2);
  const unsigned drow = wid * 8 + ((lane >> 2) & 7);
  const unsigned kvoff = drow * 256 + (lane >> 5) * 64 + (((lane & 3) ^ ((drow >> 2) & 3)) * 16);
  const unsigned vvoff = drow * 256 + (lane >> 5) * 64 + (lane & 3) * 16;
  const unsigned kdst = ldsb + L1_K + wid * 2048, vdst = ldsb + L1_V + wid * 2048;
  auto dmaK = [&](int t) AI { dma16(kdst + (t % 3) * 16384, kvoff, rK, t * 16384); dma16(kdst + (t % 3) * 16384 + 1024, kvoff, rK, t * 16384 + 128); };
  auto dmaV = [&](int t) AI { dma16(vdst + (t & 1) * 16384, vvoff, rV, t * 16384); dma16(vdst + (t & 1) * 16384 + 1024, vvoff, rV, t * 16384 + 128); };
  constexpr int DEPTH = 6;
  bf16x8 ring[DEPTH][4];
  const rsrc_t rWq = mk_rsrc(ws + WS_WQT + (size_t)(g * 8 + wid) * 65536, 65536);
  const int rot = ROT ? (rbk & 15) : 0;
  auto wq_load = [&](int s, int i) AI { return ldfrag(rWq, fo + i * 1024, ((s + rot) & 15) * 4096); };
  {
    const u32x4 sav = *(const u32x4*)(ws + WS_MOD + fo), cav = *(const u32x4*)(ws + WS_MOD + 1024 + fo);
    u32x4 xv[8];
#pragma unroll
    for (int i = 0; i < 8; ++i) xv[i] = *(const u32x4*)((const char*)p.x + (size_t)(rbk * 64 + wid * 8 + i) * 1024 + fo);
    float st[16];
#pragma unroll
    for (int i = 0; i < 16; ++i) st[i] = p.lnst[(rbk * 64 + wid * 8) * 2 + i];
    SCHED_FENCE();
    if (K0_WARM == 3) dma16(kdst, fo, wid < 4 ? rK : rV, (rbk * 4 + (wid & 3)) * 1024);
    SCHED_FENCE();
    float ca[8], sa[8];
#pragma unroll
    for (int e = 0; e < 4; ++e) { ca[2 * e] = bflo(cav[e]); ca[2 * e + 1] = bfhi(cav[e]); sa[2 * e] = bflo(sav[e]); sa[2 * e + 1] = bfhi(sav[e]); }
    if (PRIO_P && wid >= 4) __builtin_amdgcn_s_setprio(1);
#pragma unroll
    for (int hf = 0; hf < 2; ++hf) {
      float lo[16], hi[16], rr[4]; unsigned t[16];
#define H_STAGE(body) _Pragma("unroll") for (int j = 0; j < 16; ++j) { const int i = hf * 4 + (j >> 2), d = j & 3; (void)i; (void)d; body; } SCHED_FENCE()
#pragma unroll
      for (int i = 0; i < 4; ++i) rr[i] = __builtin_amdgcn_rcpf(st[2 * (hf * 4 + i) + 1]);
      H_STAGE(lo[j] = bflo(xv[i][d]); hi[j] = bfhi(xv[i][d]));
      H_STAGE(lo[j] -= st[2 * i]; hi[j] -= st[2 * i]);
      H_STAGE(lo[j] = rb(lo[j]); hi[j] = rb(hi[j]));
      if (PRIO_P && hf == 1) __builtin_amdgcn_s_setprio(0);
      H_STAGE(lo[j] *= rr[j >> 2]; hi[j] *= rr[j >> 2]);
      H_STAGE(lo[j] = rb(lo[j]); hi[j] = rb(hi[j]));
#pragma unroll
      for (int i = 0; i < 4; ++i) ring[hf][i] = wq_load(hf, i);
      H_STAGE(lo[j] *= ca[2 * d]; hi[j] *= ca[2 * d + 1]);
      H_STAGE(lo[j] = rb(lo[j]); hi[j] = rb(hi[j]));
      H_STAGE(lo[j] += sa[2 * d]; hi[j] += sa[2 * d + 1]);
      H_STAGE(t[j] = pk2(lo[j], hi[j]));
#pragma unroll
      for (int i = 0; i < 4; ++i) *(u32x4*)(lds + L1_HT + (wid * 8 + hf * 4 + i) * HT_STRIDE + fo) = (u32x4){t[4 * i], t[4 * i + 1], t[4 * i + 2], t[4 * i + 3]};
#pragma unroll
      for (int i = 0; i < 2; ++i) ring[2][hf * 2 + i] = wq_load(2, hf * 2 + i);
      SCHED_FENCE();
    }
  }

  Retile rtl; const u16* rsrc_w; int rld, rk0, rn0; bool rperm; size_t rdst; unsigned rhalf = 2048;
  if (wid < 4) { const int id = rbk * 4 + wid, s = id >> 3, ng = id & 7; rsrc_w = p.wo; rld = 512; rk0 = g * 512 + 32 * s; rn0 = 64 * ng; rperm = false; rdst = WS_WOT + (size_t)(g * 8 + ng) * 65536 + s * 2048; rhalf = 32768; }
  else {
    int id = blockIdx.x * 4 + wid - 4, s, ng; rperm = true;
    if (id < 128)      { ng = id >> 4; s = id & 15; rsrc_w = p.w2; rld = 512; rdst = WS_W2T + (size_t)id * 4096; }
    else if (id < 576) { id -= 128; ng = id >> 4; s = id & 15; rsrc_w = p.w3; rld = F_; rdst = WS_W3T + (size_t)id * 4096; }
    else               { id -= 576; ng = id & 7; s = id >> 3; rsrc_w = p.w4; rld = 512; rdst = WS_W4T + (size_t)(ng * 56 + s) * 4096; }
    rk0 = 64 * (s >> 1) + 2 * (s & 1); rn0 = 64 * ng;
  }
  gu32* flags = (gu32*)(ws + WS_FLAG);
  gu32* flag = flags + (FL_W + g) * 32;
  gu32* flagw = flags + (FL_W + (lane & 7)) * 32;
  STAMP(1);
  __syncthreads();
  STAMP(2);
  if (wid >= 4 && PRIO_Q > 0) __builtin_amdgcn_s_setprio(1);

  const char* bl = lds + L1_HT + a * HT_STRIDE + g4 * 16;
  auto bread = [&](int s, int qt) AI { return *(const bf16x8*)(bl + 64 * ((s + rot) & 15) + qt * 16 * HT_STRIDE); };
  f32x16 o[4]; bf16x8 qf[8]; float l0, l1, negmc;
  {
    f32x4 acc[1][4][4];
    gemm_steps<4, 4, 16, DEPTH, 1>(acc, ring, wq_load, bread, [&](auto S_, auto M_) AI {
      constexpr int s = S_.value, m = M_.value;
      if constexpr (s < 3 && (m & 3) == 1) ring[3 + s][m >> 2] = wq_load(3 + s, m >> 2);
      if constexpr (s == PRIO_Q && m == 0) __builtin_amdgcn_s_setprio(0);
      if constexpr (s == 11 && m == 8) { WAIT_VM0(); dmaK(0); }
      if constexpr (s == 12 && m == 0) dmaK(1);
      if constexpr (s == 12 && m == 8) dmaV(0);
      if constexpr (s == 13 && m == 0) dmaK(2);
      if constexpr (s == 13 && m == 8) dmaV(1);
    });
    STAMP(3);
    __syncthreads();
#pragma unroll
    for (int qt = 0; qt < 4; ++qt) {
      u32x4 w0, w1; pack_acc16(acc[0], qt, w0, w1);
      char* dst = lds + L1_HT + ((wid >> 1) * 64 + qt * 16 + a) * QX_STRIDE + ((wid & 1) * 64 + g4 * 16) * 2;
      *(u32x4*)dst = w0; *(u32x4*)(dst + 16) = w1;
    }
    asm volatile("s_waitcnt vmcnt(0)" ::: "memory");
    __syncthreads();
#pragma unroll
    for (int s = 0; s < 8; ++s) qf[s] = *(const bf16x8*)(lds + L1_HT + ((wid >> 1) * 64 + (wid & 1) * 32 + r32) * QX_STRIDE + 32 * s + 16 * hh);
  }
  STAMP(4);

  const unsigned kx = (r32 >> 2) & 3;
  const char* kb0 = lds + L1_K + 2048 * (r32 >> 3) + 64 * (r32 & 7) + 16 * ((0 + hh) ^ kx);
  const char* kb1 = lds + L1_K + 2048 * (r32 >> 3) + 64 * (r32 & 7) + 16 * ((2 + hh) ^ kx);
  const char* vb = lds + L1_V + (4 * hh + ((lane >> 2) & 3)) * 64 + ((lane >> 4) & 1) * 32 + (lane & 3) * 8;
  auto kfrag = [&](int t, int m) AI { return *(const bf16x8*)(((m & 1) ? kb1 : kb0) + (t % 3) * 16384 + 512 * ((m & 7) >> 1) + 8192 * (m >> 3)); };
  auto vfrag = [&](int t, int m) AI {
    const char* q = vb + (t & 1) * 16384 + (m >> 2) * 4096 + (m & 3) * 512;
    bf16x4 lo = __builtin_amdgcn_ds_read_tr16_b64_v4bf16(LDSP(bf16x4, q)), hi = __builtin_amdgcn_ds_read_tr16_b64_v4bf16(LDSP(bf16x4, q + 2048));
    return __builtin_shufflevector(lo, hi, 0, 1, 2, 3, 4, 5, 6, 7);
  };
  auto fixup = [&](const f32x16& S, float l0p, float l1p, u32x4& pa, u32x4& pb, bool pend, u32x4& pend0, u32x4& pend1) AI {
    const float mx = swap32_max(__builtin_fmaf(max16(S), SM_C, negmc));
    const float dm = fmaxf(__builtin_ceilf(mx), 0.f);
    const float f = __builtin_amdgcn_exp2f(-dm);
    negmc -= dm; l0 = l0p * f; l1 = l1p * f;
#pragma unroll
    for (int db = 0; db < 4; ++db) o[db] *= f;
    if (pend) {
#pragma unroll
      for (int q = 0; q < 4; ++q) { pend0[q] = scale_pk(pend0[q], f); pend1[q] = scale_pk(pend1[q], f); }
    }
    sm_half(S, negmc, l0, l1, pa, pb);
  };

  f32x16 Sa0, Sa1, Sb0, Sb1;
  u32x4 Pa0, Pa1, Pb0, Pb1, P2, P3;
  bf16x8 kf[4], vf[4];
  {
#pragma unroll
    for (int m = 0; m < 16; ++m) {
      const bf16x8 k = kfrag(0, m);
      if (m < 8) Sa0 = MFMA32(k, qf[m & 7], m == 0 ? (f32x16){} : Sa0); else Sa1 = MFMA32(k, qf[m & 7], m == 8 ? (f32x16){} : Sa1);
    }
  }
#pragma unroll
  for (int m = 0; m < 3; ++m) kf[m] = kfrag(1, m);
  negmc = -swap32_max(fmaxf(max16(Sa0), max16(Sa1))) * SM_C;
  l0 = 0.f; l1 = 0.f;
  sm_half(Sa0, negmc, l0, l1, Pa0, Pa1);
#pragma unroll
  for (int db = 0; db < 4; ++db) o[db] = (f32x16){};
  SCHED_FENCE();

  const rsrc_t rWo = mk_rsrc(ws + WS_WOT + (size_t)(g * 8 + wid) * 65536, 65536);
  const rsrc_t rRt = mk_rsrc(ws + rdst, 36864);
  bf16x8 ring2[12][2];
  auto wo_load = [&](int sg, int i) AI { return __builtin_bit_cast(bf16x8, __builtin_amdgcn_raw_buffer_load_b128(rWo, fo + i * 1024, (sg >> 4) * 32768 + ((sg + rot) & 15) * 2048, 16)); };
  unsigned flagv = 0;

  auto tile = [&](auto T_, const f32x16& C1, f32x16& N0, f32x16& N1, u32x4& Pc0, u32x4& Pc1, u32x4& Pn0, u32x4& Pn1) AI {
    constexpr int T = T_.value;
    if (T == 4) STAMP(12);
    if (PRIO_A && wid >= 4) __builtin_amdgcn_s_setprio(1);
    {
      const float l0p = l0, l1p = l1; SmPipe sp;
#pragma unroll
      for (int m = 0; m < 16; ++m) {
        if (T < 7) {
          if (m < 8) N0 = MFMA32(kf[m & 3], qf[m & 7], m == 0 ? (f32x16){} : N0); else N1 = MFMA32(kf[m & 3], qf[m & 7], m == 8 ? (f32x16){} : N1);
          if (m + 3 < 16) kf[(m + 3) & 3] = kfrag(T + 1, m + 3);
        }
        if (m >= 13) vf[m - 13] = vfrag(T, m - 13);
        sm_gap(m, sp, C1, negmc, l0, l1, P2, P3);
        if (T == 7) ring2[m >> 1][m & 1] = wo_load(m >> 1, m & 1);
        if (T < 7) SCHED_FENCE();
      }
      MEM_FENCE();
      if (__builtin_expect(__any(!(l0 + l1 <= SM_LIMIT)), 0)) fixup(C1, l0p, l1p, P2, P3, true, Pc0, Pc1);
    }
    if (T == 4) STAMP(13);
    if (PRIO_A == 1) __builtin_amdgcn_s_setprio(0);
    {
      const float l0p = l0, l1p = l1; SmPipe sp;
#pragma unroll
      for (int m = 0; m < 16; ++m) {
        const u32x4 pp = (m < 4) ? Pc0 : (m < 8) ? Pc1 : (m < 12) ? P2 : P3;
        o[m & 3] = MFMA32(vf[m & 3], __builtin_bit_cast(bf16x8, pp), o[m & 3]);
        if (m + 3 < 16) vf[(m + 3) & 3] = vfrag(T, m + 3);
        if (T < 7) sm_gap(m, sp, N0, negmc, l0, l1, Pn0, Pn1);
        if (T == 7 && m < 8) ring2[8 + (m >> 1)][m & 1] = wo_load(8 + (m >> 1), m & 1);
        if (PRIO_A == 2 && m == 4) __builtin_amdgcn_s_setprio(0);
        if (T < 7 && m == 13) {
          if (T == 6 && wid == 0) {
            for (unsigned spins = 0; !__all(flagv == 32u) && spins < SPIN_CAP; ++spins) { __builtin_amdgcn_s_sleep(2); flagv = __hip_atomic_load(flagw, __ATOMIC_RELAXED, __HIP_MEMORY_SCOPE_AGENT); }
          }
          if (T == 4) STAMP(14);
          if (T == 2) WAIT_VM0();
          if (T == 0)      asm volatile("s_waitcnt lgkmcnt(0)" : "+v"(vf[2]), "+v"(vf[3])::"memory");
          else if (T == 1) asm volatile("s_waitcnt vmcnt(8) lgkmcnt(0)" : "+v"(vf[2]), "+v"(vf[3])::"memory");
          else if (T == 3) asm volatile("s_waitcnt vmcnt(4) lgkmcnt(0)" : "+v"(vf[2]), "+v"(vf[3])::"memory");
          else             asm volatile("s_waitcnt vmcnt(0) lgkmcnt(0)" : "+v"(vf[2]), "+v"(vf[3])::"memory");
          __builtin_amdgcn_s_barrier();
          MEM_FENCE();
          if (T == 4) STAMP(15);
          if (T == 4 && tid == 0) __hip_atomic_fetch_add(flag, 1u, __ATOMIC_RELAXED, __HIP_MEMORY_SCOPE_AGENT);
          if (T == 5 && wid == 0) flagv = __hip_atomic_load(flagw, __ATOMIC_RELAXED, __HIP_MEMORY_SCOPE_AGENT);
        }
        if (T < 6 && m == 13) { kf[0] = kfrag(T + 2, 0); kf[1] = kfrag(T + 2, 1); }
        if (T < 6 && m == 14) kf[2] = kfrag(T + 2, 2);
        if (T < 5 && m == 14) dmaK(T + 3);
        if (T < 6 && m == 15) dmaV(T + 2);
        if (T == 0 && m == 15) { retile_load(rtl, rsrc_w, rld, rk0, rperm, rn0, lane); MEM_FENCE(); }
        if (T == 2 && m == 15) {
#pragma unroll
          for (int i = 0; i < 4; ++i) __builtin_amdgcn_raw_buffer_store_b128(retile_frag(rtl, i), rRt, fo + (i >> 1) * rhalf + (i & 1) * 1024, 0, 16);
        }
        SCHED_FENCE();
      }
      MEM_FENCE();
      if (T < 7) {
        if (__builtin_expect(__any(!(l0 + l1 <= SM_LIMIT)), 0)) fixup(N0, l0p, l1p, Pn0, Pn1, false, Pc0, Pc1);
      }
    }
  };
  STAMP(5);
  tile(IC<0>{}, Sa1, Sb0, Sb1, Pa0, Pa1, Pb0, Pb1);
  tile(IC<1>{}, Sb1, Sa0, Sa1, Pb0, Pb1, Pa0, Pa1);
  tile(IC<2>{}, Sa1, Sb0, Sb1, Pa0, Pa1, Pb0, Pb1);
  tile(IC<3>{}, Sb1, Sa0, Sa1, Pb0, Pb1, Pa0, Pa1);
  tile(IC<4>{}, Sa1, Sb0, Sb1, Pa0, Pa1, Pb0, Pb1);
  tile(IC<5>{}, Sb1, Sa0, Sa1, Pb0, Pb1, Pa0, Pa1);
  STAMP(6);
  tile(IC<6>{}, Sa1, Sb0, Sb1, Pa0, Pa1, Pb0, Pb1);
  tile(IC<7>{}, Sb1, Sa0, Sa1, Pb0, Pb1, Pa0, Pa1);
  STAMP(7);

  {
    const float inv = 1.f / swap32_add(l0 + l1);
    char* dst = lds + L1_HT + ((wid & 1) * 32 + r32) * HT_STRIDE + ((wid >> 1) * 128 + 4 * hh) * 2;
#pragma unroll
    for (int db = 0; db < 4; ++db)
#pragma unroll
      for (int gq = 0; gq < 4; ++gq) {
        u32x2 w = {pk2(o[db][4 * gq] * inv, o[db][4 * gq + 1] * inv), pk2(o[db][4 * gq + 2] * inv, o[db][4 * gq + 3] * inv)};
        *(u32x2*)(dst + (32 * db + 8 * gq) * 2) = w;
      }
  }
  __syncthreads();
  STAMP(8);
  if (wid >= 4 && PRIO_O > 0) __builtin_amdgcn_s_setprio(1);
  {
    f32x4 acc[2][2][4];
    const rsrc_t rP = mk_rsrc(ws + WS_PART + (size_t)((rbk * 8 + g) * 8 + wid) * 8192, 8192);
    auto pstore = [&](int ps, int qt) AI {
      u32x4 w;
#pragma unroll
      for (int r = 0; r < 4; ++r) w[r] = pk2(acc[ps][0][qt][r], acc[ps][1][qt][r]);
      __builtin_amdgcn_raw_buffer_store_b128(w, rP, fo + (qt * 2 + ps) * 1024, 0, PART_AUX);
    };
    gemm_steps<2, 4, 32, 12, 2, 2>(acc, ring2, wo_load, bread, [&](auto S_, auto M_) AI {
      if constexpr (S_.value == PRIO_O && M_.value == 0) __builtin_amdgcn_s_setprio(0);
      if constexpr (S_.value >= 20 && S_.value < 22 && (M_.value & 3) == 1) pstore(0, (S_.value - 20) * 2 + (M_.value >> 2));
      if constexpr (S_.value == P0_STEP && M_.value == 7) {
        WAIT_VM0(); asm volatile("s_waitcnt vmcnt(0)" ::: "memory"); __builtin_amdgcn_s_barrier(); MEM_FENCE();
        if (tid == 0) __hip_atomic_fetch_add(flags + (FL_P0 + rbk) * 32, 1u, __ATOMIC_RELAXED, __HIP_MEMORY_SCOPE_AGENT);
      } });
    STAMP(9);
#pragma unroll
    for (int qt = 0; qt < 4; ++qt) pstore(1, qt);
  }
  STAMP(10);

  const rsrc_t rWs = mk_rsrc(ws, (unsigned)WS_END);
  auto ld1 = [&](size_t off, unsigned soff) AI { return __builtin_amdgcn_raw_buffer_load_b128(rWs, (unsigned)off, soff, 16); };
  auto ldf1 = [&](size_t off, unsigned soff) AI { return __builtin_bit_cast(bf16x8, __builtin_amdgcn_raw_buffer_load_b128(rWs, (unsigned)off, soff, 16)); };
  auto st1 = [&](size_t off, u32x4 v) AI { __builtin_amdgcn_raw_buffer_store_b128(v, rWs, (unsigned)off, 0, 16); };
  auto publish = [&](int idx) AI {
    asm volatile("s_waitcnt vmcnt(0) lgkmcnt(0)" ::: "memory"); __builtin_amdgcn_s_barrier(); MEM_FENCE();
    if (tid == 0) __hip_atomic_fetch_add(flags + idx * 32, 1u, __ATOMIC_RELAXED, __HIP_MEMORY_SCOPE_AGENT);
  };
  auto await = [&](int pw, int idx, unsigned want) AI {
    if (wid == pw) {
      unsigned spins = 0;
      while (!__all(__hip_atomic_load(flags + idx * 32, __ATOMIC_RELAXED, __HIP_MEMORY_SCOPE_AGENT) >= want) && ++spins < SPIN_CAP) __builtin_amdgcn_s_sleep(1);
    }
    MEM_FENCE(); __builtin_amdgcn_s_barrier(); MEM_FENCE();
  };
  const int b = blockIdx.x;
  const u16* modb = (const u16*)(ws + WS_MOD);
  publish(FL_P + rbk);
  STAMP(11);
  RT(1, 1);

  RT(2, 0);
  STAMPK(2, 0);
  {
    f32x2* red = (f32x2*)(lds + 16 * HT_STRIDE);
    const int rt = (b & 7) * 16 + (b >> 4), nh = (b >> 3) & 1;
    const int prb = rt >> 2, qt = rt & 3, row = rt * 16 + a, col = wid * 64 + g4 * 16;
    constexpr int D1 = 12, D1E = 12;
    bf16x8 A[D1][2];
    const int G = nh * 4 + (wid >> 1), ps = wid & 1;
    const rsrc_t rW = mk_rsrc(ws + WS_W1T + (size_t)G * 65536 + ps * 2048, 65536);
    auto aload = [&](int s, int i) AI { return ldfrag(rW, fo, (s * 4 + i) * 1024); };
    u32x4 pv[8][2];
    const size_t poff = WS_PART + (size_t)((prb * 64 + wid) * 8 + qt * 2) * 1024 + fo;
    const int prot = 0;
    await(0, FL_P0 + prb, 8);
    if (wid != 7) {
#pragma unroll
      for (int gg = 0; gg < 8; ++gg) pv[gg][0] = ld1(poff, (((gg + prot) & 7) * 64) * 1024);
    }
    SCHED_FENCE();
    await(7, FL_P + prb, 8);
    STAMPK(2, 1);
#pragma unroll
    for (int gg = 0; gg < 8; ++gg) pv[gg][1] = ld1(poff, (((gg + prot) & 7) * 64 + 1) * 1024);
    if (wid == 7) {
#pragma unroll
      for (int gg = 0; gg < 8; ++gg) pv[gg][0] = ld1(poff, (((gg + prot) & 7) * 64) * 1024);
    }
    u32x4 xv[2], gav[2], cbv[2], sbv[2];
#pragma unroll
    for (int h = 0; h < 2; ++h) {
      xv[h] = *(const u32x4*)(p.x + (size_t)row * D_ + col + 8 * h);
      gav[h] = *(const u32x4*)(modb + 1024 + col + 8 * h); sbv[h] = *(const u32x4*)(modb + 1536 + col + 8 * h); cbv[h] = *(const u32x4*)(modb + 2048 + col + 8 * h);
    }
    SCHED_FENCE();
    __builtin_amdgcn_s_barrier();
#pragma unroll
    for (int s = 0; s < D1E; ++s) { A[s][0] = aload(s, 0); A[s][1] = aload(s, 1); }
    SCHED_FENCE();
    float v[16];
#pragma unroll
    for (int d = 0; d < 8; ++d) { v[2 * d] = bflo(pv[0][d & 1][d >> 1]); v[2 * d + 1] = bfhi(pv[0][d & 1][d >> 1]); }
#pragma unroll
    for (int gg = 1; gg < 8; ++gg) {
#pragma unroll
      for (int d = 0; d < 8; ++d) { v[2 * d] += bflo(pv[gg][d & 1][d >> 1]); v[2 * d + 1] += bfhi(pv[gg][d & 1][d >> 1]); }
      SCHED_FENCE();
    }
#pragma unroll
    for (int d = 0; d < 8; ++d) {
      const unsigned ga = gav[d >> 2][d & 3], xx = xv[d >> 2][d & 3];
      const unsigned x2 = pk2(__builtin_fmaf(bflo(ga), v[2 * d], bflo(xx)), __builtin_fmaf(bfhi(ga), v[2 * d + 1], bfhi(xx)));
      xv[d >> 2][d & 3] = x2; v[2 * d] = bflo(x2); v[2 * d + 1] = bfhi(x2);
    }
    if (nh == 0) { st1(WS_X2 + (size_t)row * 1024 + col * 2, xv[0]); st1(WS_X2 + (size_t)row * 1024 + col * 2 + 16, xv[1]); }
    STAMPK(2, 2);
    auto row_stats = [&](f32x2* rd, float& mean, float& rstd) AI {
      float s0 = 0, s1 = 0, q0 = 0, q1 = 0;
#pragma unroll
      for (int e = 0; e < 8; ++e) { s0 += v[2 * e]; s1 += v[2 * e + 1]; q0 = __builtin_fmaf(v[2 * e], v[2 * e], q0); q1 = __builtin_fmaf(v[2 * e + 1], v[2 * e + 1], q1); }
      float s = swap32_add(swap16_add(s0 + s1)), q = swap32_add(swap16_add(q0 + q1));
      if (g4 == 0) rd[wid * 16 + a] = (f32x2){s, q};
      asm volatile("s_waitcnt lgkmcnt(0)" ::: "memory"); __builtin_amdgcn_s_barrier(); MEM_FENCE();
      f32x2 t = rd[a];
#pragma unroll
      for (int w = 1; w < 8; ++w) t += rd[w * 16 + a];
      mean = t[0] * (1.f / 512.f);
      rstd = __builtin_amdgcn_rsqf(fmaxf(t[1] * (1.f / 512.f) - mean * mean, 0.f) + 1e-5f);
    };
    float mean, rstd;
#pragma unroll
    for (int s = D1E; s < D1; ++s) { A[s][0] = aload(s, 0); A[s][1] = aload(s, 1); }
    SCHED_FENCE();
    row_stats(red, mean, rstd);
    u32x4 hv[2];
#pragma unroll
    for (int d = 0; d < 8; ++d) {
      const unsigned cb = cbv[d >> 2][d & 3], sb = sbv[d >> 2][d & 3];
      const unsigned hb = pk2((v[2 * d] - mean) * rstd * bflo(cb) + bflo(sb), (v[2 * d + 1] - mean) * rstd * bfhi(cb) + bfhi(sb));
      hv[d & 1][d >> 1] = hb; v[2 * d] = bflo(hb); v[2 * d + 1] = bfhi(hb);
    }
    if (nh == 1) { st1(WS_HB + (size_t)row * 1024 + wid * 128 + g4 * 16, hv[0]); st1(WS_HB + (size_t)row * 1024 + wid * 128 + 64 + g4 * 16, hv[1]); }
    row_stats(red + 128, mean, rstd);
    const float nm = -mean * rstd;
#pragma unroll
    for (int h = 0; h < 2; ++h) {
      u32x4 o;
#pragma unroll
      for (int e = 0; e < 4; ++e) o[e] = pk2(__builtin_fmaf(v[8 * h + 2 * e], rstd, nm), __builtin_fmaf(v[8 * h + 2 * e + 1], rstd, nm));
      *(u32x4*)(lds + a * HT_STRIDE + (col + 8 * h) * 2) = o;
    }
    STAMPK(2, 3);
    asm volatile("s_waitcnt lgkmcnt(0)" ::: "memory"); __builtin_amdgcn_s_barrier(); MEM_FENCE();
    STAMPK(2, 4);
    f32x4 acc[1][2][1];
    gemm_steps<2, 1, 16, D1, 1, 4>(acc, A, aload, [&](int s, int) AI { return *(const bf16x8*)(bl + 64 * s); }, [](auto, auto) AI {});
    STAMPK(2, 5);
    u32x4 ov;
#pragma unroll
    for (int rg = 0; rg < 4; ++rg) ov[rg] = pk2(gelu_tanh(acc[0][0][0][rg]), gelu_tanh(acc[0][1][0][rg]));
    st1(WS_H1 + (size_t)(rt * 16 + 2 * G + ps) * 1024 + fo, ov);
    STAMPK(2, 6);
    publish(FL_H1 + rt);
    RT(2, 1);
    STAMPK(2, 7);
  }

  const int rb2 = (b & 7) * 4 + (b >> 6), ns = (b >> 3) & 7;
  const int ng = ns * 7 + (wid < 7 ? wid : 6);
  const size_t w3off = WS_W3T + (size_t)(ng >> 1) * 65536 + (ng & 1) * 2048 + fo;
  bf16x8 A[16][2];
  RT(3, 0);
  STAMPK(3, 0);
  {
    const int rt2 = (b & 7) * 8 + (b >> 5), nq = (b >> 3) & 3;
    const int G = nq * 2 + ((wid & 3) >> 1), ps = wid & 1;
    if (wid < 4) {
      const size_t aoff = WS_W2T + (size_t)G * 65536 + ps * 2048 + fo;
#pragma unroll
      for (int s = 0; s < 16; ++s) { A[s][0] = ldf1(aoff, (s * 4) * 1024); A[s][1] = ldf1(aoff, (s * 4 + 1) * 1024); }
    }
    SCHED_FENCE();
    { const int t = 2 * rt2 - 1 + (lane & 3); await(7, FL_H1 + (t < 0 ? 0 : t > 127 ? 127 : t), 2); }
    STAMPK(3, 1);
    const rsrc_t rH = mk_rsrc(ws + WS_H1, (unsigned)NXB);
    const int sh = (wid < 2) ? -1 : (wid < 4) ? 1 : (wid < 6) ? -2 : 2;
    u32x4 hf[2][2], hbv[2];
#pragma unroll
    for (int qt = 0; qt < 2; ++qt) {
      const int r = rt2 * 32 + qt * 16 + a + sh;
      const unsigned ho = (unsigned)((r >> 4) * 16384 + ((r & 15) + 16 * g4) * 16);
      hf[qt][0] = __builtin_amdgcn_raw_buffer_load_b128(rH, ho, (wid * 2) * 1024, 16); hf[qt][1] = __builtin_amdgcn_raw_buffer_load_b128(rH, ho, (wid * 2 + 1) * 1024, 16);
    }
    const size_t xoff = (size_t)(rt2 * 32 + a) * 1024 + G * 128 + ps * 64 + g4 * 16;
    if (wid < 4) { hbv[0] = ld1(WS_HB + xoff, 0); hbv[1] = ld1(WS_HB + xoff, 16384); }
#pragma unroll
    for (int qt = 0; qt < 2; ++qt) { *(u32x4*)(lds + (qt * 16 + wid * 2) * 1024 + fo) = hf[qt][0]; *(u32x4*)(lds + (qt * 16 + wid * 2 + 1) * 1024 + fo) = hf[qt][1]; }
    asm volatile("s_waitcnt lgkmcnt(0)" ::: "memory"); __builtin_amdgcn_s_barrier(); MEM_FENCE();
    STAMPK(3, 2);
    f32x2* red = (f32x2*)(lds + 32768);
    if (wid < 4) {
      f32x4 acc[1][2][2];
      gemm_steps<2, 2, 16, 16, 1, 3>(acc, A, [&](int, int) AI { return bf16x8{}; }, [&](int s, int qt) AI { return *(const bf16x8*)(lds + (qt * 16 + s) * 1024 + fo); }, [](auto, auto) AI {});
#pragma unroll
      for (int qt = 0; qt < 2; ++qt) {
        u32x4 ov; float s = 0, q = 0;
#pragma unroll
        for (int rg = 0; rg < 4; ++rg) {
          ov[rg] = pk2(bflo(hbv[qt][rg]) + acc[0][0][qt][rg], bfhi(hbv[qt][rg]) + acc[0][1][qt][rg]);
          const float lo = bflo(ov[rg]), hi = bfhi(ov[rg]); s += lo + hi; q += lo * lo + hi * hi;
        }
        st1(WS_X1 + xoff + qt * 16384, ov);
        s = swap32_add(swap16_add(s)); q = swap32_add(swap16_add(q));
        if (g4 == 0) red[wid * 32 + qt * 16 + a] = (f32x2){s, q};
      }
    } else if (wid < 7) {
#pragma unroll
      for (int s = 0; s < 16; ++s) { A[s][0] = ldf1(w3off, (s * 4) * 1024); A[s][1] = ldf1(w3off, (s * 4 + 1) * 1024); }
    }
    asm volatile("s_waitcnt lgkmcnt(0)" ::: "memory"); __builtin_amdgcn_s_barrier(); MEM_FENCE();
    if (tid < 32) {
      const f32x2 t = red[tid] + red[32 + tid] + red[64 + tid] + red[96 + tid];
      __hip_atomic_store((gu64*)(ws + WS_ST + (size_t)(rt2 * 32 + tid) * 32 + nq * 8), __builtin_bit_cast(unsigned long long, t), __ATOMIC_RELAXED, __HIP_MEMORY_SCOPE_AGENT);
    }
    STAMPK(3, 3);
    publish(FL_X1 + (rt2 >> 1));
    RT(3, 1);
    STAMPK(3, 4);
  }

  bf16x8 W[7][4];
  const size_t woff = WS_W4T + (size_t)(ns * 56 + wid * 7) * 4096 + fo;
  auto rot7 = [&](int s) AI { const int t = s + (ROT ? (ns < 7 ? ns : 0) : 0); return t >= 7 ? t - 7 : t; };
  RT(4, 0);
  STAMPK(4, 0);
  {
    if (wid < 4) {
#pragma unroll
      for (int s = 0; s < 16; ++s) { A[s][0] = ldf1(w3off, (s * 4) * 1024); A[s][1] = ldf1(w3off, (s * 4 + 1) * 1024); }
    }
    SCHED_FENCE();
    await(7, FL_X1 + rb2, 8);
    STAMPK(4, 1);
    u32x4 xv[8];
    const u32x4 stv = ld1(WS_ST + (size_t)(rb2 * 64 + wid * 8) * 32 + (lane & 15) * 16, 0);
#pragma unroll
    for (int i = 0; i < 8; ++i) xv[i] = ld1(WS_X1 + (size_t)(rb2 * 64 + wid * 8) * 1024 + fo, i * 1024);
    SCHED_FENCE();
    const float sm = dpp_add<0xB1>(__uint_as_float(stv[0]) + __uint_as_float(stv[2])) * (1.f / 512.f);
    const float sq = dpp_add<0xB1>(__uint_as_float(stv[1]) + __uint_as_float(stv[3])) * (1.f / 512.f);
    const float rsl = __builtin_amdgcn_rsqf(fmaxf(sq - sm * sm, 0.f) + 1e-5f), nml = -sm * rsl;
    float rs[8], nm[8];
#pragma unroll
    for (int i = 0; i < 8; ++i) { rs[i] = __uint_as_float(__builtin_amdgcn_readlane(__float_as_uint(rsl), 2 * i)); nm[i] = __uint_as_float(__builtin_amdgcn_readlane(__float_as_uint(nml), 2 * i)); }
    SCHED_FENCE();
#pragma unroll
    for (int hf = 0; hf < 2; ++hf) {
      float lo[16], hi[16]; unsigned t[16];
      H_STAGE(lo[j] = bflo(xv[i][d]); hi[j] = bfhi(xv[i][d]));
      H_STAGE(lo[j] = __builtin_fmaf(lo[j], rs[i], nm[i]); hi[j] = __builtin_fmaf(hi[j], rs[i], nm[i]));
      H_STAGE(t[j] = pk2(lo[j], hi[j]));
#pragma unroll
      for (int i = 0; i < 4; ++i) *(u32x4*)(lds + (wid * 8 + hf * 4 + i) * HT_STRIDE + fo) = (u32x4){t[4 * i], t[4 * i + 1], t[4 * i + 2], t[4 * i + 3]};
      SCHED_FENCE();
    }
    STAMPK(4, 2);
    asm volatile("s_waitcnt lgkmcnt(0)" ::: "memory"); __builtin_amdgcn_s_barrier(); MEM_FENCE();
    STAMPK(4, 3);
    if (wid < 7) {
      f32x4 acc[2][2][2];
      if (wid >= 4) __builtin_amdgcn_s_setprio(1);
      const size_t dst = WS_U + (size_t)(rb2 * 4 * 56 + ng) * 1024 + fo;
      u32x4 ow;
      auto gpart = [&](int ps, int k) AI {
        unsigned w = pk2(gelu_tanh(acc[ps][0][k >> 2][k & 3]), gelu_tanh(acc[ps][1][k >> 2][k & 3])); PIN(w); ow[k & 3] = w;
        if ((k & 3) == 3) st1(dst + (size_t)(2 * ps + (k >> 2)) * 56 * 1024, ow);
      };
      gemm_steps<2, 2, 32, 16, 2, 3>(acc, A, [&](int s, int i) AI { return A[s & 15][i]; },
                                     [&](int s, int qt) AI { return *(const bf16x8*)(bl + ((s >> 4) * 2 + qt) * 16 * HT_STRIDE + 64 * (s & 15)); },
                                     [&](auto S_, auto M_) AI {
                                       constexpr int s = S_.value, m = M_.value;
                                       if constexpr (s == PRIO_G3 && m == 0) __builtin_amdgcn_s_setprio(0);
                                       if constexpr (s >= 17 && (s & 1) && m == 1) gpart(0, (s - 17) >> 1);
                                       if constexpr (s >= 17 && s < 31 && m >= 2) { constexpr int k = (s - 17) * 2 + m - 2; W[k >> 2][k & 3] = ldf1(woff + (k & 3) * 1024, rot7(k >> 2) * 4096); }
                                     });
      STAMPK(4, 4);
#pragma unroll
      for (int k = 0; k < 8; ++k) gpart(1, k);
    } else {
#pragma unroll
      for (int k = 0; k < 28; ++k) W[k >> 2][k & 3] = ldf1(woff + (k & 3) * 1024, rot7(k >> 2) * 4096);
    }
    STAMPK(4, 5);
    publish(FL_U + rb2);
    RT(4, 1);
    STAMPK(4, 6);
  }

  RT(5, 0);
  STAMPK(5, 0);
  {
    bf16x8 B[7][4];
    const size_t uoff = WS_U + (size_t)(rb2 * 4 * 56 + wid * 7) * 1024 + fo;
    const int oqt = wid >> 1, orp = wid & 1, orow = rb2 * 64 + oqt * 16 + a, ocol = ns * 64 + g4 * 16 + orp * 8;
    u32x2 x1a, x1b; u32x4 x2v, gbv;
    SCHED_FENCE();
    await(0, FL_U + rb2, 8);
#pragma unroll
    for (int s = 0; s < G4_AHEAD; ++s)
#pragma unroll
      for (int i = 0; i < 4; ++i) B[s][i] = ldf1(uoff, (i * 56 + rot7(s)) * 1024);
    SCHED_FENCE();
    STAMPK(5, 1);
    f32x4 acc[4][4] = {};
#pragma unroll
    for (int s = 0; s < 7; ++s) {
#pragma unroll
      for (int i = 0; i < 4; ++i) {
#pragma unroll
        for (int qt = 0; qt < 4; ++qt) acc[i][qt] = MFMA16(W[s][i], B[s][qt], acc[i][qt]);
        if (s + G4_AHEAD < 7) B[s + G4_AHEAD][i] = ldf1(uoff, (i * 56 + rot7(s + G4_AHEAD)) * 1024);
        SCHED_FENCE();
      }
      if (s == 5) {
        const size_t x1o = WS_X1 + (size_t)orow * 1024 + ns * 128 + g4 * 16 + orp * 8;
        x1a = __builtin_amdgcn_raw_buffer_load_b64(rWs, (unsigned)x1o, 0, 16); x1b = __builtin_amdgcn_raw_buffer_load_b64(rWs, (unsigned)x1o, 64, 16);
        x2v = ld1(WS_X2 + (size_t)orow * 1024 + ocol * 2, 0); gbv = *(const u32x4*)(modb + 2560 + ocol);
        SCHED_FENCE();
      }
    }
    STAMPK(5, 2);
#pragma unroll
    for (int qt = 0; qt < 4; ++qt)
#pragma unroll
      for (int rg = 0; rg < 4; ++rg)
        *(f32x4*)(lds + ((wid * 4 + qt) * 4 + rg) * 1024 + fo) = (f32x4){acc[0][qt][rg], acc[1][qt][rg], acc[2][qt][rg], acc[3][qt][rg]};
    STAMPK(5, 3);
    __syncthreads();
    STAMPK(5, 4);
    f32x4 s0 = {}, s1 = {};
#pragma unroll
    for (int w = 0; w < 8; ++w) {
      s0 += *(const f32x4*)(lds + ((w * 4 + oqt) * 4 + 2 * orp) * 1024 + fo);
      s1 += *(const f32x4*)(lds + ((w * 4 + oqt) * 4 + 2 * orp + 1) * 1024 + fo);
    }
    const float h2[8] = {s0[0], s0[1], s0[2], s0[3], s1[0], s1[1], s1[2], s1[3]};
    const unsigned x1v[4] = {x1a[0], x1b[0], x1a[1], x1b[1]};
    u32x4 ov;
#pragma unroll
    for (int e = 0; e < 4; ++e)
      ov[e] = pk2(bflo(x2v[e]) + bflo(gbv[e]) * (bflo(x1v[e]) + h2[2 * e]), bfhi(x2v[e]) + bfhi(gbv[e]) * (bfhi(x1v[e]) + h2[2 * e + 1]));
    *(u32x4*)(p.out + (size_t)orow * D_ + ocol) = ov;
    STAMPK(5, 5);
    RT(5, 1);
    STAMPK(5, 6);
  }
}

extern "C" void kernel_launch(void* const* d_in, const int* in_sizes, int n_in, void* d_out, int out_size, void* d_ws, size_t ws_size, hipStream_t stream) {
  char* ws = (char*)d_ws;
  K0Args p0;
  p0.x = (const u16*)d_in[0]; p0.c = (const u16*)d_in[10]; p0.wmod = (const u16*)d_in[11]; p0.wq = (const u16*)d_in[3]; p0.w1 = (const u16*)d_in[6]; p0.kc = (const u16*)d_in[1]; p0.vc = (const u16*)d_in[2]; p0.ws = ws;
  K1Args p1;
  p1.x = p0.x; p1.kc = (const u16*)d_in[1]; p1.vc = (const u16*)d_in[2]; p1.wo = (const u16*)d_in[4];
  p1.w2 = (const u16*)d_in[7]; p1.w3 = (const u16*)d_in[8]; p1.w4 = (const u16*)d_in[9]; p1.lnst = (const float*)(ws + WS_Y); p1.ws = ws; p1.out = (u16*)d_out;
  (void)hipFuncSetAttribute((const void*)k1_attn, hipFuncAttributeMaxDynamicSharedMemorySize, L1_END);
  k0_prep<<<K0_BLOCKS, 512, 0, stream>>>(p0);
  k1_attn<<<256, 512, L1_END, stream>>>(p1);
}
```

```cpp
#include <hip/hip_runtime.h>
#include <hip/hip_bf16.h>
#include <type_traits>
#include <utility>

typedef unsigned short u16;
typedef __bf16 bf16x8 __attribute__((ext_vector_type(8)));
typedef __bf16 bf16x4 __attribute__((ext_vector_type(4)));
typedef __bf16 bf16x2 __attribute__((ext_vector_type(2)));
typedef float f32x16 __attribute__((ext_vector_type(16)));
typedef float f32x4 __attribute__((ext_vector_type(4)));
typedef float f32x2 __attribute__((ext_vector_type(2)));
typedef unsigned u32x2 __attribute__((ext_vector_type(2)));
typedef unsigned u32x4 __attribute__((ext_vector_type(4)));
typedef __amdgpu_buffer_rsrc_t rsrc_t;
#define DI __device__ __forceinline__
#define AI __attribute__((always_inline))
#define LDSP(T, p) ((__attribute__((address_space(3))) T*)(p))
#define SCHED_FENCE() __builtin_amdgcn_sched_barrier(0)
#define PIN(x) asm volatile("" : "+v"(x))
#define MEM_FENCE() asm volatile("" ::: "memory")
#define WAIT_VM0() __builtin_amdgcn_s_waitcnt(0x0F70)
#define STAMP(i)
#define STAMPK(k, i)
#define RT(k, i)

#define NT_SRC 1
#define K0_WARM 3
constexpr int S_ = 2048, D_ = 512, HQ = 32, HKV = 8, DH = 128, SK = 2048, WIN = 512, F_ = 1792;

constexpr size_t NXB = (size_t)S_ * D_ * 2;
constexpr size_t WS_MOD = 0;
constexpr size_t WS_Y   = 8192;
constexpr size_t WS_X2  = WS_Y  + NXB;
constexpr size_t WS_HB  = WS_X2 + NXB;
constexpr size_t WS_H1  = WS_HB + NXB;
constexpr size_t WS_X1  = WS_H1 + NXB;
constexpr size_t WS_U   = WS_X1 + NXB;
constexpr size_t WS_PART = WS_U + (size_t)S_ * F_ * 2;
constexpr size_t WS_WQT = WS_PART + 8 * NXB;
constexpr size_t WS_WOT = WS_WQT + (size_t)D_ * 4096 * 2;
constexpr size_t WS_W1T = WS_WOT + (size_t)D_ * 4096 * 2;
constexpr size_t WS_W2T = WS_W1T + (size_t)D_ * D_ * 2;
constexpr size_t WS_W3T = WS_W2T + (size_t)D_ * D_ * 2;
constexpr size_t WS_W4T = WS_W3T + (size_t)D_ * F_ * 2;
constexpr size_t WS_ST  = WS_W4T + (size_t)D_ * F_ * 2;
constexpr size_t WS_FLAG = WS_ST + (size_t)S_ * 32;
constexpr int FL_W = 0, FL_P = 8, FL_H1 = 40, FL_X1 = 168, FL_U = 200, FL_P0 = 232, FL_N = 264;
constexpr size_t WS_END = WS_FLAG + FL_N * 128;

DI float bflo(unsigned u) { return __uint_as_float(u << 16); }
DI float bfhi(unsigned u) { return __uint_as_float(u & 0xffff0000u); }
DI unsigned pk2(float lo, float hi) { f32x2 v = {lo, hi}; return __builtin_bit_cast(unsigned, __builtin_convertvector(v, bf16x2)); }
DI float rb(float x) { return __uint_as_float(pk2(0.f, x)); }
DI rsrc_t mk_rsrc(const void* p, unsigned bytes) { return __builtin_amdgcn_make_buffer_rsrc((void*)p, (short)0, (int)bytes, 0x00020000); }
DI unsigned rfl(unsigned x) { return __builtin_amdgcn_readfirstlane(x); }
DI bf16x8 ldfrag(rsrc_t r, unsigned voff, unsigned soff) { return __builtin_bit_cast(bf16x8, __builtin_amdgcn_raw_buffer_load_b128(r, voff, soff, 0)); }
template <int CTRL> DI float dpp_add(float v) {
  return v + __uint_as_float(__builtin_amdgcn_update_dpp(0u, __float_as_uint(v), CTRL, 0xf, 0xf, true));
}
DI float swap16_add(float v) { auto r = __builtin_amdgcn_permlane16_swap(__float_as_uint(v), __float_as_uint(v), false, false); return __uint_as_float(r[0]) + __uint_as_float(r[1]); }
DI float swap32_add(float v) { auto r = __builtin_amdgcn_permlane32_swap(__float_as_uint(v), __float_as_uint(v), false, false); return __uint_as_float(r[0]) + __uint_as_float(r[1]); }
DI float swap32_max(float v) { auto r = __builtin_amdgcn_permlane32_swap(__float_as_uint(v), __float_as_uint(v), false, false); return fmaxf(__uint_as_float(r[0]), __uint_as_float(r[1])); }
DI float wave_sum(float v) {
  v = dpp_add<0xB1>(v); v = dpp_add<0x4E>(v); v = dpp_add<0x141>(v); v = dpp_add<0x140>(v);
  v = swap16_add(v); v = swap32_add(v); return v;
}
DI float div_rn(float a, float d, float r) {
  float q = a * r; float e = __builtin_fmaf(-q, d, a); return __builtin_fmaf(e, r, q);
}
DI float gelu_tanh(float x) {
  const float u = 0.7978845608028654f * (x + 0.044715f * x * x * x);
  return x * __builtin_amdgcn_rcpf(1.f + __builtin_amdgcn_exp2f(-2.885390081777927f * u));
}
DI void ln_consts(float mean, float var, float& m_bf, float& d_bf, float& r) {
  m_bf = rb(mean); d_bf = rb(__builtin_sqrtf(rb(rb(var) + 1e-5f))); r = __builtin_amdgcn_rcpf(d_bf);
}
DI float ln_apply(float x, float m_bf, float d_bf, float r) { return rb(div_rn(rb(x - m_bf), d_bf, r)); }
DI u32x4 ln_row(const u32x4 xv) {
  float f[8];
#pragma unroll
  for (int e = 0; e < 4; ++e) { f[2 * e] = bflo(xv[e]); f[2 * e + 1] = bfhi(xv[e]); }
  float s = 0;
#pragma unroll
  for (int e = 0; e < 8; ++e) s += f[e];
  const float mean = wave_sum(s) * (1.f / 512.f);
  float q = 0;
#pragma unroll
  for (int e = 0; e < 8; ++e) { float d = f[e] - mean; q += d * d; }
  const float var = wave_sum(q) * (1.f / 512.f);
  float mb, db, r; ln_consts(mean, var, mb, db, r);
  u32x4 o;
#pragma unroll
  for (int e = 0; e < 4; ++e) o[e] = pk2(div_rn(rb(f[2 * e] - mb), db, r), div_rn(rb(f[2 * e + 1] - mb), db, r));
  return o;
}

#define MFMA16(a, b, c) __builtin_amdgcn_mfma_f32_16x16x32_bf16((a), (b), (c), 0, 0, 0)
#define MFMA32(a, b, c) __builtin_amdgcn_mfma_f32_32x32x16_bf16((a), (b), (c), 0, 0, 0)

DI void dma16(unsigned lds_dst, unsigned voff, rsrc_t r, unsigned soff) {
  asm volatile("s_mov_b32 m0, %0\n\ts_nop 0\n\tbuffer_load_dwordx4 %1, %2, %3 offen lds" ::"s"(lds_dst), "v"(voff), "s"(r), "s"(soff) : "memory");
}

struct Retile { u32x2 d[8]; };
DI void retile_load(Retile& t, const u16* __restrict__ W, int ldw, int k0, bool perm, int n0, int lane) {
  const int a = lane & 15, g4 = lane >> 4;
  const u16* p = W + (size_t)(k0 + (perm ? 16 : 8) * g4) * ldw + n0 + 4 * a;
  const int s2 = (perm ? 4 : 2) * ldw;
#pragma unroll
  for (int j = 0; j < 8; ++j) {
    const u32x2* q = (const u32x2*)(p + (j >> 1) * s2 + (j & 1) * ldw);
    t.d[j] = NT_SRC ? __builtin_nontemporal_load(q) : *q;
  }
}
DI u32x4 retile_frag(const Retile& t, int i) {
  u32x4 f;
#pragma unroll
  for (int p = 0; p < 4; ++p) f[p] = __builtin_amdgcn_perm(t.d[2 * p + 1][i >> 1], t.d[2 * p][i >> 1], (i & 1) ? 0x07060302u : 0x05040100u);
  return f;
}

struct K0Args { const u16 *x, *c, *wmod, *w1, *kc, *vc; char* ws; };
constexpr int K0_BLOCKS = 304 + 16;
__global__ __launch_bounds__(512) void k0_prep(const K0Args p) {
  __shared__ float red[8][64];
  const int tid = threadIdx.x, lane = tid & 63, wid = tid >> 6;
  int b = blockIdx.x;
#define WSP p.ws
  RT(0, 0);
  if (b < 48) {
    const int n0 = b * 64, cg = tid & 7, kq = tid >> 3;
    u32x4 wv[8]; float sv[8];
#pragma unroll
    for (int i = 0; i < 8; ++i) { const u32x4* q = (const u32x4*)(p.wmod + (size_t)(kq + 64 * i) * 3072 + n0 + cg * 8); wv[i] = NT_SRC ? __builtin_nontemporal_load(q) : *q; }
#pragma unroll
    for (int i = 0; i < 8; ++i) { float cv = bflo(p.c[kq + 64 * i]); sv[i] = rb(cv / (1.f + __expf(-cv))); }
    float acc[8] = {};
#pragma unroll
    for (int i = 0; i < 8; ++i)
#pragma unroll
      for (int e = 0; e < 4; ++e) { acc[2 * e] += sv[i] * bflo(wv[i][e]); acc[2 * e + 1] += sv[i] * bfhi(wv[i][e]); }
#pragma unroll
    for (int e = 0; e < 8; ++e) { float v = acc[e]; v += __shfl_xor(v, 8); v = swap16_add(v); v = swap32_add(v); acc[e] = v; }
    if (lane < 8)
#pragma unroll
      for (int e = 0; e < 8; ++e) red[wid][lane * 8 + e] = acc[e];
    __syncthreads();
    if (tid < 64) {
      float s = 0;
#pragma unroll
      for (int w = 0; w < 8; ++w) s += red[w][tid];
      s = rb(s);
      const int n = n0 + tid, seg = n >> 9;
      if (seg == 1 || seg == 4) s = rb(1.f + s);
      ((u16*)(p.ws + WS_MOD))[n] = (u16)(__float_as_uint(s) >> 16);
    }
    RT(0, 1);
    return;
  }
  b -= 48;
  if (b < 256) {
    const int row = b * 8 + wid;
    const u32x4 xv = *(const u32x4*)(p.x + (size_t)row * D_ + lane * 8);
    u32x4 wv[8];
#pragma unroll
    for (int i = 0; i < 8; ++i) wv[i] = *(const u32x4*)(p.x + (size_t)((b >> 3) * 64 + wid * 8 + i) * D_ + lane * 8);
    float f[8];
#pragma unroll
    for (int e = 0; e < 4; ++e) { f[2 * e] = bflo(xv[e]); f[2 * e + 1] = bfhi(xv[e]); }
    float s = 0;
#pragma unroll
    for (int e = 0; e < 8; ++e) s += f[e];
    const float mean = wave_sum(s) * (1.f / 512.f);
    float q = 0;
#pragma unroll
    for (int e = 0; e < 8; ++e) { float d = f[e] - mean; q += d * d; }
    const float var = wave_sum(q) * (1.f / 512.f);
    float mb, db, r; ln_consts(mean, var, mb, db, r);
    if (lane == 0) *(f32x2*)(p.ws + WS_Y + (size_t)row * 8) = (f32x2){mb, db};
#pragma unroll
    for (int i = 0; i < 8; ++i) asm volatile("" ::"v"(wv[i]));
    RT(0, 1);
    return;
  }
  b -= 256;
  Retile t; char* dst;
  {
    const int s = b; retile_load(t, p.w1, 512, 32 * s, false, 64 * wid, lane); dst = p.ws + WS_W1T + (size_t)(wid * 16 + s) * 4096;
    if (s == 0 && tid < FL_N) *(unsigned*)(p.ws + WS_FLAG + tid * 128) = 0u;
  }
#pragma unroll
  for (int i = 0; i < 4; ++i) *(u32x4*)(dst + i * 1024 + lane * 16) = retile_frag(t, i);
  RT(0, 1);
#undef WSP
#define WSP ws
}

template <int V> using IC = std::integral_constant<int, V>;
template <int N, class F, int... Is> DI void sfor_(F&& f, std::integer_sequence<int, Is...>) { (f(IC<Is>{}), ...); }
template <int N, class F> DI void sfor(F&& f) { sfor_<N>(f, std::make_integer_sequence<int, N>{}); }
template <int NI, int NQT, int NSTEP, int DEPTH, int NSET, int BD = 1, class ALoad, class BRead, class Hook>
DI void gemm_steps(f32x4 (&acc)[NSET][NI][NQT], bf16x8 (&ring)[DEPTH][NI], ALoad aload, BRead bread, Hook hook) {
  constexpr int NM = NI * NQT, PER = NSTEP / NSET;
  bf16x8 B[BD + 1][NQT];
#pragma unroll
  for (int s = 0; s < BD; ++s)
#pragma unroll
    for (int qt = 0; qt < NQT; ++qt) B[s][qt] = bread(s, qt);
  SCHED_FENCE();
  sfor<NSTEP * NM>([&](auto X) AI {
    constexpr int s = X.value / NM, m = X.value % NM, i = m / NQT, qt = m % NQT;
    if constexpr (s % PER == 0) acc[s / PER][i][qt] = MFMA16(ring[s % DEPTH][i], B[s % (BD + 1)][qt], (f32x4){});
    else                        acc[s / PER][i][qt] = MFMA16(ring[s % DEPTH][i], B[s % (BD + 1)][qt], acc[s / PER][i][qt]);
    if constexpr (m < NQT && s + BD < NSTEP) B[(s + BD) % (BD + 1)][m] = bread(s + BD, m);
    if constexpr (qt == NQT - 1 && s + DEPTH < NSTEP) ring[s % DEPTH][i] = aload(s + DEPTH, i);
    hook(IC<s>{}, IC<m>{});
    SCHED_FENCE();
  });
}

constexpr int HT_STRIDE = 1056, QX_STRIDE = 272;
constexpr int L1_HT = 0, L1_K = 69632, L1_V = L1_K + 3 * 16384, L1_WR = 65536, L1_END = 163840;
#define PRIO_G3 21
#define G4_AHEAD 2
#define ROT 1
#define P0_STEP 28
#define PRIO_P 1
#define PRIO_Q 11
#define PRIO_O 21
#define PRIO_A 0
#define GBAR 1
#define WO_SC1 1
#define OPASS 2
#define PF_ON 1
#define RT_AUX 16
#define PART_AUX 16
constexpr float SM_C = 0.12751743082459868f;
constexpr float SM_LIMIT = 1099511627776.f;

DI float max16(const f32x16& S) {
  float m = fmaxf(S[0], S[1]);
#pragma unroll
  for (int r = 2; r < 16; ++r) m = fmaxf(m, S[r]);
  return m;
}
DI unsigned scale_pk(unsigned p, float f) { return pk2(bflo(p) * f, bfhi(p) * f); }
DI void sm_half(const f32x16& S, float negmc, float& l0, float& l1, u32x4& pa, u32x4& pb) {
#pragma unroll
  for (int p = 0; p < 4; ++p) {
    const float e0 = __builtin_amdgcn_exp2f(__builtin_fmaf(S[2 * p], SM_C, negmc)), e1 = __builtin_amdgcn_exp2f(__builtin_fmaf(S[2 * p + 1], SM_C, negmc));
    l0 += e0; l1 += e1; pa[p] = pk2(e0, e1);
  }
#pragma unroll
  for (int p = 0; p < 4; ++p) {
    const float e0 = __builtin_amdgcn_exp2f(__builtin_fmaf(S[8 + 2 * p], SM_C, negmc)), e1 = __builtin_amdgcn_exp2f(__builtin_fmaf(S[8 + 2 * p + 1], SM_C, negmc));
    l0 += e0; l1 += e1; pb[p] = pk2(e0, e1);
  }
}
struct SmPipe { float x[16], e[16]; };
DI void sm_gap(const int m, SmPipe& sp, const f32x16& S, float negmc, float& l0, float& l1, u32x4& pa, u32x4& pb) {
#pragma unroll
  for (int e = 0; e < 16; ++e) {
    const int f = e * 13 / 16;
    if (f + 2 == m) {
      if (e & 1) {
        l1 += sp.e[e]; PIN(l1);
        unsigned w = pk2(sp.e[e - 1], sp.e[e]); PIN(w);
        if (e < 8) pa[e >> 1] = w; else pb[(e - 8) >> 1] = w;
      } else { l0 += sp.e[e]; PIN(l0); }
    }
  }
#pragma unroll
  for (int e = 0; e < 16; ++e) if (e * 13 / 16 + 1 == m) sp.e[e] = __builtin_amdgcn_exp2f(sp.x[e]);
#pragma unroll
  for (int e = 0; e < 16; ++e) if (e * 13 / 16 == m) sp.x[e] = __builtin_fmaf(S[e], SM_C, negmc);
}
DI void pack_acc16(const f32x4 (&acc)[4][4], int qt, u32x4& w0, u32x4& w1) {
#pragma unroll
  for (int rg = 0; rg < 2; ++rg) {
    w0[2 * rg] = pk2(acc[0][qt][rg], acc[1][qt][rg]); w0[2 * rg + 1] = pk2(acc[2][qt][rg], acc[3][qt][rg]);
    w1[2 * rg] = pk2(acc[0][qt][2 + rg], acc[1][qt][2 + rg]); w1[2 * rg + 1] = pk2(acc[2][qt][2 + rg], acc[3][qt][2 + rg]);
  }
}

typedef __attribute__((address_space(1))) unsigned gu32;
typedef __attribute__((address_space(1))) unsigned long long gu64;
constexpr unsigned SPIN_CAP = 1u << 20;
struct K1Args { const u16 *x, *wq, *kc, *vc, *wo, *w2, *w3, *w4; const float* lnst; char* ws; u16* out; };

__global__ __launch_bounds__(512, 2) __attribute__((amdgpu_waves_per_eu(2, 2)))
void k1_attn(const K1Args p) {
  extern __shared__ __attribute__((aligned(16))) char lds[];
  char* __restrict__ ws = p.ws;
  const int tid = threadIdx.x, lane = tid & 63;
  const int wid = (int)rfl((unsigned)tid >> 6);
  const int g = blockIdx.x & 7, rbk = blockIdx.x >> 3;
  const int a = lane & 15, g4 = lane >> 4, r32 = lane & 31, hh = lane >> 5;
  const unsigned ldsb = (unsigned)(size_t)lds;
  const unsigned fo = lane * 16;
  RT(1, 0);
  STAMP(0);
  const rsrc_t rK = mk_rsrc(p.kc + ((size_t)g * SK + (SK - WIN)) * DH, WIN * DH * 2), rV = mk_rsrc(p.vc + ((size_t)g * SK + (SK - WIN)) * DH, WIN * DH * 2);
  const unsigned drow = wid * 8 + ((lane >> 2) & 7);
  const unsigned kvoff = drow * 256 + (lane >> 5) * 64 + (((lane & 3) ^ ((drow >> 2) & 3)) * 16);
  const unsigned vvoff = drow * 256 + (lane >> 5) * 64 + (lane & 3) * 16;
  const unsigned kdst = ldsb + L1_K + wid * 2048, vdst = ldsb + L1_V + wid * 2048;
  auto dmaK = [&](int t) AI { dma16(kdst + (t % 3) * 16384, kvoff, rK, t * 16384); dma16(kdst + (t % 3) * 16384 + 1024, kvoff, rK, t * 16384 + 128); };
  auto dmaV = [&](int t) AI { dma16(vdst + (t & 1) * 16384, vvoff, rV, t * 16384); dma16(vdst + (t & 1) * 16384 + 1024, vvoff, rV, t * 16384 + 128); };
  const rsrc_t rWq = mk_rsrc(p.wq, D_ * 4096 * 2);
  const unsigned wr8 = lane >> 3, wcol = (unsigned)(g * 512 + wid * 64) * 2u;
  const unsigned wvo0 = wr8 * 8192u + wcol + (((lane & 7) ^ (((wr8 >> 1) & 1) << 1)) * 16), wvo1 = wr8 * 8192u + wcol + (((lane & 7) ^ ((((wr8 >> 1) & 1) | 2) << 1)) * 16);
  const unsigned wdst = ldsb + L1_WR + wid * 12288;
  const int rot = ROT ? (rbk & 15) : 0;
  auto wq_dma = [&](int s, int j) AI { dma16(wdst + (s % 3) * 4096 + j * 1024, (j & 1) ? wvo1 : wvo0, rWq, (unsigned)(((s + rot) & 15) * 32 + j * 8) * 8192u); };
  {
    const u32x4 sav = *(const u32x4*)(ws + WS_MOD + fo), cav = *(const u32x4*)(ws + WS_MOD + 1024 + fo);
    u32x4 xv[8];
#pragma unroll
    for (int i = 0; i < 8; ++i) xv[i] = *(const u32x4*)((const char*)p.x + (size_t)(rbk * 64 + wid * 8 + i) * 1024 + fo);
    float st[16];
#pragma unroll
    for (int i = 0; i < 16; ++i) st[i] = p.lnst[(rbk * 64 + wid * 8) * 2 + i];
    SCHED_FENCE();
    float ca[8], sa[8];
#pragma unroll
    for (int e = 0; e < 4; ++e) { ca[2 * e] = bflo(cav[e]); ca[2 * e + 1] = bfhi(cav[e]); sa[2 * e] = bflo(sav[e]); sa[2 * e + 1] = bfhi(sav[e]); }
    if (PRIO_P && wid >= 4) __builtin_amdgcn_s_setprio(1);
#pragma unroll
    for (int hf = 0; hf < 2; ++hf) {
      float lo[16], hi[16], rr[4]; unsigned t[16];
#define H_STAGE(body) _Pragma("unroll") for (int j = 0; j < 16; ++j) { const int i = hf * 4 + (j >> 2), d = j & 3; (void)i; (void)d; body; } SCHED_FENCE()
#pragma unroll
      for (int i = 0; i < 4; ++i) rr[i] = __builtin_amdgcn_rcpf(st[2 * (hf * 4 + i) + 1]);
      H_STAGE(lo[j] = bflo(xv[i][d]); hi[j] = bfhi(xv[i][d]));
      if (hf == 0) {
        WAIT_VM0();
#pragma unroll
        for (int k = 0; k < 4; ++k) wq_dma(0, k);
        dma16(wdst + 8192, fo, wid < 4 ? rK : rV, (rbk * 4 + (wid & 3)) * 1024);
        SCHED_FENCE();
      }
      H_STAGE(lo[j] -= st[2 * i]; hi[j] -= st[2 * i]);
      H_STAGE(lo[j] = rb(lo[j]); hi[j] = rb(hi[j]));
      if (PRIO_P && hf == 1) __builtin_amdgcn_s_setprio(0);
      H_STAGE(lo[j] *= rr[j >> 2]; hi[j] *= rr[j >> 2]);
      H_STAGE(lo[j] = rb(lo[j]); hi[j] = rb(hi[j]));
#pragma unroll
      for (int k = 0; k < 4; ++k) wq_dma(1 + hf, k);
      SCHED_FENCE();
      H_STAGE(lo[j] *= ca[2 * d]; hi[j] *= ca[2 * d + 1]);
      H_STAGE(lo[j] = rb(lo[j]); hi[j] = rb(hi[j]));
      H_STAGE(lo[j] += sa[2 * d]; hi[j] += sa[2 * d + 1]);
      H_STAGE(t[j] = pk2(lo[j], hi[j]));
#pragma unroll
      for (int i = 0; i < 4; ++i) {
        const int r = wid * 8 + hf * 4 + i;
        *(u32x4*)(lds + r * 1024 + ((lane ^ (r & 15)) * 16)) = (u32x4){t[4 * i], t[4 * i + 1], t[4 * i + 2], t[4 * i + 3]};
      }
      SCHED_FENCE();
    }
  }

  Retile rtl; const u16* rsrc_w; int rld, rk0, rn0; bool rperm; size_t rdst; unsigned rhalf = 2048;
  if (wid < 4) { const int id = rbk * 4 + wid, s = id >> 3, ng = id & 7; rsrc_w = p.wo; rld = 512; rk0 = g * 512 + 32 * s; rn0 = 64 * ng; rperm = false; rdst = WS_WOT + (size_t)(g * 8 + ng) * 65536 + s * 2048; rhalf = 32768; }
  else {
    int id = blockIdx.x * 4 + wid - 4, s, ng; rperm = true;
    if (id < 128)      { ng = id >> 4; s = id & 15; rsrc_w = p.w2; rld = 512; rdst = WS_W2T + (size_t)id * 4096; }
    else if (id < 576) { id -= 128; ng = id >> 4; s = id & 15; rsrc_w = p.w3; rld = F_; rdst = WS_W3T + (size_t)id * 4096; }
    else               { id -= 576; ng = id & 7; s = id >> 3; rsrc_w = p.w4; rld = 512; rdst = WS_W4T + (size_t)(ng * 56 + s) * 4096; }
    rk0 = 64 * (s >> 1) + 2 * (s & 1); rn0 = 64 * ng;
  }
  gu32* flags = (gu32*)(ws + WS_FLAG);
  gu32* flag = flags + (FL_W + g) * 32;
  gu32* flagw = flags + (FL_W + (lane & 7)) * 32;
  STAMP(1);
  __syncthreads();
  STAMP(2);
  if (wid >= 4 && PRIO_Q > 0) __builtin_amdgcn_s_setprio(1);

  const char* bl = lds + L1_HT + a * HT_STRIDE + g4 * 16;
  auto bread = [&](int s, int qt) AI { return *(const bf16x8*)(bl + 64 * ((s + rot) & 15) + qt * 16 * HT_STRIDE); };
  f32x16 o[4]; bf16x8 qf[8]; float l0, l1, negmc;
  {
    f32x4 acc[4][4];
    bf16x4 Alo[2][4], Ahi[2][4]; bf16x8 Bq[2][4];
    const unsigned hb0 = (unsigned)(a * 1024 + ((g4 ^ a) * 16));
    const unsigned tsw = ((lane >> 3) & 1) | ((g4 & 1) << 1);
    const char* tb[4];
#pragma unroll
    for (int i = 0; i < 4; ++i) tb[i] = lds + L1_WR + wid * 12288 + (8 * g4 + ((lane >> 2) & 3)) * 128 + ((i ^ tsw) * 32) + (lane & 3) * 8;
    auto aread = [&](int s, int k) AI {
      const bf16x4 v = __builtin_amdgcn_ds_read_tr16_b64_v4bf16(LDSP(bf16x4, tb[k >> 1] + (s % 3) * 4096 + (k & 1) * 512));
      if (k & 1) Ahi[s & 1][k >> 1] = v; else Alo[s & 1][k >> 1] = v;
    };
    auto hread = [&](int s, int qt) AI { return *(const bf16x8*)(lds + (hb0 ^ (unsigned)(((s + rot) & 15) * 64)) + qt * 16384); };
    asm volatile("s_waitcnt vmcnt(8)" ::: "memory");
#pragma unroll
    for (int k = 0; k < 8; ++k) aread(0, k);
#pragma unroll
    for (int qt = 0; qt < 4; ++qt) Bq[0][qt] = hread(0, qt);
    SCHED_FENCE();
    sfor<256>([&](auto X) AI {
      constexpr int s = X.value >> 4, m = X.value & 15, i = m >> 2, qt = m & 3;
      const bf16x8 af = __builtin_shufflevector(Alo[s & 1][i], Ahi[s & 1][i], 0, 1, 2, 3, 4, 5, 6, 7);
      if constexpr (s == 0) acc[i][qt] = MFMA16(af, Bq[0][qt], (f32x4){}); else acc[i][qt] = MFMA16(af, Bq[s & 1][qt], acc[i][qt]);
      if constexpr (s == PRIO_Q && m == 0) __builtin_amdgcn_s_setprio(0);
      if constexpr (s < 15) {
        if constexpr (m < 4) Bq[(s + 1) & 1][m] = hread(s + 1, m);
        if constexpr (m == 3) { if constexpr (s < 14) asm volatile("s_waitcnt vmcnt(4)" ::: "memory"); else asm volatile("s_waitcnt vmcnt(0)" ::: "memory"); }
        if constexpr (m >= 4 && m < 12) aread(s + 1, m - 4);
        if constexpr (m >= 13 && s + 3 < 16) { if constexpr (m == 13) wq_dma(s + 3, 0); wq_dma(s + 3, m - 12); }
      }
      SCHED_FENCE();
    });
    STAMP(3);
    __syncthreads();
    dmaK(0);
    SCHED_FENCE();
#pragma unroll
    for (int qt = 0; qt < 4; ++qt) {
      char* dst = lds + L1_HT + ((wid >> 1) * 64 + qt * 16 + a) * QX_STRIDE + ((wid & 1) * 64 + g4 * 4) * 2;
#pragma unroll
      for (int i = 0; i < 4; ++i) *(u32x2*)(dst + i * 32) = (u32x2){pk2(acc[i][qt][0], acc[i][qt][1]), pk2(acc[i][qt][2], acc[i][qt][3])};
    }
    SCHED_FENCE();
    dmaK(1); dmaV(0);
    asm volatile("s_waitcnt vmcnt(4)" ::: "memory");
    __syncthreads();
#pragma unroll
    for (int s = 0; s < 8; ++s) qf[s] = *(const bf16x8*)(lds + L1_HT + ((wid >> 1) * 64 + (wid & 1) * 32 + r32) * QX_STRIDE + 32 * s + 16 * hh);
  }
  STAMP(4);

  const unsigned kx = (r32 >> 2) & 3;
  const char* kb0 = lds + L1_K + 2048 * (r32 >> 3) + 64 * (r32 & 7) + 16 * ((0 + hh) ^ kx);
  const char* kb1 = lds + L1_K + 2048 * (r32 >> 3) + 64 * (r32 & 7) + 16 * ((2 + hh) ^ kx);
  const char* vb = lds + L1_V + (4 * hh + ((lane >> 2) & 3)) * 64 + ((lane >> 4) & 1) * 32 + (lane & 3) * 8;
  auto kfrag = [&](int t, int m) AI { return *(const bf16x8*)(((m & 1) ? kb1 : kb0) + (t % 3) * 16384 + 512 * ((m & 7) >> 1) + 8192 * (m >> 3)); };
  auto vfrag = [&](int t, int m) AI {
    const char* q = vb + (t & 1) * 16384 + (m >> 2) * 4096 + (m & 3) * 512;
    bf16x4 lo = __builtin_amdgcn_ds_read_tr16_b64_v4bf16(LDSP(bf16x4, q)), hi = __builtin_amdgcn_ds_read_tr16_b64_v4bf16(LDSP(bf16x4, q + 2048));
    return __builtin_shufflevector(lo, hi, 0, 1, 2, 3, 4, 5, 6, 7);
  };
  auto fixup = [&](const f32x16& S, float l0p, float l1p, u32x4& pa, u32x4& pb, bool pend, u32x4& pend0, u32x4& pend1) AI {
    const float mx = swap32_max(__builtin_fmaf(max16(S), SM_C, negmc));
    const float dm = fmaxf(__builtin_ceilf(mx), 0.f);
    const float f = __builtin_amdgcn_exp2f(-dm);
    negmc -= dm; l0 = l0p * f; l1 = l1p * f;
#pragma unroll
    for (int db = 0; db < 4; ++db) o[db] *= f;
    if (pend) {
#pragma unroll
      for (int q = 0; q < 4; ++q) { pend0[q] = scale_pk(pend0[q], f); pend1[q] = scale_pk(pend1[q], f); }
    }
    sm_half(S, negmc, l0, l1, pa, pb);
  };

  f32x16 Sa0, Sa1, Sb0, Sb1;
  u32x4 Pa0, Pa1, Pb0, Pb1, P2, P3;
  bf16x8 kf[4], vf[4];
  {
#pragma unroll
    for (int m = 0; m < 16; ++m) {
      const bf16x8 k = kfrag(0, m);
      if (m < 8) Sa0 = MFMA32(k, qf[m & 7], m == 0 ? (f32x16){} : Sa0); else Sa1 = MFMA32(k, qf[m & 7], m == 8 ? (f32x16){} : Sa1);
      if (m == 6) dmaK(2);
      if (m == 12) dmaV(1);
    }
  }
  negmc = -swap32_max(fmaxf(max16(Sa0), max16(Sa1))) * SM_C;
  SCHED_FENCE();
  asm volatile("s_waitcnt vmcnt(4)" ::: "memory"); __builtin_amdgcn_s_barrier(); MEM_FENCE();
#pragma unroll
  for (int m = 0; m < 3; ++m) kf[m] = kfrag(1, m);
  l0 = 0.f; l1 = 0.f;
  sm_half(Sa0, negmc, l0, l1, Pa0, Pa1);
#pragma unroll
  for (int db = 0; db < 4; ++db) o[db] = (f32x16){};
  SCHED_FENCE();

  const rsrc_t rWo = mk_rsrc(ws + WS_WOT + (size_t)(g * 8 + wid) * 65536, 65536);
  const rsrc_t rRt = mk_rsrc(ws + rdst, 36864);
  bf16x8 ring2[12][2];
  auto wo_load = [&](int sg, int i) AI { return __builtin_bit_cast(bf16x8, __builtin_amdgcn_raw_buffer_load_b128(rWo, fo + i * 1024, (sg >> 4) * 32768 + ((sg + rot) & 15) * 2048, 16)); };
  unsigned flagv = 0;

  auto tile = [&](auto T_, const f32x16& C1, f32x16& N0, f32x16& N1, u32x4& Pc0, u32x4& Pc1, u32x4& Pn0, u32x4& Pn1) AI {
    constexpr int T = T_.value;
    if (T == 4) STAMP(12);
    if (PRIO_A && wid >= 4) __builtin_amdgcn_s_setprio(1);
    {
      const float l0p = l0, l1p = l1; SmPipe sp;
#pragma unroll
      for (int m = 0; m < 16; ++m) {
        if (T < 7) {
          if (m < 8) N0 = MFMA32(kf[m & 3], qf[m & 7], m == 0 ? (f32x16){} : N0); else N1 = MFMA32(kf[m & 3], qf[m & 7], m == 8 ? (f32x16){} : N1);
          if (m + 3 < 16) kf[(m + 3) & 3] = kfrag(T + 1, m + 3);
        }
        if (m >= 13) vf[m - 13] = vfrag(T, m - 13);
        sm_gap(m, sp, C1, negmc, l0, l1, P2, P3);
        if (T == 7) ring2[m >> 1][m & 1] = wo_load(m >> 1, m & 1);
        if (T < 7) SCHED_FENCE();
      }
      MEM_FENCE();
      if (__builtin_expect(__any(!(l0 + l1 <= SM_LIMIT)), 0)) fixup(C1, l0p, l1p, P2, P3, true, Pc0, Pc1);
    }
    if (T == 4) STAMP(13);
    if (PRIO_A == 1) __builtin_amdgcn_s_setprio(0);
    {
      const float l0p = l0, l1p = l1; SmPipe sp;
#pragma unroll
      for (int m = 0; m < 16; ++m) {
        const u32x4 pp = (m < 4) ? Pc0 : (m < 8) ? Pc1 : (m < 12) ? P2 : P3;
        o[m & 3] = MFMA32(vf[m & 3], __builtin_bit_cast(bf16x8, pp), o[m & 3]);
        if (m + 3 < 16) vf[(m + 3) & 3] = vfrag(T, m + 3);
        if (T < 7) sm_gap(m, sp, N0, negmc, l0, l1, Pn0, Pn1);
        if (T == 7 && m < 8) ring2[8 + (m >> 1)][m & 1] = wo_load(8 + (m >> 1), m & 1);
        if (PRIO_A == 2 && m == 4) __builtin_amdgcn_s_setprio(0);
        if (T < 7 && m == 13) {
          if (T == 6 && wid == 0) {
            for (unsigned spins = 0; !__all(flagv == 32u) && spins < SPIN_CAP; ++spins) { __builtin_amdgcn_s_sleep(2); flagv = __hip_atomic_load(flagw, __ATOMIC_RELAXED, __HIP_MEMORY_SCOPE_AGENT); }
          }
          if (T == 4) STAMP(14);
          if (T == 2) WAIT_VM0();
          if (T == 1) asm volatile("s_waitcnt vmcnt(8) lgkmcnt(0)" : "+v"(vf[2]), "+v"(vf[3])::"memory");
          else if (T == 3) asm volatile("s_waitcnt vmcnt(4) lgkmcnt(0)" : "+v"(vf[2]), "+v"(vf[3])::"memory");
          else             asm volatile("s_waitcnt vmcnt(0) lgkmcnt(0)" : "+v"(vf[2]), "+v"(vf[3])::"memory");
          __builtin_amdgcn_s_barrier();
          MEM_FENCE();
          if (T == 4) STAMP(15);
          if (T == 4 && tid == 0) __hip_atomic_fetch_add(flag, 1u, __ATOMIC_RELAXED, __HIP_MEMORY_SCOPE_AGENT);
          if (T == 5 && wid == 0) flagv = __hip_atomic_load(flagw, __ATOMIC_RELAXED, __HIP_MEMORY_SCOPE_AGENT);
        }
        if (T < 6 && m == 13) { kf[0] = kfrag(T + 2, 0); kf[1] = kfrag(T + 2, 1); }
        if (T < 6 && m == 14) kf[2] = kfrag(T + 2, 2);
        if (T < 5 && m == 14) dmaK(T + 3);
        if (T < 6 && m == 15) dmaV(T + 2);
        if (T == 0 && m == 15) { retile_load(rtl, rsrc_w, rld, rk0, rperm, rn0, lane); MEM_FENCE(); }
        if (T == 2 && m == 15) {
#pragma unroll
          for (int i = 0; i < 4; ++i) __builtin_amdgcn_raw_buffer_store_b128(retile_frag(rtl, i), rRt, fo + (i >> 1) * rhalf + (i & 1) * 1024, 0, 16);
        }
        SCHED_FENCE();
      }
      MEM_FENCE();
      if (T < 7) {
        if (__builtin_expect(__any(!(l0 + l1 <= SM_LIMIT)), 0)) fixup(N0, l0p, l1p, Pn0, Pn1, false, Pc0, Pc1);
      }
    }
  };
  STAMP(5);
  tile(IC<0>{}, Sa1, Sb0, Sb1, Pa0, Pa1, Pb0, Pb1);
  tile(IC<1>{}, Sb1, Sa0, Sa1, Pb0, Pb1, Pa0, Pa1);
  tile(IC<2>{}, Sa1, Sb0, Sb1, Pa0, Pa1, Pb0, Pb1);
  tile(IC<3>{}, Sb1, Sa0, Sa1, Pb0, Pb1, Pa0, Pa1);
  tile(IC<4>{}, Sa1, Sb0, Sb1, Pa0, Pa1, Pb0, Pb1);
  tile(IC<5>{}, Sb1, Sa0, Sa1, Pb0, Pb1, Pa0, Pa1);
  STAMP(6);
  tile(IC<6>{}, Sa1, Sb0, Sb1, Pa0, Pa1, Pb0, Pb1);
  tile(IC<7>{}, Sb1, Sa0, Sa1, Pb0, Pb1, Pa0, Pa1);
  STAMP(7);

  {
    const float inv = 1.f / swap32_add(l0 + l1);
    char* dst = lds + L1_HT + ((wid & 1) * 32 + r32) * HT_STRIDE + ((wid >> 1) * 128 + 4 * hh) * 2;
#pragma unroll
    for (int db = 0; db < 4; ++db)
#pragma unroll
      for (int gq = 0; gq < 4; ++gq) {
        u32x2 w = {pk2(o[db][4 * gq] * inv, o[db][4 * gq + 1] * inv), pk2(o[db][4 * gq + 2] * inv, o[db][4 * gq + 3] * inv)};
        *(u32x2*)(dst + (32 * db + 8 * gq) * 2) = w;
      }
  }
  __syncthreads();
  STAMP(8);
  if (wid >= 4 && PRIO_O > 0) __builtin_amdgcn_s_setprio(1);
  {
    f32x4 acc[2][2][4];
    const rsrc_t rP = mk_rsrc(ws + WS_PART + (size_t)((rbk * 8 + g) * 8 + wid) * 8192, 8192);
    auto pstore = [&](int ps, int qt) AI {
      u32x4 w;
#pragma unroll
      for (int r = 0; r < 4; ++r) w[r] = pk2(acc[ps][0][qt][r], acc[ps][1][qt][r]);
      __builtin_amdgcn_raw_buffer_store_b128(w, rP, fo + (qt * 2 + ps) * 1024, 0, PART_AUX);
    };
    gemm_steps<2, 4, 32, 12, 2, 2>(acc, ring2, wo_load, bread, [&](auto S_, auto M_) AI {
      if constexpr (S_.value == PRIO_O && M_.value == 0) __builtin_amdgcn_s_setprio(0);
      if constexpr (S_.value >= 20 && S_.value < 22 && (M_.value & 3) == 1) pstore(0, (S_.value - 20) * 2 + (M_.value >> 2));
      if constexpr (S_.value == P0_STEP && M_.value == 7) {
        WAIT_VM0(); asm volatile("s_waitcnt vmcnt(0)" ::: "memory"); __builtin_amdgcn_s_barrier(); MEM_FENCE();
        if (tid == 0) __hip_atomic_fetch_add(flags + (FL_P0 + rbk) * 32, 1u, __ATOMIC_RELAXED, __HIP_MEMORY_SCOPE_AGENT);
      } });
    STAMP(9);
#pragma unroll
    for (int qt = 0; qt < 4; ++qt) pstore(1, qt);
  }
  STAMP(10);

  const rsrc_t rWs = mk_rsrc(ws, (unsigned)WS_END);
  auto ld1 = [&](size_t off, unsigned soff) AI { return __builtin_amdgcn_raw_buffer_load_b128(rWs, (unsigned)off, soff, 16); };
  auto ldf1 = [&](size_t off, unsigned soff) AI { return __builtin_bit_cast(bf16x8, __builtin_amdgcn_raw_buffer_load_b128(rWs, (unsigned)off, soff, 16)); };
  auto st1 = [&](size_t off, u32x4 v) AI { __builtin_amdgcn_raw_buffer_store_b128(v, rWs, (unsigned)off, 0, 16); };
  auto publish = [&](int idx) AI {
    asm volatile("s_waitcnt vmcnt(0) lgkmcnt(0)" ::: "memory"); __builtin_amdgcn_s_barrier(); MEM_FENCE();
    if (tid == 0) __hip_atomic_fetch_add(flags + idx * 32, 1u, __ATOMIC_RELAXED, __HIP_MEMORY_SCOPE_AGENT);
  };
  auto await = [&](int pw, int idx, unsigned want) AI {
    if (wid == pw) {
      unsigned spins = 0;
      while (!__all(__hip_atomic_load(flags + idx * 32, __ATOMIC_RELAXED, __HIP_MEMORY_SCOPE_AGENT) >= want) && ++spins < SPIN_CAP) __builtin_amdgcn_s_sleep(1);
    }
    MEM_FENCE(); __builtin_amdgcn_s_barrier(); MEM_FENCE();
  };
  const int b = blockIdx.x;
  const u16* modb = (const u16*)(ws + WS_MOD);
  publish(FL_P + rbk);
  STAMP(11);
  RT(1, 1);

  RT(2, 0);
  STAMPK(2, 0);
  {
    f32x2* red = (f32x2*)(lds + 16 * HT_STRIDE);
    const int rt = (b & 7) * 16 + (b >> 4), nh = (b >> 3) & 1;
    const int prb = rt >> 2, qt = rt & 3, row = rt * 16 + a, col = wid * 64 + g4 * 16;
    constexpr int D1 = 12, D1E = 12;
    bf16x8 A[D1][2];
    const int G = nh * 4 + (wid >> 1), ps = wid & 1;
    const rsrc_t rW = mk_rsrc(ws + WS_W1T + (size_t)G * 65536 + ps * 2048, 65536);
    auto aload = [&](int s, int i) AI { return ldfrag(rW, fo, (s * 4 + i) * 1024); };
    u32x4 pv[8][2];
    const size_t poff = WS_PART + (size_t)((prb * 64 + wid) * 8 + qt * 2) * 1024 + fo;
    const int prot = 0;
    await(0, FL_P0 + prb, 8);
    if (wid != 7) {
#pragma unroll
      for (int gg = 0; gg < 8; ++gg) pv[gg][0] = ld1(poff, (((gg + prot) & 7) * 64) * 1024);
    }
    SCHED_FENCE();
    await(7, FL_P + prb, 8);
    STAMPK(2, 1);
#pragma unroll
    for (int gg = 0; gg < 8; ++gg) pv[gg][1] = ld1(poff, (((gg + prot) & 7) * 64 + 1) * 1024);
    if (wid == 7) {
#pragma unroll
      for (int gg = 0; gg < 8; ++gg) pv[gg][0] = ld1(poff, (((gg + prot) & 7) * 64) * 1024);
    }
    u32x4 xv[2], gav[2], cbv[2], sbv[2];
#pragma unroll
    for (int h = 0; h < 2; ++h) {
      xv[h] = *(const u32x4*)(p.x + (size_t)row * D_ + col + 8 * h);
      gav[h] = *(const u32x4*)(modb + 1024 + col + 8 * h); sbv[h] = *(const u32x4*)(modb + 1536 + col + 8 * h); cbv[h] = *(const u32x4*)(modb + 2048 + col + 8 * h);
    }
    SCHED_FENCE();
    __builtin_amdgcn_s_barrier();
#pragma unroll
    for (int s = 0; s < D1E; ++s) { A[s][0] = aload(s, 0); A[s][1] = aload(s, 1); }
    SCHED_FENCE();
    float v[16];
#pragma unroll
    for (int d = 0; d < 8; ++d) { v[2 * d] = bflo(pv[0][d & 1][d >> 1]); v[2 * d + 1] = bfhi(pv[0][d & 1][d >> 1]); }
#pragma unroll
    for (int gg = 1; gg < 8; ++gg) {
#pragma unroll
      for (int d = 0; d < 8; ++d) { v[2 * d] += bflo(pv[gg][d & 1][d >> 1]); v[2 * d + 1] += bfhi(pv[gg][d & 1][d >> 1]); }
      SCHED_FENCE();
    }
#pragma unroll
    for (int d = 0; d < 8; ++d) {
      const unsigned ga = gav[d >> 2][d & 3], xx = xv[d >> 2][d & 3];
      const unsigned x2 = pk2(__builtin_fmaf(bflo(ga), v[2 * d], bflo(xx)), __builtin_fmaf(bfhi(ga), v[2 * d + 1], bfhi(xx)));
      xv[d >> 2][d & 3] = x2; v[2 * d] = bflo(x2); v[2 * d + 1] = bfhi(x2);
    }
    if (nh == 0) { st1(WS_X2 + (size_t)row * 1024 + col * 2, xv[0]); st1(WS_X2 + (size_t)row * 1024 + col * 2 + 16, xv[1]); }
    STAMPK(2, 2);
    auto row_stats = [&](f32x2* rd, float& mean, float& rstd) AI {
      float s0 = 0, s1 = 0, q0 = 0, q1 = 0;
#pragma unroll
      for (int e = 0; e < 8; ++e) { s0 += v[2 * e]; s1 += v[2 * e + 1]; q0 = __builtin_fmaf(v[2 * e], v[2 * e], q0); q1 = __builtin_fmaf(v[2 * e + 1], v[2 * e + 1], q1); }
      float s = swap32_add(swap16_add(s0 + s1)), q = swap32_add(swap16_add(q0 + q1));
      if (g4 == 0) rd[wid * 16 + a] = (f32x2){s, q};
      asm volatile("s_waitcnt lgkmcnt(0)" ::: "memory"); __builtin_amdgcn_s_barrier(); MEM_FENCE();
      f32x2 t = rd[a];
#pragma unroll
      for (int w = 1; w < 8; ++w) t += rd[w * 16 + a];
      mean = t[0] * (1.f / 512.f);
      rstd = __builtin_amdgcn_rsqf(fmaxf(t[1] * (1.f / 512.f) - mean * mean, 0.f) + 1e-5f);
    };
    float mean, rstd;
#pragma unroll
    for (int s = D1E; s < D1; ++s) { A[s][0] = aload(s, 0); A[s][1] = aload(s, 1); }
    SCHED_FENCE();
    row_stats(red, mean, rstd);
    u32x4 hv[2];
#pragma unroll
    for (int d = 0; d < 8; ++d) {
      const unsigned cb = cbv[d >> 2][d & 3], sb = sbv[d >> 2][d & 3];
      const unsigned hb = pk2((v[2 * d] - mean) * rstd * bflo(cb) + bflo(sb), (v[2 * d + 1] - mean) * rstd * bfhi(cb) + bfhi(sb));
      hv[d & 1][d >> 1] = hb; v[2 * d] = bflo(hb); v[2 * d + 1] = bfhi(hb);
    }
    if (nh == 1) { st1(WS_HB + (size_t)row * 1024 + wid * 128 + g4 * 16, hv[0]); st1(WS_HB + (size_t)row * 1024 + wid * 128 + 64 + g4 * 16, hv[1]); }
    row_stats(red + 128, mean, rstd);
    const float nm = -mean * rstd;
#pragma unroll
    for (int h = 0; h < 2; ++h) {
      u32x4 o;
#pragma unroll
      for (int e = 0; e < 4; ++e) o[e] = pk2(__builtin_fmaf(v[8 * h + 2 * e], rstd, nm), __builtin_fmaf(v[8 * h + 2 * e + 1], rstd, nm));
      *(u32x4*)(lds + a * HT_STRIDE + (col + 8 * h) * 2) = o;
    }
    STAMPK(2, 3);
    asm volatile("s_waitcnt lgkmcnt(0)" ::: "memory"); __builtin_amdgcn_s_barrier(); MEM_FENCE();
    STAMPK(2, 4);
    f32x4 acc[1][2][1];
    gemm_steps<2, 1, 16, D1, 1, 4>(acc, A, aload, [&](int s, int) AI { return *(const bf16x8*)(bl + 64 * s); }, [](auto, auto) AI {});
    STAMPK(2, 5);
    u32x4 ov;
#pragma unroll
    for (int rg = 0; rg < 4; ++rg) ov[rg] = pk2(gelu_tanh(acc[0][0][0][rg]), gelu_tanh(acc[0][1][0][rg]));
    st1(WS_H1 + (size_t)(rt * 16 + 2 * G + ps) * 1024 + fo, ov);
    STAMPK(2, 6);
    publish(FL_H1 + rt);
    RT(2, 1);
    STAMPK(2, 7);
  }

  const int rb2 = (b & 7) * 4 + (b >> 6), ns = (b >> 3) & 7;
  const int ng = ns * 7 + (wid < 7 ? wid : 6);
  const size_t w3off = WS_W3T + (size_t)(ng >> 1) * 65536 + (ng & 1) * 2048 + fo;
  bf16x8 A[16][2];
  RT(3, 0);
  STAMPK(3, 0);
  {
    const int rt2 = (b & 7) * 8 + (b >> 5), nq = (b >> 3) & 3;
    const int G = nq * 2 + ((wid & 3) >> 1), ps = wid & 1;
    if (wid < 4) {
      const size_t aoff = WS_W2T + (size_t)G * 65536 + ps * 2048 + fo;
#pragma unroll
      for (int s = 0; s < 16; ++s) { A[s][0] = ldf1(aoff, (s * 4) * 1024); A[s][1] = ldf1(aoff, (s * 4 + 1) * 1024); }
    }
    SCHED_FENCE();
    { const int t = 2 * rt2 - 1 + (lane & 3); await(7, FL_H1 + (t < 0 ? 0 : t > 127 ? 127 : t), 2); }
    STAMPK(3, 1);
    const rsrc_t rH = mk_rsrc(ws + WS_H1, (unsigned)NXB);
    const int sh = (wid < 2) ? -1 : (wid < 4) ? 1 : (wid < 6) ? -2 : 2;
    u32x4 hf[2][2], hbv[2];
#pragma unroll
    for (int qt = 0; qt < 2; ++qt) {
      const int r = rt2 * 32 + qt * 16 + a + sh;
      const unsigned ho = (unsigned)((r >> 4) * 16384 + ((r & 15) + 16 * g4) * 16);
      hf[qt][0] = __builtin_amdgcn_raw_buffer_load_b128(rH, ho, (wid * 2) * 1024, 16); hf[qt][1] = __builtin_amdgcn_raw_buffer_load_b128(rH, ho, (wid * 2 + 1) * 1024, 16);
    }
    const size_t xoff = (size_t)(rt2 * 32 + a) * 1024 + G * 128 + ps * 64 + g4 * 16;
    if (wid < 4) { hbv[0] = ld1(WS_HB + xoff, 0); hbv[1] = ld1(WS_HB + xoff, 16384); }
#pragma unroll
    for (int qt = 0; qt < 2; ++qt) { *(u32x4*)(lds + (qt * 16 + wid * 2) * 1024 + fo) = hf[qt][0]; *(u32x4*)(lds + (qt * 16 + wid * 2 + 1) * 1024 + fo) = hf[qt][1]; }
    asm volatile("s_waitcnt lgkmcnt(0)" ::: "memory"); __builtin_amdgcn_s_barrier(); MEM_FENCE();
    STAMPK(3, 2);
    f32x2* red = (f32x2*)(lds + 32768);
    if (wid < 4) {
      f32x4 acc[1][2][2];
      gemm_steps<2, 2, 16, 16, 1, 3>(acc, A, [&](int, int) AI { return bf16x8{}; }, [&](int s, int qt) AI { return *(const bf16x8*)(lds + (qt * 16 + s) * 1024 + fo); }, [](auto, auto) AI {});
#pragma unroll
      for (int qt = 0; qt < 2; ++qt) {
        u32x4 ov; float s = 0, q = 0;
#pragma unroll
        for (int rg = 0; rg < 4; ++rg) {
          ov[rg] = pk2(bflo(hbv[qt][rg]) + acc[0][0][qt][rg], bfhi(hbv[qt][rg]) + acc[0][1][qt][rg]);
          const float lo = bflo(ov[rg]), hi = bfhi(ov[rg]); s += lo + hi; q += lo * lo + hi * hi;
        }
        st1(WS_X1 + xoff + qt * 16384, ov);
        s = swap32_add(swap16_add(s)); q = swap32_add(swap16_add(q));
        if (g4 == 0) red[wid * 32 + qt * 16 + a] = (f32x2){s, q};
      }
    } else if (wid < 7) {
#pragma unroll
      for (int s = 0; s < 16; ++s) { A[s][0] = ldf1(w3off, (s * 4) * 1024); A[s][1] = ldf1(w3off, (s * 4 + 1) * 1024); }
    }
    asm volatile("s_waitcnt lgkmcnt(0)" ::: "memory"); __builtin_amdgcn_s_barrier(); MEM_FENCE();
    if (tid < 32) {
      const f32x2 t = red[tid] + red[32 + tid] + red[64 + tid] + red[96 + tid];
      __hip_atomic_store((gu64*)(ws + WS_ST + (size_t)(rt2 * 32 + tid) * 32 + nq * 8), __builtin_bit_cast(unsigned long long, t), __ATOMIC_RELAXED, __HIP_MEMORY_SCOPE_AGENT);
    }
    STAMPK(3, 3);
    publish(FL_X1 + (rt2 >> 1));
    RT(3, 1);
    STAMPK(3, 4);
  }

  bf16x8 W[7][4];
  const size_t woff = WS_W4T + (size_t)(ns * 56 + wid * 7) * 4096 + fo;
  auto rot7 = [&](int s) AI { const int t = s + (ROT ? (ns < 7 ? ns : 0) : 0); return t >= 7 ? t - 7 : t; };
  RT(4, 0);
  STAMPK(4, 0);
  {
    if (wid < 4) {
#pragma unroll
      for (int s = 0; s < 16; ++s) { A[s][0] = ldf1(w3off, (s * 4) * 1024); A[s][1] = ldf1(w3off, (s * 4 + 1) * 1024); }
    }
    SCHED_FENCE();
    await(7, FL_X1 + rb2, 8);
    STAMPK(4, 1);
    u32x4 xv[8];
    const u32x4 stv = ld1(WS_ST + (size_t)(rb2 * 64 + wid * 8) * 32 + (lane & 15) * 16, 0);
#pragma unroll
    for (int i = 0; i < 8; ++i) xv[i] = ld1(WS_X1 + (size_t)(rb2 * 64 + wid * 8) * 1024 + fo, i * 1024);
    SCHED_FENCE();
    const float sm = dpp_add<0xB1>(__uint_as_float(stv[0]) + __uint_as_float(stv[2])) * (1.f / 512.f);
    const float sq = dpp_add<0xB1>(__uint_as_float(stv[1]) + __uint_as_float(stv[3])) * (1.f / 512.f);
    const float rsl = __builtin_amdgcn_rsqf(fmaxf(sq - sm * sm, 0.f) + 1e-5f), nml = -sm * rsl;
    float rs[8], nm[8];
#pragma unroll
    for (int i = 0; i < 8; ++i) { rs[i] = __uint_as_float(__builtin_amdgcn_readlane(__float_as_uint(rsl), 2 * i)); nm[i] = __uint_as_float(__builtin_amdgcn_readlane(__float_as_uint(nml), 2 * i)); }
    SCHED_FENCE();
#pragma unroll
    for (int hf = 0; hf < 2; ++hf) {
      float lo[16], hi[16]; unsigned t[16];
      H_STAGE(lo[j] = bflo(xv[i][d]); hi[j] = bfhi(xv[i][d]));
      H_STAGE(lo[j] = __builtin_fmaf(lo[j], rs[i], nm[i]); hi[j] = __builtin_fmaf(hi[j], rs[i], nm[i]));
      H_STAGE(t[j] = pk2(lo[j], hi[j]));
#pragma unroll
      for (int i = 0; i < 4; ++i) *(u32x4*)(lds + (wid * 8 + hf * 4 + i) * HT_STRIDE + fo) = (u32x4){t[4 * i], t[4 * i + 1], t[4 * i + 2], t[4 * i + 3]};
      SCHED_FENCE();
    }
    STAMPK(4, 2);
    asm volatile("s_waitcnt lgkmcnt(0)" ::: "memory"); __builtin_amdgcn_s_barrier(); MEM_FENCE();
    STAMPK(4, 3);
    if (wid < 7) {
      f32x4 acc[2][2][2];
      if (wid >= 4) __builtin_amdgcn_s_setprio(1);
      const size_t dst = WS_U + (size_t)(rb2 * 4 * 56 + ng) * 1024 + fo;
      u32x4 ow;
      auto gpart = [&](int ps, int k) AI {
        unsigned w = pk2(gelu_tanh(acc[ps][0][k >> 2][k & 3]), gelu_tanh(acc[ps][1][k >> 2][k & 3])); PIN(w); ow[k & 3] = w;
        if ((k & 3) == 3) st1(dst + (size_t)(2 * ps + (k >> 2)) * 56 * 1024, ow);
      };
      gemm_steps<2, 2, 32, 16, 2, 3>(acc, A, [&](int s, int i) AI { return A[s & 15][i]; },
                                     [&](int s, int qt) AI { return *(const bf16x8*)(bl + ((s >> 4) * 2 + qt) * 16 * HT_STRIDE + 64 * (s & 15)); },
                                     [&](auto S_, auto M_) AI {
                                       constexpr int s = S_.value, m = M_.value;
                                       if constexpr (s == PRIO_G3 && m == 0) __builtin_amdgcn_s_setprio(0);
                                       if constexpr (s >= 17 && (s & 1) && m == 1) gpart(0, (s - 17) >> 1);
                                       if constexpr (s >= 17 && s < 31 && m >= 2) { constexpr int k = (s - 17) * 2 + m - 2; W[k >> 2][k & 3] = ldf1(woff + (k & 3) * 1024, rot7(k >> 2) * 4096); }
                                     });
      STAMPK(4, 4);
#pragma unroll
      for (int k = 0; k < 8; ++k) gpart(1, k);
    } else {
#pragma unroll
      for (int k = 0; k < 28; ++k) W[k >> 2][k & 3] = ldf1(woff + (k & 3) * 1024, rot7(k >> 2) * 4096);
    }
    STAMPK(4, 5);
    publish(FL_U + rb2);
    RT(4, 1);
    STAMPK(4, 6);
  }

  RT(5, 0);
  STAMPK(5, 0);
  {
    bf16x8 B[7][4];
    const size_t uoff = WS_U + (size_t)(rb2 * 4 * 56 + wid * 7) * 1024 + fo;
    const int oqt = wid >> 1, orp = wid & 1, orow = rb2 * 64 + oqt * 16 + a, ocol = ns * 64 + g4 * 16 + orp * 8;
    u32x2 x1a, x1b; u32x4 x2v, gbv;
    SCHED_FENCE();
    await(0, FL_U + rb2, 8);
#pragma unroll
    for (int s = 0; s < G4_AHEAD; ++s)
#pragma unroll
      for (int i = 0; i < 4; ++i) B[s][i] = ldf1(uoff, (i * 56 + rot7(s)) * 1024);
    SCHED_FENCE();
    STAMPK(5, 1);
    f32x4 acc[4][4] = {};
#pragma unroll
    for (int s = 0; s < 7; ++s) {
#pragma unroll
      for (int i = 0; i < 4; ++i) {
#pragma unroll
        for (int qt = 0; qt < 4; ++qt) acc[i][qt] = MFMA16(W[s][i], B[s][qt], acc[i][qt]);
        if (s + G4_AHEAD < 7) B[s + G4_AHEAD][i] = ldf1(uoff, (i * 56 + rot7(s + G4_AHEAD)) * 1024);
        SCHED_FENCE();
      }
      if (s == 5) {
        const size_t x1o = WS_X1 + (size_t)orow * 1024 + ns * 128 + g4 * 16 + orp * 8;
        x1a = __builtin_amdgcn_raw_buffer_load_b64(rWs, (unsigned)x1o, 0, 16); x1b = __builtin_amdgcn_raw_buffer_load_b64(rWs, (unsigned)x1o, 64, 16);
        x2v = ld1(WS_X2 + (size_t)orow * 1024 + ocol * 2, 0); gbv = *(const u32x4*)(modb + 2560 + ocol);
        SCHED_FENCE();
      }
    }
    STAMPK(5, 2);
#pragma unroll
    for (int qt = 0; qt < 4; ++qt)
#pragma unroll
      for (int rg = 0; rg < 4; ++rg)
        *(f32x4*)(lds + ((wid * 4 + qt) * 4 + rg) * 1024 + fo) = (f32x4){acc[0][qt][rg], acc[1][qt][rg], acc[2][qt][rg], acc[3][qt][rg]};
    STAMPK(5, 3);
    __syncthreads();
    STAMPK(5, 4);
    f32x4 s0 = {}, s1 = {};
#pragma unroll
    for (int w = 0; w < 8; ++w) {
      s0 += *(const f32x4*)(lds + ((w * 4 + oqt) * 4 + 2 * orp) * 1024 + fo);
      s1 += *(const f32x4*)(lds + ((w * 4 + oqt) * 4 + 2 * orp + 1) * 1024 + fo);
    }
    const float h2[8] = {s0[0], s0[1], s0[2], s0[3], s1[0], s1[1], s1[2], s1[3]};
    const unsigned x1v[4] = {x1a[0], x1b[0], x1a[1], x1b[1]};
    u32x4 ov;
#pragma unroll
    for (int e = 0; e < 4; ++e)
      ov[e] = pk2(bflo(x2v[e]) + bflo(gbv[e]) * (bflo(x1v[e]) + h2[2 * e]), bfhi(x2v[e]) + bfhi(gbv[e]) * (bfhi(x1v[e]) + h2[2 * e + 1]));
    *(u32x4*)(p.out + (size_t)orow * D_ + ocol) = ov;
    STAMPK(5, 5);
    RT(5, 1);
    STAMPK(5, 6);
  }
}

extern "C" void kernel_launch(void* const* d_in, const int* in_sizes, int n_in, void* d_out, int out_size, void* d_ws, size_t ws_size, hipStream_t stream) {
  char* ws = (char*)d_ws;
  K0Args p0;
  p0.x = (const u16*)d_in[0]; p0.c = (const u16*)d_in[10]; p0.wmod = (const u16*)d_in[11]; p0.w1 = (const u16*)d_in[6]; p0.kc = (const u16*)d_in[1]; p0.vc = (const u16*)d_in[2]; p0.ws = ws;
  K1Args p1;
  p1.x = p0.x; p1.wq = (const u16*)d_in[3]; p1.kc = (const u16*)d_in[1]; p1.vc = (const u16*)d_in[2]; p1.wo = (const u16*)d_in[4];
  p1.w2 = (const u16*)d_in[7]; p1.w3 = (const u16*)d_in[8]; p1.w4 = (const u16*)d_in[9]; p1.lnst = (const float*)(ws + WS_Y); p1.ws = ws; p1.out = (u16*)d_out;
  (void)hipFuncSetAttribute((const void*)k1_attn, hipFuncAttributeMaxDynamicSharedMemorySize, L1_END);
  k0_prep<<<K0_BLOCKS, 512, 0, stream>>>(p0);
  k1_attn<<<256, 512, L1_END, stream>>>(p1);
}
```

```cpp
#include <hip/hip_runtime.h>
#include <hip/hip_bf16.h>
#include <type_traits>
#include <utility>

typedef unsigned short u16;
typedef __bf16 bf16x8 __attribute__((ext_vector_type(8)));
typedef __bf16 bf16x4 __attribute__((ext_vector_type(4)));
typedef __bf16 bf16x2 __attribute__((ext_vector_type(2)));
typedef float f32x16 __attribute__((ext_vector_type(16)));
typedef float f32x4 __attribute__((ext_vector_type(4)));
typedef float f32x2 __attribute__((ext_vector_type(2)));
typedef unsigned u32x2 __attribute__((ext_vector_type(2)));
typedef unsigned u32x4 __attribute__((ext_vector_type(4)));
typedef __amdgpu_buffer_rsrc_t rsrc_t;
#define DI __device__ __forceinline__
#define AI __attribute__((always_inline))
#define LDSP(T, p) ((__attribute__((address_space(3))) T*)(p))
#define SCHED_FENCE() __builtin_amdgcn_sched_barrier(0)
#define PIN(x) asm volatile("" : "+v"(x))
#define MEM_FENCE() asm volatile("" ::: "memory")
#define WAIT_VM0() __builtin_amdgcn_s_waitcnt(0x0F70)
#define STAMP(i)
#define STAMPK(k, i)
#define RT(k, i)

#define ROUND_S 0
#define NT_SRC 1
#define K0_WARM 3
constexpr int S_ = 2048, D_ = 512, HQ = 32, HKV = 8, DH = 128, SK = 2048, WIN = 512, F_ = 1792;

constexpr size_t NXB = (size_t)S_ * D_ * 2;
constexpr size_t WS_MOD = 0;
constexpr size_t WS_Y   = 8192;
constexpr size_t WS_X2  = WS_Y  + NXB;
constexpr size_t WS_HB  = WS_X2 + NXB;
constexpr size_t WS_H1  = WS_HB + NXB;
constexpr size_t WS_X1  = WS_H1 + NXB;
constexpr size_t WS_U   = WS_X1 + NXB;
constexpr size_t WS_PART = WS_U + (size_t)S_ * F_ * 2;
constexpr size_t WS_WQT = WS_PART + 8 * NXB;
constexpr size_t WS_WOT = WS_WQT + (size_t)D_ * 4096 * 2;
constexpr size_t WS_W1T = WS_WOT + (size_t)D_ * 4096 * 2;
constexpr size_t WS_W2T = WS_W1T + (size_t)D_ * D_ * 2;
constexpr size_t WS_W3T = WS_W2T + (size_t)D_ * D_ * 2;
constexpr size_t WS_W4T = WS_W3T + (size_t)D_ * F_ * 2;
constexpr size_t WS_ST  = WS_W4T + (size_t)D_ * F_ * 2;
constexpr size_t WS_FLAG = WS_ST + (size_t)S_ * 32;
constexpr int FL_W = 0, FL_P = 8, FL_H1 = 40, FL_X1 = 168, FL_U = 200, FL_P0 = 232, FL_N = 264;
constexpr size_t WS_END = WS_FLAG + FL_N * 128;

DI float bflo(unsigned u) { return __uint_as_float(u << 16); }
DI float bfhi(unsigned u) { return __uint_as_float(u & 0xffff0000u); }
DI unsigned pk2(float lo, float hi) { f32x2 v = {lo, hi}; return __builtin_bit_cast(unsigned, __builtin_convertvector(v, bf16x2)); }
DI float rb(float x) { return __uint_as_float(pk2(0.f, x)); }
DI rsrc_t mk_rsrc(const void* p, unsigned bytes) { return __builtin_amdgcn_make_buffer_rsrc((void*)p, (short)0, (int)bytes, 0x00020000); }
DI unsigned rfl(unsigned x) { return __builtin_amdgcn_readfirstlane(x); }
DI bf16x8 ldfrag(rsrc_t r, unsigned voff, unsigned soff) { return __builtin_bit_cast(bf16x8, __builtin_amdgcn_raw_buffer_load_b128(r, voff, soff, 0)); }
template <int CTRL> DI float dpp_add(float v) {
  return v + __uint_as_float(__builtin_amdgcn_update_dpp(0u, __float_as_uint(v), CTRL, 0xf, 0xf, true));
}
DI float swap16_add(float v) { auto r = __builtin_amdgcn_permlane16_swap(__float_as_uint(v), __float_as_uint(v), false, false); return __uint_as_float(r[0]) + __uint_as_float(r[1]); }
DI float swap32_add(float v) { auto r = __builtin_amdgcn_permlane32_swap(__float_as_uint(v), __float_as_uint(v), false, false); return __uint_as_float(r[0]) + __uint_as_float(r[1]); }
DI float swap32_max(float v) { auto r = __builtin_amdgcn_permlane32_swap(__float_as_uint(v), __float_as_uint(v), false, false); return fmaxf(__uint_as_float(r[0]), __uint_as_float(r[1])); }
DI float wave_sum(float v) {
  v = dpp_add<0xB1>(v); v = dpp_add<0x4E>(v); v = dpp_add<0x141>(v); v = dpp_add<0x140>(v);
  v = swap16_add(v); v = swap32_add(v); return v;
}
DI float div_rn(float a, float d, float r) {
  float q = a * r; float e = __builtin_fmaf(-q, d, a); return __builtin_fmaf(e, r, q);
}
DI float gelu_tanh(float x) {
  const float u = 0.7978845608028654f * (x + 0.044715f * x * x * x);
  return x * __builtin_amdgcn_rcpf(1.f + __builtin_amdgcn_exp2f(-2.885390081777927f * u));
}
DI void ln_consts(float mean, float var, float& m_bf, float& d_bf, float& r) {
  m_bf = rb(mean); d_bf = rb(__builtin_sqrtf(rb(rb(var) + 1e-5f))); r = __builtin_amdgcn_rcpf(d_bf);
}
DI float ln_apply(float x, float m_bf, float d_bf, float r) { return rb(div_rn(rb(x - m_bf), d_bf, r)); }
DI u32x4 ln_row(const u32x4 xv) {
  float f[8];
#pragma unroll
  for (int e = 0; e < 4; ++e) { f[2 * e] = bflo(xv[e]); f[2 * e + 1] = bfhi(xv[e]); }
  float s = 0;
#pragma unroll
  for (int e = 0; e < 8; ++e) s += f[e];
  const float mean = wave_sum(s) * (1.f / 512.f);
  float q = 0;
#pragma unroll
  for (int e = 0; e < 8; ++e) { float d = f[e] - mean; q += d * d; }
  const float var = wave_sum(q) * (1.f / 512.f);
  float mb, db, r; ln_consts(mean, var, mb, db, r);
  u32x4 o;
#pragma unroll
  for (int e = 0; e < 4; ++e) o[e] = pk2(div_rn(rb(f[2 * e] - mb), db, r), div_rn(rb(f[2 * e + 1] - mb), db, r));
  return o;
}

#define MFMA16(a, b, c) __builtin_amdgcn_mfma_f32_16x16x32_bf16((a), (b), (c), 0, 0, 0)
#define MFMA32(a, b, c) __builtin_amdgcn_mfma_f32_32x32x16_bf16((a), (b), (c), 0, 0, 0)

DI void dma16(unsigned lds_dst, unsigned voff, rsrc_t r, unsigned soff) {
  asm volatile("s_mov_b32 m0, %0\n\ts_nop 0\n\tbuffer_load_dwordx4 %1, %2, %3 offen lds" ::"s"(lds_dst), "v"(voff), "s"(r), "s"(soff) : "memory");
}

struct Retile { u32x2 d[8]; };
DI void retile_load(Retile& t, const u16* __restrict__ W, int ldw, int k0, bool perm, int n0, int lane) {
  const int a = lane & 15, g4 = lane >> 4;
  const u16* p = W + (size_t)(k0 + (perm ? 16 : 8) * g4) * ldw + n0 + 4 * a;
  const int s2 = (perm ? 4 : 2) * ldw;
#pragma unroll
  for (int j = 0; j < 8; ++j) {
    const u32x2* q = (const u32x2*)(p + (j >> 1) * s2 + (j & 1) * ldw);
    t.d[j] = NT_SRC ? __builtin_nontemporal_load(q) : *q;
  }
}
DI u32x4 retile_frag(const Retile& t, int i) {
  u32x4 f;
#pragma unroll
  for (int p = 0; p < 4; ++p) f[p] = __builtin_amdgcn_perm(t.d[2 * p + 1][i >> 1], t.d[2 * p][i >> 1], (i & 1) ? 0x07060302u : 0x05040100u);
  return f;
}

struct K0Args { const u16 *x, *c, *wmod, *wq, *w1, *kc, *vc; char* ws; };
constexpr int K0_BLOCKS = 304 + 128 + 16;
__global__ __launch_bounds__(512) void k0_prep(const K0Args p) {
  __shared__ float red[8][64];
  const int tid = threadIdx.x, lane = tid & 63, wid = tid >> 6;
  int b = blockIdx.x;
#define WSP p.ws
  RT(0, 0);
  if (b < 48) {
    const int n0 = b * 64, cg = tid & 7, kq = tid >> 3;
    u32x4 wv[8]; float sv[8];
#pragma unroll
    for (int i = 0; i < 8; ++i) { const u32x4* q = (const u32x4*)(p.wmod + (size_t)(kq + 64 * i) * 3072 + n0 + cg * 8); wv[i] = NT_SRC ? __builtin_nontemporal_load(q) : *q; }
#pragma unroll
    for (int i = 0; i < 8; ++i) { float cv = bflo(p.c[kq + 64 * i]); sv[i] = rb(cv / (1.f + __expf(-cv))); }
    float acc[8] = {};
#pragma unroll
    for (int i = 0; i < 8; ++i)
#pragma unroll
      for (int e = 0; e < 4; ++e) { acc[2 * e] += sv[i] * bflo(wv[i][e]); acc[2 * e + 1] += sv[i] * bfhi(wv[i][e]); }
#pragma unroll
    for (int e = 0; e < 8; ++e) { float v = acc[e]; v += __shfl_xor(v, 8); v = swap16_add(v); v = swap32_add(v); acc[e] = v; }
    if (lane < 8)
#pragma unroll
      for (int e = 0; e < 8; ++e) red[wid][lane * 8 + e] = acc[e];
    __syncthreads();
    if (tid < 64) {
      float s = 0;
#pragma unroll
      for (int w = 0; w < 8; ++w) s += red[w][tid];
      s = rb(s);
      const int n = n0 + tid, seg = n >> 9;
      if (seg == 1 || seg == 4) s = rb(1.f + s);
      ((u16*)(p.ws + WS_MOD))[n] = (u16)(__float_as_uint(s) >> 16);
    }
    RT(0, 1);
    return;
  }
  b -= 48;
  if (b < 256) {
    const int row = b * 8 + wid;
    const u32x4 xv = *(const u32x4*)(p.x + (size_t)row * D_ + lane * 8);
    u32x4 wv[8];
#pragma unroll
    for (int i = 0; i < 8; ++i) wv[i] = *(const u32x4*)(p.x + (size_t)((b >> 3) * 64 + wid * 8 + i) * D_ + lane * 8);
    float f[8];
#pragma unroll
    for (int e = 0; e < 4; ++e) { f[2 * e] = bflo(xv[e]); f[2 * e + 1] = bfhi(xv[e]); }
    float s = 0;
#pragma unroll
    for (int e = 0; e < 8; ++e) s += f[e];
    const float mean = wave_sum(s) * (1.f / 512.f);
    float q = 0;
#pragma unroll
    for (int e = 0; e < 8; ++e) { float d = f[e] - mean; q += d * d; }
    const float var = wave_sum(q) * (1.f / 512.f);
    float mb, db, r; ln_consts(mean, var, mb, db, r);
    if (lane == 0) *(f32x2*)(p.ws + WS_Y + (size_t)row * 8) = (f32x2){mb, db};
#pragma unroll
    for (int i = 0; i < 8; ++i) asm volatile("" ::"v"(wv[i]));
    RT(0, 1);
    return;
  }
  b -= 256;
  Retile t; char* dst;
  if (b < 128) { const int g = b & 7, s = b >> 3, ng = g * 8 + wid; retile_load(t, p.wq, 4096, 32 * s, false, 64 * ng, lane); dst = p.ws + WS_WQT + (size_t)(ng * 16 + s) * 4096; }
  else {
    const int s = b - 128; retile_load(t, p.w1, 512, 32 * s, false, 64 * wid, lane); dst = p.ws + WS_W1T + (size_t)(wid * 16 + s) * 4096;
    if (s == 0 && tid < FL_N) *(unsigned*)(p.ws + WS_FLAG + tid * 128) = 0u;
  }
#pragma unroll
  for (int i = 0; i < 4; ++i) *(u32x4*)(dst + i * 1024 + lane * 16) = retile_frag(t, i);
  RT(0, 1);
#undef WSP
#define WSP ws
}

template <int V> using IC = std::integral_constant<int, V>;
template <int N, class F, int... Is> DI void sfor_(F&& f, std::integer_sequence<int, Is...>) { (f(IC<Is>{}), ...); }
template <int N, class F> DI void sfor(F&& f) { sfor_<N>(f, std::make_integer_sequence<int, N>{}); }
template <int NI, int NQT, int NSTEP, int DEPTH, int NSET, int BD = 1, class ALoad, class BRead, class Hook>
DI void gemm_steps(f32x4 (&acc)[NSET][NI][NQT], bf16x8 (&ring)[DEPTH][NI], ALoad aload, BRead bread, Hook hook) {
  constexpr int NM = NI * NQT, PER = NSTEP / NSET;
  bf16x8 B[BD + 1][NQT];
#pragma unroll
  for (int s = 0; s < BD; ++s)
#pragma unroll
    for (int qt = 0; qt < NQT; ++qt) B[s][qt] = bread(s, qt);
  SCHED_FENCE();
  sfor<NSTEP * NM>([&](auto X) AI {
    constexpr int s = X.value / NM, m = X.value % NM, i = m / NQT, qt = m % NQT;
    if constexpr (s % PER == 0) acc[s / PER][i][qt] = MFMA16(ring[s % DEPTH][i], B[s % (BD + 1)][qt], (f32x4){});
    else                        acc[s / PER][i][qt] = MFMA16(ring[s % DEPTH][i], B[s % (BD + 1)][qt], acc[s / PER][i][qt]);
    if constexpr (m < NQT && s + BD < NSTEP) B[(s + BD) % (BD + 1)][m] = bread(s + BD, m);
    if constexpr (qt == NQT - 1 && s + DEPTH < NSTEP) ring[s % DEPTH][i] = aload(s + DEPTH, i);
    hook(IC<s>{}, IC<m>{});
    SCHED_FENCE();
  });
}

constexpr int HT_STRIDE = 1056, QX_STRIDE = 272;
constexpr int L1_HT = 0, L1_K = 69632, L1_V = L1_K + 3 * 16384, L1_END = L1_V + 2 * 16384;
#define PRIO_G3 21
#define G4_AHEAD 2
#define ROT 1
#define P0_STEP 28
#define PRIO_P 1
#define PRIO_Q 11
#define PRIO_O 21
#define PRIO_A 0
#define GBAR 1
#define WO_SC1 1
#define OPASS 2
#define PF_ON 1
#define RT_AUX 16
#define PART_AUX 16
constexpr float SM_C = 0.12751743082459868f;
constexpr float SM_LIMIT = 1099511627776.f;

DI float sm_arg(float s, float negmc) {
  return __builtin_fmaf(s, SM_C, negmc);
}
DI float max16(const f32x16& S) {
  float m = fmaxf(S[0], S[1]);
#pragma unroll
  for (int r = 2; r < 16; ++r) m = fmaxf(m, S[r]);
  return m;
}
DI unsigned scale_pk(unsigned p, float f) { return pk2(bflo(p) * f, bfhi(p) * f); }
DI void sm_half(const f32x16& S, float negmc, float& l0, float& l1, u32x4& pa, u32x4& pb) {
#pragma unroll
  for (int p = 0; p < 4; ++p) {
    const float e0 = __builtin_amdgcn_exp2f(sm_arg(S[2 * p], negmc)), e1 = __builtin_amdgcn_exp2f(sm_arg(S[2 * p + 1], negmc));
    l0 += e0; l1 += e1; pa[p] = pk2(e0, e1);
  }
#pragma unroll
  for (int p = 0; p < 4; ++p) {
    const float e0 = __builtin_amdgcn_exp2f(sm_arg(S[8 + 2 * p], negmc)), e1 = __builtin_amdgcn_exp2f(sm_arg(S[8 + 2 * p + 1], negmc));
    l0 += e0; l1 += e1; pb[p] = pk2(e0, e1);
  }
}
struct SmPipe { float r[16], x[16], e[16]; };
DI void sm_gap(const int m, SmPipe& sp, const f32x16& S, float negmc, float& l0, float& l1, u32x4& pa, u32x4& pb) {
  constexpr int D = ROUND_S ? 1 : 0, SPAN = 13 - D;
#pragma unroll
  for (int e = 0; e < 16; ++e) {
    const int f = e * SPAN / 16 + D;
    if (f + 2 == m) {
      if (e & 1) {
        l1 += sp.e[e]; PIN(l1);
        unsigned w = pk2(sp.e[e - 1], sp.e[e]); PIN(w);
        if (e < 8) pa[e >> 1] = w; else pb[(e - 8) >> 1] = w;
      } else { l0 += sp.e[e]; PIN(l0); }
    }
  }
#pragma unroll
  for (int e = 0; e < 16; ++e) if (e * SPAN / 16 + D + 1 == m) sp.e[e] = __builtin_amdgcn_exp2f(sp.x[e]);
#pragma unroll
  for (int e = 0; e < 16; ++e) if (e * SPAN / 16 + D == m) {
    if (ROUND_S == 0) sp.x[e] = __builtin_fmaf(S[e], SM_C, negmc);
    else if (ROUND_S == 1) sp.x[e] = __builtin_fmaf(sp.r[e], SM_C, negmc);
    else sp.x[e] = __builtin_fmaf(rb(sp.r[e]), 1.4426950408889634f, negmc);
  }
  if (ROUND_S) {
#pragma unroll
    for (int e = 0; e < 16; ++e) if (e * SPAN / 16 == m) sp.r[e] = (ROUND_S == 1) ? rb(S[e]) : rb(S[e]) * 0.08838834764831845f;
  }
}
DI void pack_acc16(const f32x4 (&acc)[4][4], int qt, u32x4& w0, u32x4& w1) {
#pragma unroll
  for (int rg = 0; rg < 2; ++rg) {
    w0[2 * rg] = pk2(acc[0][qt][rg], acc[1][qt][rg]); w0[2 * rg + 1] = pk2(acc[2][qt][rg], acc[3][qt][rg]);
    w1[2 * rg] = pk2(acc[0][qt][2 + rg], acc[1][qt][2 + rg]); w1[2 * rg + 1] = pk2(acc[2][qt][2 + rg], acc[3][qt][2 + rg]);
  }
}

typedef __attribute__((address_space(1))) unsigned gu32;
typedef __attribute__((address_space(1))) unsigned long long gu64;
constexpr unsigned SPIN_CAP = 1u << 20;
struct K1Args { const u16 *x, *kc, *vc, *wo, *w2, *w3, *w4; const float* lnst; char* ws; u16* out; };

__global__ __launch_bounds__(512, 2) __attribute__((amdgpu_waves_per_eu(2, 2)))
void k1_attn(const K1Args p) {
  extern __shared__ __attribute__((aligned(16))) char lds[];
  char* __restrict__ ws = p.ws;
  const int tid = threadIdx.x, lane = tid & 63;
  const int wid = (int)rfl((unsigned)tid >> 6);
  const int g = blockIdx.x & 7, rbk = blockIdx.x >> 3;
  const int a = lane & 15, g4 = lane >> 4, r32 = lane & 31, hh = lane >> 5;
  const unsigned ldsb = (unsigned)(size_t)lds;
  const unsigned fo = lane * 16;
  RT(1, 0);
  STAMP(0);
  const rsrc_t rK = mk_rsrc(p.kc + ((size_t)g * SK + (SK - WIN)) * DH, WIN * DH * 2), rV = mk_rsrc(p.vc + ((size_t)g * SK + (SK - WIN)) * DH, WIN * DH * 2);
  const unsigned drow = wid * 8 + ((lane >> 2) & 7);
  const unsigned kvoff = drow * 256 + (lane >> 5) * 64 + (((lane & 3) ^ ((drow >> 2) & 3)) * 16);
  const unsigned vvoff = drow * 256 + (lane >> 5) * 64 + (lane & 3) * 16;
  const unsigned kdst = ldsb + L1_K + wid * 2048, vdst = ldsb + L1_V + wid * 2048;
  auto dmaK = [&](int t) AI { dma16(kdst + (t % 3) * 16384, kvoff, rK, t * 16384); dma16(kdst + (t % 3) * 16384 + 1024, kvoff, rK, t * 16384 + 128); };
  auto dmaV = [&](int t) AI { dma16(vdst + (t & 1) * 16384, vvoff, rV, t * 16384); dma16(vdst + (t & 1) * 16384 + 1024, vvoff, rV, t * 16384 + 128); };
  constexpr int DEPTH = 6;
  bf16x8 ring[DEPTH][4];
  const rsrc_t rWq = mk_rsrc(ws + WS_WQT + (size_t)(g * 8 + wid) * 65536, 65536);
  const int rot = ROT ? (rbk & 15) : 0;
  auto wq_load = [&](int s, int i) AI { return ldfrag(rWq, fo + i * 1024, ((s + rot) & 15) * 4096); };
  {
    const u32x4 sav = *(const u32x4*)(ws + WS_MOD + fo), cav = *(const u32x4*)(ws + WS_MOD + 1024 + fo);
    u32x4 xv[8];
#pragma unroll
    for (int i = 0; i < 8; ++i) xv[i] = *(const u32x4*)((const char*)p.x + (size_t)(rbk * 64 + wid * 8 + i) * 1024 + fo);
    float st[16];
#pragma unroll
    for (int i = 0; i < 16; ++i) st[i] = p.lnst[(rbk * 64 + wid * 8) * 2 + i];
    SCHED_FENCE();
    if (K0_WARM == 3) dma16(kdst, fo, wid < 4 ? rK : rV, (rbk * 4 + (wid & 3)) * 1024);
    SCHED_FENCE();
    float ca[8], sa[8];
#pragma unroll
    for (int e = 0; e < 4; ++e) { ca[2 * e] = bflo(cav[e]); ca[2 * e + 1] = bfhi(cav[e]); sa[2 * e] = bflo(sav[e]); sa[2 * e + 1] = bfhi(sav[e]); }
    if (PRIO_P && wid >= 4) __builtin_amdgcn_s_setprio(1);
#pragma unroll
    for (int hf = 0; hf < 2; ++hf) {
      float lo[16], hi[16], rr[4]; unsigned t[16];
#define H_STAGE(body) _Pragma("unroll") for (int j = 0; j < 16; ++j) { const int i = hf * 4 + (j >> 2), d = j & 3; (void)i; (void)d; body; } SCHED_FENCE()
#pragma unroll
      for (int i = 0; i < 4; ++i) rr[i] = __builtin_amdgcn_rcpf(st[2 * (hf * 4 + i) + 1]);
      H_STAGE(lo[j] = bflo(xv[i][d]); hi[j] = bfhi(xv[i][d]));
      H_STAGE(lo[j] -= st[2 * i]; hi[j] -= st[2 * i]);
      H_STAGE(lo[j] = rb(lo[j]); hi[j] = rb(hi[j]));
      if (PRIO_P && hf == 1) __builtin_amdgcn_s_setprio(0);
      H_STAGE(lo[j] *= rr[j >> 2]; hi[j] *= rr[j >> 2]);
      H_STAGE(lo[j] = rb(lo[j]); hi[j] = rb(hi[j]));
#pragma unroll
      for (int i = 0; i < 4; ++i) ring[hf][i] = wq_load(hf, i);
      H_STAGE(lo[j] *= ca[2 * d]; hi[j] *= ca[2 * d + 1]);
      H_STAGE(lo[j] = rb(lo[j]); hi[j] = rb(hi[j]));
      H_STAGE(lo[j] += sa[2 * d]; hi[j] += sa[2 * d + 1]);
      H_STAGE(t[j] = pk2(lo[j], hi[j]));
#pragma unroll
      for (int i = 0; i < 4; ++i) *(u32x4*)(lds + L1_HT + (wid * 8 + hf * 4 + i) * HT_STRIDE + fo) = (u32x4){t[4 * i], t[4 * i + 1], t[4 * i + 2], t[4 * i + 3]};
#pragma unroll
      for (int i = 0; i < 2; ++i) ring[2][hf * 2 + i] = wq_load(2, hf * 2 + i);
      SCHED_FENCE();
    }
  }

  Retile rtl; const u16* rsrc_w; int rld, rk0, rn0; bool rperm; size_t rdst; unsigned rhalf = 2048;
  if (wid < 4) { const int id = rbk * 4 + wid, s = id >> 3, ng = id & 7; rsrc_w = p.wo; rld = 512; rk0 = g * 512 + 32 * s; rn0 = 64 * ng; rperm = false; rdst = WS_WOT + (size_t)(g * 8 + ng) * 65536 + s * 2048; rhalf = 32768; }
  else {
    int id = blockIdx.x * 4 + wid - 4, s, ng; rperm = true;
    if (id < 128)      { ng = id >> 4; s = id & 15; rsrc_w = p.w2; rld = 512; rdst = WS_W2T + (size_t)id * 4096; }
    else if (id < 576) { id -= 128; ng = id >> 4; s = id & 15; rsrc_w = p.w3; rld = F_; rdst = WS_W3T + (size_t)id * 4096; }
    else               { id -= 576; ng = id & 7; s = id >> 3; rsrc_w = p.w4; rld = 512; rdst = WS_W4T + (size_t)(ng * 56 + s) * 4096; }
    rk0 = 64 * (s >> 1) + 2 * (s & 1); rn0 = 64 * ng;
  }
  gu32* flags = (gu32*)(ws + WS_FLAG);
  gu32* flag = flags + (FL_W + g) * 32;
  gu32* flagw = flags + (FL_W + (lane & 7)) * 32;
  STAMP(1);
  __syncthreads();
  STAMP(2);
  if (wid >= 4 && PRIO_Q > 0) __builtin_amdgcn_s_setprio(1);

  const char* bl = lds + L1_HT + a * HT_STRIDE + g4 * 16;
  auto bread = [&](int s, int qt) AI { return *(const bf16x8*)(bl + 64 * ((s + rot) & 15) + qt * 16 * HT_STRIDE); };
  f32x16 o[4]; bf16x8 qf[8]; float l0, l1, negmc;
  {
    f32x4 acc[1][4][4];
    gemm_steps<4, 4, 16, DEPTH, 1>(acc, ring, wq_load, bread, [&](auto S_, auto M_) AI {
      constexpr int s = S_.value, m = M_.value;
      if constexpr (s < 3 && (m & 3) == 1) ring[3 + s][m >> 2] = wq_load(3 + s, m >> 2);
      if constexpr (s == PRIO_Q && m == 0) __builtin_amdgcn_s_setprio(0);
      if constexpr (s == 11 && m == 8) { WAIT_VM0(); dmaK(0); }
      if constexpr (s == 12 && m == 0) dmaK(1);
      if constexpr (s == 12 && m == 8) dmaV(0);
      if constexpr (s == 13 && m == 0) dmaK(2);
      if constexpr (s == 13 && m == 8) dmaV(1);
    });
    STAMP(3);
    __syncthreads();
#pragma unroll
    for (int qt = 0; qt < 4; ++qt) {
      u32x4 w0, w1; pack_acc16(acc[0], qt, w0, w1);
      char* dst = lds + L1_HT + ((wid >> 1) * 64 + qt * 16 + a) * QX_STRIDE + ((wid & 1) * 64 + g4 * 16) * 2;
      *(u32x4*)dst = w0; *(u32x4*)(dst + 16) = w1;
    }
    asm volatile("s_waitcnt vmcnt(0)" ::: "memory");
    __syncthreads();
#pragma unroll
    for (int s = 0; s < 8; ++s) qf[s] = *(const bf16x8*)(lds + L1_HT + ((wid >> 1) * 64 + (wid & 1) * 32 + r32) * QX_STRIDE + 32 * s + 16 * hh);
  }
  STAMP(4);

  const unsigned kx = (r32 >> 2) & 3;
  const char* kb0 = lds + L1_K + 2048 * (r32 >> 3) + 64 * (r32 & 7) + 16 * ((0 + hh) ^ kx);
  const char* kb1 = lds + L1_K + 2048 * (r32 >> 3) + 64 * (r32 & 7) + 16 * ((2 + hh) ^ kx);
  const char* vb = lds + L1_V + (4 * hh + ((lane >> 2) & 3)) * 64 + ((lane >> 4) & 1) * 32 + (lane & 3) * 8;
  auto kfrag = [&](int t, int m) AI { return *(const bf16x8*)(((m & 1) ? kb1 : kb0) + (t % 3) * 16384 + 512 * ((m & 7) >> 1) + 8192 * (m >> 3)); };
  auto vfrag = [&](int t, int m) AI {
    const char* q = vb + (t & 1) * 16384 + (m >> 2) * 4096 + (m & 3) * 512;
    bf16x4 lo = __builtin_amdgcn_ds_read_tr16_b64_v4bf16(LDSP(bf16x4, q)), hi = __builtin_amdgcn_ds_read_tr16_b64_v4bf16(LDSP(bf16x4, q + 2048));
    return __builtin_shufflevector(lo, hi, 0, 1, 2, 3, 4, 5, 6, 7);
  };
  auto fixup = [&](const f32x16& S, float l0p, float l1p, u32x4& pa, u32x4& pb, bool pend, u32x4& pend0, u32x4& pend1) AI {
    const float mx = swap32_max(sm_arg(max16(S), negmc));
    const float dm = fmaxf(__builtin_ceilf(mx), 0.f);
    const float f = __builtin_amdgcn_exp2f(-dm);
    negmc -= dm; l0 = l0p * f; l1 = l1p * f;
#pragma unroll
    for (int db = 0; db < 4; ++db) o[db] *= f;
    if (pend) {
#pragma unroll
      for (int q = 0; q < 4; ++q) { pend0[q] = scale_pk(pend0[q], f); pend1[q] = scale_pk(pend1[q], f); }
    }
    sm_half(S, negmc, l0, l1, pa, pb);
  };

  f32x16 Sa0, Sa1, Sb0, Sb1;
  u32x4 Pa0, Pa1, Pb0, Pb1, P2, P3;
  bf16x8 kf[4], vf[4];
  {
#pragma unroll
    for (int m = 0; m < 16; ++m) {
      const bf16x8 k = kfrag(0, m);
      if (m < 8) Sa0 = MFMA32(k, qf[m & 7], m == 0 ? (f32x16){} : Sa0); else Sa1 = MFMA32(k, qf[m & 7], m == 8 ? (f32x16){} : Sa1);
    }
  }
#pragma unroll
  for (int m = 0; m < 3; ++m) kf[m] = kfrag(1, m);
  negmc = -sm_arg(swap32_max(fmaxf(max16(Sa0), max16(Sa1))), 0.f);
  l0 = 0.f; l1 = 0.f;
  sm_half(Sa0, negmc, l0, l1, Pa0, Pa1);
#pragma unroll
  for (int db = 0; db < 4; ++db) o[db] = (f32x16){};
  SCHED_FENCE();

  const rsrc_t rWo = mk_rsrc(ws + WS_WOT + (size_t)(g * 8 + wid) * 65536, 65536);
  const rsrc_t rRt = mk_rsrc(ws + rdst, 36864);
  bf16x8 ring2[12][2];
  auto wo_load = [&](int sg, int i) AI { return __builtin_bit_cast(bf16x8, __builtin_amdgcn_raw_buffer_load_b128(rWo, fo + i * 1024, (sg >> 4) * 32768 + ((sg + rot) & 15) * 2048, 16)); };
  unsigned flagv = 0;

  auto tile = [&](auto T_, const f32x16& C1, f32x16& N0, f32x16& N1, u32x4& Pc0, u32x4& Pc1, u32x4& Pn0, u32x4& Pn1) AI {
    constexpr int T = T_.value;
    if (T == 4) STAMP(12);
    if (PRIO_A && wid >= 4) __builtin_amdgcn_s_setprio(1);
    {
      const float l0p = l0, l1p = l1; SmPipe sp;
#pragma unroll
      for (int m = 0; m < 16; ++m) {
        if (T < 7) {
          if (m < 8) N0 = MFMA32(kf[m & 3], qf[m & 7], m == 0 ? (f32x16){} : N0); else N1 = MFMA32(kf[m & 3], qf[m & 7], m == 8 ? (f32x16){} : N1);
          if (m + 3 < 16) kf[(m + 3) & 3] = kfrag(T + 1, m + 3);
        }
        if (m >= 13) vf[m - 13] = vfrag(T, m - 13);
        sm_gap(m, sp, C1, negmc, l0, l1, P2, P3);
        if (T == 7) ring2[m >> 1][m & 1] = wo_load(m >> 1, m & 1);
        if (T < 7) SCHED_FENCE();
      }
      MEM_FENCE();
      if (__builtin_expect(__any(!(l0 + l1 <= SM_LIMIT)), 0)) fixup(C1, l0p, l1p, P2, P3, true, Pc0, Pc1);
    }
    if (T == 4) STAMP(13);
    if (PRIO_A == 1) __builtin_amdgcn_s_setprio(0);
    {
      const float l0p = l0, l1p = l1; SmPipe sp;
#pragma unroll
      for (int m = 0; m < 16; ++m) {
        const u32x4 pp = (m < 4) ? Pc0 : (m < 8) ? Pc1 : (m < 12) ? P2 : P3;
        o[m & 3] = MFMA32(vf[m & 3], __builtin_bit_cast(bf16x8, pp), o[m & 3]);
        if (m + 3 < 16) vf[(m + 3) & 3] = vfrag(T, m + 3);
        if (T < 7) sm_gap(m, sp, N0, negmc, l0, l1, Pn0, Pn1);
        if (T == 7 && m < 8) ring2[8 + (m >> 1)][m & 1] = wo_load(8 + (m >> 1), m & 1);
        if (PRIO_A == 2 && m == 4) __builtin_amdgcn_s_setprio(0);
        if (T < 7 && m == 13) {
          if (T == 6 && wid == 0) {
            for (unsigned spins = 0; !__all(flagv == 32u) && spins < SPIN_CAP; ++spins) { __builtin_amdgcn_s_sleep(2); flagv = __hip_atomic_load(flagw, __ATOMIC_RELAXED, __HIP_MEMORY_SCOPE_AGENT); }
          }
          if (T == 4) STAMP(14);
          if (T == 2) WAIT_VM0();
          if (T == 0)      asm volatile("s_waitcnt lgkmcnt(0)" : "+v"(vf[2]), "+v"(vf[3])::"memory");
          else if (T == 1) asm volatile("s_waitcnt vmcnt(8) lgkmcnt(0)" : "+v"(vf[2]), "+v"(vf[3])::"memory");
          else if (T == 3) asm volatile("s_waitcnt vmcnt(4) lgkmcnt(0)" : "+v"(vf[2]), "+v"(vf[3])::"memory");
          else             asm volatile("s_waitcnt vmcnt(0) lgkmcnt(0)" : "+v"(vf[2]), "+v"(vf[3])::"memory");
          __builtin_amdgcn_s_barrier();
          MEM_FENCE();
          if (T == 4) STAMP(15);
          if (T == 4 && tid == 0) __hip_atomic_fetch_add(flag, 1u, __ATOMIC_RELAXED, __HIP_MEMORY_SCOPE_AGENT);
          if (T == 5 && wid == 0) flagv = __hip_atomic_load(flagw, __ATOMIC_RELAXED, __HIP_MEMORY_SCOPE_AGENT);
        }
        if (T < 6 && m == 13) { kf[0] = kfrag(T + 2, 0); kf[1] = kfrag(T + 2, 1); }
        if (T < 6 && m == 14) kf[2] = kfrag(T + 2, 2);
        if (T < 5 && m == 14) dmaK(T + 3);
        if (T < 6 && m == 15) dmaV(T + 2);
        if (T == 0 && m == 15) { retile_load(rtl, rsrc_w, rld, rk0, rperm, rn0, lane); MEM_FENCE(); }
        if (T == 2 && m == 15) {
#pragma unroll
          for (int i = 0; i < 4; ++i) __builtin_amdgcn_raw_buffer_store_b128(retile_frag(rtl, i), rRt, fo + (i >> 1) * rhalf + (i & 1) * 1024, 0, 16);
        }
        SCHED_FENCE();
      }
      MEM_FENCE();
      if (T < 7) {
        if (__builtin_expect(__any(!(l0 + l1 <= SM_LIMIT)), 0)) fixup(N0, l0p, l1p, Pn0, Pn1, false, Pc0, Pc1);
      }
    }
  };
  STAMP(5);
  tile(IC<0>{}, Sa1, Sb0, Sb1, Pa0, Pa1, Pb0, Pb1);
  tile(IC<1>{}, Sb1, Sa0, Sa1, Pb0, Pb1, Pa0, Pa1);
  tile(IC<2>{}, Sa1, Sb0, Sb1, Pa0, Pa1, Pb0, Pb1);
  tile(IC<3>{}, Sb1, Sa0, Sa1, Pb0, Pb1, Pa0, Pa1);
  tile(IC<4>{}, Sa1, Sb0, Sb1, Pa0, Pa1, Pb0, Pb1);
  tile(IC<5>{}, Sb1, Sa0, Sa1, Pb0, Pb1, Pa0, Pa1);
  STAMP(6);
  tile(IC<6>{}, Sa1, Sb0, Sb1, Pa0, Pa1, Pb0, Pb1);
  tile(IC<7>{}, Sb1, Sa0, Sa1, Pb0, Pb1, Pa0, Pa1);
  STAMP(7);

  {
    const float inv = 1.f / swap32_add(l0 + l1);
    char* dst = lds + L1_HT + ((wid & 1) * 32 + r32) * HT_STRIDE + ((wid >> 1) * 128 + 4 * hh) * 2;
#pragma unroll
    for (int db = 0; db < 4; ++db)
#pragma unroll
      for (int gq = 0; gq < 4; ++gq) {
        u32x2 w = {pk2(o[db][4 * gq] * inv, o[db][4 * gq + 1] * inv), pk2(o[db][4 * gq + 2] * inv, o[db][4 * gq + 3] * inv)};
        *(u32x2*)(dst + (32 * db + 8 * gq) * 2) = w;
      }
  }
  __syncthreads();
  STAMP(8);
  if (wid >= 4 && PRIO_O > 0) __builtin_amdgcn_s_setprio(1);
  {
    f32x4 acc[2][2][4];
    const rsrc_t rP = mk_rsrc(ws + WS_PART + (size_t)((rbk * 8 + g) * 8 + wid) * 8192, 8192);
    auto pstore = [&](int ps, int qt) AI {
      u32x4 w;
#pragma unroll
      for (int r = 0; r < 4; ++r) w[r] = pk2(acc[ps][0][qt][r], acc[ps][1][qt][r]);
      __builtin_amdgcn_raw_buffer_store_b128(w, rP, fo + (qt * 2 + ps) * 1024, 0, PART_AUX);
    };
    gemm_steps<2, 4, 32, 12, 2, 2>(acc, ring2, wo_load, bread, [&](auto S_, auto M_) AI {
      if constexpr (S_.value == PRIO_O && M_.value == 0) __builtin_amdgcn_s_setprio(0);
      if constexpr (S_.value >= 20 && S_.value < 22 && (M_.value & 3) == 1) pstore(0, (S_.value - 20) * 2 + (M_.value >> 2));
      if constexpr (S_.value == P0_STEP && M_.value == 7) {
        WAIT_VM0(); asm volatile("s_waitcnt vmcnt(0)" ::: "memory"); __builtin_amdgcn_s_barrier(); MEM_FENCE();
        if (tid == 0) __hip_atomic_fetch_add(flags + (FL_P0 + rbk) * 32, 1u, __ATOMIC_RELAXED, __HIP_MEMORY_SCOPE_AGENT);
      } });
    STAMP(9);
#pragma unroll
    for (int qt = 0; qt < 4; ++qt) pstore(1, qt);
  }
  STAMP(10);

  auto tail = [&](const int lane, const int a, const int g4, const unsigned fo, const int tid, const char* bl) AI {
  const rsrc_t rWs = mk_rsrc(ws, (unsigned)WS_END);
  auto ld1 = [&](size_t off, unsigned soff) AI { return __builtin_amdgcn_raw_buffer_load_b128(rWs, (unsigned)off, soff, 16); };
  auto ldf1 = [&](size_t off, unsigned soff) AI { return __builtin_bit_cast(bf16x8, __builtin_amdgcn_raw_buffer_load_b128(rWs, (unsigned)off, soff, 16)); };
  auto st1 = [&](size_t off, u32x4 v) AI { __builtin_amdgcn_raw_buffer_store_b128(v, rWs, (unsigned)off, 0, 16); };
  auto publish = [&](int idx) AI {
    asm volatile("s_waitcnt vmcnt(0) lgkmcnt(0)" ::: "memory"); __builtin_amdgcn_s_barrier(); MEM_FENCE();
    if (tid == 0) __hip_atomic_fetch_add(flags + idx * 32, 1u, __ATOMIC_RELAXED, __HIP_MEMORY_SCOPE_AGENT);
  };
  auto await = [&](int pw, int idx, unsigned want) AI {
    if (wid == pw) {
      unsigned spins = 0;
      while (!__all(__hip_atomic_load(flags + idx * 32, __ATOMIC_RELAXED, __HIP_MEMORY_SCOPE_AGENT) >= want) && ++spins < SPIN_CAP) __builtin_amdgcn_s_sleep(1);
    }
    MEM_FENCE(); __builtin_amdgcn_s_barrier(); MEM_FENCE();
  };
  const int b = blockIdx.x;
  const u16* modb = (const u16*)(ws + WS_MOD);
  publish(FL_P + rbk);
  STAMP(11);
  RT(1, 1);

  RT(2, 0);
  STAMPK(2, 0);
  {
    f32x2* red = (f32x2*)(lds + 16 * HT_STRIDE);
    const int rt = (b & 7) * 16 + (b >> 4), nh = (b >> 3) & 1;
    const int prb = rt >> 2, qt = rt & 3, row = rt * 16 + a, col = wid * 64 + g4 * 16;
    constexpr int D1 = 12, D1E = 12;
    bf16x8 A[D1][2];
    const int G = nh * 4 + (wid >> 1), ps = wid & 1;
    const rsrc_t rW = mk_rsrc(ws + WS_W1T + (size_t)G * 65536 + ps * 2048, 65536);
    auto aload = [&](int s, int i) AI { return ldfrag(rW, fo, (s * 4 + i) * 1024); };
    u32x4 pv[8][2];
    const size_t poff = WS_PART + (size_t)((prb * 64 + wid) * 8 + qt * 2) * 1024 + fo;
    const int prot = 0;
    await(0, FL_P0 + prb, 8);
    if (wid != 7) {
#pragma unroll
      for (int gg = 0; gg < 8; ++gg) pv[gg][0] = ld1(poff, (((gg + prot) & 7) * 64) * 1024);
    }
    SCHED_FENCE();
    await(7, FL_P + prb, 8);
    STAMPK(2, 1);
#pragma unroll
    for (int gg = 0; gg < 8; ++gg) pv[gg][1] = ld1(poff, (((gg + prot) & 7) * 64 + 1) * 1024);
    if (wid == 7) {
#pragma unroll
      for (int gg = 0; gg < 8; ++gg) pv[gg][0] = ld1(poff, (((gg + prot) & 7) * 64) * 1024);
    }
    u32x4 xv[2], gav[2], cbv[2], sbv[2];
#pragma unroll
    for (int h = 0; h < 2; ++h) {
      xv[h] = *(const u32x4*)(p.x + (size_t)row * D_ + col + 8 * h);
      gav[h] = *(const u32x4*)(modb + 1024 + col + 8 * h); sbv[h] = *(const u32x4*)(modb + 1536 + col + 8 * h); cbv[h] = *(const u32x4*)(modb + 2048 + col + 8 * h);
    }
    SCHED_FENCE();
    __builtin_amdgcn_s_barrier();
#pragma unroll
    for (int s = 0; s < D1E; ++s) { A[s][0] = aload(s, 0); A[s][1] = aload(s, 1); }
    SCHED_FENCE();
    float v[16];
#pragma unroll
    for (int d = 0; d < 8; ++d) { v[2 * d] = bflo(pv[0][d & 1][d >> 1]); v[2 * d + 1] = bfhi(pv[0][d & 1][d >> 1]); }
#pragma unroll
    for (int gg = 1; gg < 8; ++gg) {
#pragma unroll
      for (int d = 0; d < 8; ++d) { v[2 * d] += bflo(pv[gg][d & 1][d >> 1]); v[2 * d + 1] += bfhi(pv[gg][d & 1][d >> 1]); }
      SCHED_FENCE();
    }
#pragma unroll
    for (int d = 0; d < 8; ++d) {
      const unsigned ga = gav[d >> 2][d & 3], xx = xv[d >> 2][d & 3];
      const unsigned x2 = pk2(__builtin_fmaf(bflo(ga), v[2 * d], bflo(xx)), __builtin_fmaf(bfhi(ga), v[2 * d + 1], bfhi(xx)));
      xv[d >> 2][d & 3] = x2; v[2 * d] = bflo(x2); v[2 * d + 1] = bfhi(x2);
    }
    if (nh == 0) { st1(WS_X2 + (size_t)row * 1024 + col * 2, xv[0]); st1(WS_X2 + (size_t)row * 1024 + col * 2 + 16, xv[1]); }
    STAMPK(2, 2);
    auto row_stats = [&](f32x2* rd, float& mean, float& rstd) AI {
      float s0 = 0, s1 = 0, q0 = 0, q1 = 0;
#pragma unroll
      for (int e = 0; e < 8; ++e) { s0 += v[2 * e]; s1 += v[2 * e + 1]; q0 = __builtin_fmaf(v[2 * e], v[2 * e], q0); q1 = __builtin_fmaf(v[2 * e + 1], v[2 * e + 1], q1); }
      float s = swap32_add(swap16_add(s0 + s1)), q = swap32_add(swap16_add(q0 + q1));
      if (g4 == 0) rd[wid * 16 + a] = (f32x2){s, q};
      asm volatile("s_waitcnt lgkmcnt(0)" ::: "memory"); __builtin_amdgcn_s_barrier(); MEM_FENCE();
      f32x2 t = rd[a];
#pragma unroll
      for (int w = 1; w < 8; ++w) t += rd[w * 16 + a];
      mean = t[0] * (1.f / 512.f);
      rstd = __builtin_amdgcn_rsqf(fmaxf(t[1] * (1.f / 512.f) - mean * mean, 0.f) + 1e-5f);
    };
    float mean, rstd;
#pragma unroll
    for (int s = D1E; s < D1; ++s) { A[s][0] = aload(s, 0); A[s][1] = aload(s, 1); }
    SCHED_FENCE();
    row_stats(red, mean, rstd);
    u32x4 hv[2];
#pragma unroll
    for (int d = 0; d < 8; ++d) {
      const unsigned cb = cbv[d >> 2][d & 3], sb = sbv[d >> 2][d & 3];
      const unsigned hb = pk2((v[2 * d] - mean) * rstd * bflo(cb) + bflo(sb), (v[2 * d + 1] - mean) * rstd * bfhi(cb) + bfhi(sb));
      hv[d & 1][d >> 1] = hb; v[2 * d] = bflo(hb); v[2 * d + 1] = bfhi(hb);
    }
    if (nh == 1) { st1(WS_HB + (size_t)row * 1024 + wid * 128 + g4 * 16, hv[0]); st1(WS_HB + (size_t)row * 1024 + wid * 128 + 64 + g4 * 16, hv[1]); }
    row_stats(red + 128, mean, rstd);
    const float nm = -mean * rstd;
#pragma unroll
    for (int h = 0; h < 2; ++h) {
      u32x4 o;
#pragma unroll
      for (int e = 0; e < 4; ++e) o[e] = pk2(__builtin_fmaf(v[8 * h + 2 * e], rstd, nm), __builtin_fmaf(v[8 * h + 2 * e + 1], rstd, nm));
      *(u32x4*)(lds + a * HT_STRIDE + (col + 8 * h) * 2) = o;
    }
    STAMPK(2, 3);
    asm volatile("s_waitcnt lgkmcnt(0)" ::: "memory"); __builtin_amdgcn_s_barrier(); MEM_FENCE();
    STAMPK(2, 4);
    f32x4 acc[1][2][1];
    gemm_steps<2, 1, 16, D1, 1, 4>(acc, A, aload, [&](int s, int) AI { return *(const bf16x8*)(bl + 64 * s); }, [](auto, auto) AI {});
    STAMPK(2, 5);
    u32x4 ov;
#pragma unroll
    for (int rg = 0; rg < 4; ++rg) ov[rg] = pk2(gelu_tanh(acc[0][0][0][rg]), gelu_tanh(acc[0][1][0][rg]));
    st1(WS_H1 + (size_t)(rt * 16 + 2 * G + ps) * 1024 + fo, ov);
    STAMPK(2, 6);
    publish(FL_H1 + rt);
    RT(2, 1);
    STAMPK(2, 7);
  }

  const int rb2 = (b & 7) * 4 + (b >> 6), ns = (b >> 3) & 7;
  const int ng = ns * 7 + (wid < 7 ? wid : 6);
  const size_t w3off = WS_W3T + (size_t)(ng >> 1) * 65536 + (ng & 1) * 2048 + fo;
  bf16x8 A[16][2];
  RT(3, 0);
  STAMPK(3, 0);
  {
    const int rt2 = (b & 7) * 8 + (b >> 5), nq = (b >> 3) & 3;
    const int G = nq * 2 + ((wid & 3) >> 1), ps = wid & 1;
    if (wid < 4) {
      const size_t aoff = WS_W2T + (size_t)G * 65536 + ps * 2048 + fo;
#pragma unroll
      for (int s = 0; s < 16; ++s) { A[s][0] = ldf1(aoff, (s * 4) * 1024); A[s][1] = ldf1(aoff, (s * 4 + 1) * 1024); }
    }
    SCHED_FENCE();
    { const int t = 2 * rt2 - 1 + (lane & 3); await(7, FL_H1 + (t < 0 ? 0 : t > 127 ? 127 : t), 2); }
    STAMPK(3, 1);
    const rsrc_t rH = mk_rsrc(ws + WS_H1, (unsigned)NXB);
    const int sh = (wid < 2) ? -1 : (wid < 4) ? 1 : (wid < 6) ? -2 : 2;
    u32x4 hf[2][2], hbv[2];
#pragma unroll
    for (int qt = 0; qt < 2; ++qt) {
      const int r = rt2 * 32 + qt * 16 + a + sh;
      const unsigned ho = (unsigned)((r >> 4) * 16384 + ((r & 15) + 16 * g4) * 16);
      hf[qt][0] = __builtin_amdgcn_raw_buffer_load_b128(rH, ho, (wid * 2) * 1024, 16); hf[qt][1] = __builtin_amdgcn_raw_buffer_load_b128(rH, ho, (wid * 2 + 1) * 1024, 16);
    }
    const size_t xoff = (size_t)(rt2 * 32 + a) * 1024 + G * 128 + ps * 64 + g4 * 16;
    if (wid < 4) { hbv[0] = ld1(WS_HB + xoff, 0); hbv[1] = ld1(WS_HB + xoff, 16384); }
#pragma unroll
    for (int qt = 0; qt < 2; ++qt) { *(u32x4*)(lds + (qt * 16 + wid * 2) * 1024 + fo) = hf[qt][0]; *(u32x4*)(lds + (qt * 16 + wid * 2 + 1) * 1024 + fo) = hf[qt][1]; }
    asm volatile("s_waitcnt lgkmcnt(0)" ::: "memory"); __builtin_amdgcn_s_barrier(); MEM_FENCE();
    STAMPK(3, 2);
    f32x2* red = (f32x2*)(lds + 32768);
    if (wid < 4) {
      f32x4 acc[1][2][2];
      gemm_steps<2, 2, 16, 16, 1, 3>(acc, A, [&](int, int) AI { return bf16x8{}; }, [&](int s, int qt) AI { return *(const bf16x8*)(lds + (qt * 16 + s) * 1024 + fo); }, [](auto, auto) AI {});
#pragma unroll
      for (int qt = 0; qt < 2; ++qt) {
        u32x4 ov; float s = 0, q = 0;
#pragma unroll
        for (int rg = 0; rg < 4; ++rg) {
          ov[rg] = pk2(bflo(hbv[qt][rg]) + acc[0][0][qt][rg], bfhi(hbv[qt][rg]) + acc[0][1][qt][rg]);
          const float lo = bflo(ov[rg]), hi = bfhi(ov[rg]); s += lo + hi; q += lo * lo + hi * hi;
        }
        st1(WS_X1 + xoff + qt * 16384, ov);
        s = swap32_add(swap16_add(s)); q = swap32_add(swap16_add(q));
        if (g4 == 0) red[wid * 32 + qt * 16 + a] = (f32x2){s, q};
      }
    } else if (wid < 7) {
#pragma unroll
      for (int s = 0; s < 16; ++s) { A[s][0] = ldf1(w3off, (s * 4) * 1024); A[s][1] = ldf1(w3off, (s * 4 + 1) * 1024); }
    }
    asm volatile("s_waitcnt lgkmcnt(0)" ::: "memory"); __builtin_amdgcn_s_barrier(); MEM_FENCE();
    if (tid < 32) {
      const f32x2 t = red[tid] + red[32 + tid] + red[64 + tid] + red[96 + tid];
      __hip_atomic_store((gu64*)(ws + WS_ST + (size_t)(rt2 * 32 + tid) * 32 + nq * 8), __builtin_bit_cast(unsigned long long, t), __ATOMIC_RELAXED, __HIP_MEMORY_SCOPE_AGENT);
    }
    STAMPK(3, 3);
    publish(FL_X1 + (rt2 >> 1));
    RT(3, 1);
    STAMPK(3, 4);
  }

  bf16x8 W[7][4];
  const size_t woff = WS_W4T + (size_t)(ns * 56 + wid * 7) * 4096 + fo;
  auto rot7 = [&](int s) AI { const int t = s + (ROT ? (ns < 7 ? ns : 0) : 0); return t >= 7 ? t - 7 : t; };
  RT(4, 0);
  STAMPK(4, 0);
  {
    if (wid < 4) {
#pragma unroll
      for (int s = 0; s < 16; ++s) { A[s][0] = ldf1(w3off, (s * 4) * 1024); A[s][1] = ldf1(w3off, (s * 4 + 1) * 1024); }
    }
    SCHED_FENCE();
    await(7, FL_X1 + rb2, 8);
    STAMPK(4, 1);
    u32x4 xv[8];
    const u32x4 stv = ld1(WS_ST + (size_t)(rb2 * 64 + wid * 8) * 32 + (lane & 15) * 16, 0);
#pragma unroll
    for (int i = 0; i < 8; ++i) xv[i] = ld1(WS_X1 + (size_t)(rb2 * 64 + wid * 8) * 1024 + fo, i * 1024);
    SCHED_FENCE();
    const float sm = dpp_add<0xB1>(__uint_as_float(stv[0]) + __uint_as_float(stv[2])) * (1.f / 512.f);
    const float sq = dpp_add<0xB1>(__uint_as_float(stv[1]) + __uint_as_float(stv[3])) * (1.f / 512.f);
    const float rsl = __builtin_amdgcn_rsqf(fmaxf(sq - sm * sm, 0.f) + 1e-5f), nml = -sm * rsl;
    float rs[8], nm[8];
#pragma unroll
    for (int i = 0; i < 8; ++i) { rs[i] = __uint_as_float(__builtin_amdgcn_readlane(__float_as_uint(rsl), 2 * i)); nm[i] = __uint_as_float(__builtin_amdgcn_readlane(__float_as_uint(nml), 2 * i)); }
    SCHED_FENCE();
#pragma unroll
    for (int hf = 0; hf < 2; ++hf) {
      float lo[16], hi[16]; unsigned t[16];
      H_STAGE(lo[j] = bflo(xv[i][d]); hi[j] = bfhi(xv[i][d]));
      H_STAGE(lo[j] = __builtin_fmaf(lo[j], rs[i], nm[i]); hi[j] = __builtin_fmaf(hi[j], rs[i], nm[i]));
      H_STAGE(t[j] = pk2(lo[j], hi[j]));
#pragma unroll
      for (int i = 0; i < 4; ++i) *(u32x4*)(lds + (wid * 8 + hf * 4 + i) * HT_STRIDE + fo) = (u32x4){t[4 * i], t[4 * i + 1], t[4 * i + 2], t[4 * i + 3]};
      SCHED_FENCE();
    }
    STAMPK(4, 2);
    asm volatile("s_waitcnt lgkmcnt(0)" ::: "memory"); __builtin_amdgcn_s_barrier(); MEM_FENCE();
    STAMPK(4, 3);
    if (wid < 7) {
      f32x4 acc[2][2][2];
      if (wid >= 4) __builtin_amdgcn_s_setprio(1);
      const size_t dst = WS_U + (size_t)(rb2 * 4 * 56 + ng) * 1024 + fo;
      u32x4 ow;
      auto gpart = [&](int ps, int k) AI {
        unsigned w = pk2(gelu_tanh(acc[ps][0][k >> 2][k & 3]), gelu_tanh(acc[ps][1][k >> 2][k & 3])); PIN(w); ow[k & 3] = w;
        if ((k & 3) == 3) st1(dst + (size_t)(2 * ps + (k >> 2)) * 56 * 1024, ow);
      };
      gemm_steps<2, 2, 32, 16, 2, 3>(acc, A, [&](int s, int i) AI { return A[s & 15][i]; },
                                     [&](int s, int qt) AI { return *(const bf16x8*)(bl + ((s >> 4) * 2 + qt) * 16 * HT_STRIDE + 64 * (s & 15)); },
                                     [&](auto S_, auto M_) AI {
                                       constexpr int s = S_.value, m = M_.value;
                                       if constexpr (s == PRIO_G3 && m == 0) __builtin_amdgcn_s_setprio(0);
                                       if constexpr (s >= 17 && (s & 1) && m == 1) gpart(0, (s - 17) >> 1);
                                       if constexpr (s >= 17 && s < 31 && m >= 2) { constexpr int k = (s - 17) * 2 + m - 2; W[k >> 2][k & 3] = ldf1(woff + (k & 3) * 1024, rot7(k >> 2) * 4096); }
                                     });
      STAMPK(4, 4);
#pragma unroll
      for (int k = 0; k < 8; ++k) gpart(1, k);
    } else {
#pragma unroll
      for (int k = 0; k < 28; ++k) W[k >> 2][k & 3] = ldf1(woff + (k & 3) * 1024, rot7(k >> 2) * 4096);
    }
    STAMPK(4, 5);
    publish(FL_U + rb2);
    RT(4, 1);
    STAMPK(4, 6);
  }

  RT(5, 0);
  STAMPK(5, 0);
  {
    bf16x8 B[7][4];
    const size_t uoff = WS_U + (size_t)(rb2 * 4 * 56 + wid * 7) * 1024 + fo;
    const int oqt = wid >> 1, orp = wid & 1, orow = rb2 * 64 + oqt * 16 + a, ocol = ns * 64 + g4 * 16 + orp * 8;
    u32x2 x1a, x1b; u32x4 x2v, gbv;
    SCHED_FENCE();
    await(0, FL_U + rb2, 8);
#pragma unroll
    for (int s = 0; s < G4_AHEAD; ++s)
#pragma unroll
      for (int i = 0; i < 4; ++i) B[s][i] = ldf1(uoff, (i * 56 + rot7(s)) * 1024);
    SCHED_FENCE();
    STAMPK(5, 1);
    f32x4 acc[4][4] = {};
#pragma unroll
    for (int s = 0; s < 7; ++s) {
#pragma unroll
      for (int i = 0; i < 4; ++i) {
#pragma unroll
        for (int qt = 0; qt < 4; ++qt) acc[i][qt] = MFMA16(W[s][i], B[s][qt], acc[i][qt]);
        if (s + G4_AHEAD < 7) B[s + G4_AHEAD][i] = ldf1(uoff, (i * 56 + rot7(s + G4_AHEAD)) * 1024);
        SCHED_FENCE();
      }
      if (s == 5) {
        const size_t x1o = WS_X1 + (size_t)orow * 1024 + ns * 128 + g4 * 16 + orp * 8;
        x1a = __builtin_amdgcn_raw_buffer_load_b64(rWs, (unsigned)x1o, 0, 16); x1b = __builtin_amdgcn_raw_buffer_load_b64(rWs, (unsigned)x1o, 64, 16);
        x2v = ld1(WS_X2 + (size_t)orow * 1024 + ocol * 2, 0); gbv = *(const u32x4*)(modb + 2560 + ocol);
        SCHED_FENCE();
      }
    }
    STAMPK(5, 2);
#pragma unroll
    for (int qt = 0; qt < 4; ++qt)
#pragma unroll
      for (int rg = 0; rg < 4; ++rg)
        *(f32x4*)(lds + ((wid * 4 + qt) * 4 + rg) * 1024 + fo) = (f32x4){acc[0][qt][rg], acc[1][qt][rg], acc[2][qt][rg], acc[3][qt][rg]};
    STAMPK(5, 3);
    __syncthreads();
    STAMPK(5, 4);
    f32x4 s0 = {}, s1 = {};
#pragma unroll
    for (int w = 0; w < 8; ++w) {
      s0 += *(const f32x4*)(lds + ((w * 4 + oqt) * 4 + 2 * orp) * 1024 + fo);
      s1 += *(const f32x4*)(lds + ((w * 4 + oqt) * 4 + 2 * orp + 1) * 1024 + fo);
    }
    const float h2[8] = {s0[0], s0[1], s0[2], s0[3], s1[0], s1[1], s1[2], s1[3]};
    const unsigned x1v[4] = {x1a[0], x1b[0], x1a[1], x1b[1]};
    u32x4 ov;
#pragma unroll
    for (int e = 0; e < 4; ++e)
      ov[e] = pk2(bflo(x2v[e]) + bflo(gbv[e]) * (bflo(x1v[e]) + h2[2 * e]), bfhi(x2v[e]) + bfhi(gbv[e]) * (bfhi(x1v[e]) + h2[2 * e + 1]));
    *(u32x4*)(p.out + (size_t)orow * D_ + ocol) = ov;
    STAMPK(5, 5);
    RT(5, 1);
    STAMPK(5, 6);
  }
  };
  {
    int l2; asm volatile("v_mbcnt_lo_u32_b32 %0, -1, 0\n\tv_mbcnt_hi_u32_b32 %0, -1, %0" : "=v"(l2));
    tail(l2, l2 & 15, l2 >> 4, (unsigned)l2 * 16u, wid * 64 + l2, lds + (l2 & 15) * HT_STRIDE + (l2 >> 4) * 16);
  }
}

extern "C" void kernel_launch(void* const* d_in, const int* in_sizes, int n_in, void* d_out, int out_size, void* d_ws, size_t ws_size, hipStream_t stream) {
  char* ws = (char*)d_ws;
  K0Args p0;
  p0.x = (const u16*)d_in[0]; p0.c = (const u16*)d_in[10]; p0.wmod = (const u16*)d_in[11]; p0.wq = (const u16*)d_in[3]; p0.w1 = (const u16*)d_in[6]; p0.kc = (const u16*)d_in[1]; p0.vc = (const u16*)d_in[2]; p0.ws = ws;
  K1Args p1;
  p1.x = p0.x; p1.kc = (const u16*)d_in[1]; p1.vc = (const u16*)d_in[2]; p1.wo = (const u16*)d_in[4];
  p1.w2 = (const u16*)d_in[7]; p1.w3 = (const u16*)d_in[8]; p1.w4 = (const u16*)d_in[9]; p1.lnst = (const float*)(ws + WS_Y); p1.ws = ws; p1.out = (u16*)d_out;
  (void)hipFuncSetAttribute((const void*)k1_attn, hipFuncAttributeMaxDynamicSharedMemorySize, L1_END);
  k0_prep<<<K0_BLOCKS, 512, 0, stream>>>(p0);
  k1_attn<<<256, 512, L1_END, stream>>>(p1);
}
```

```cpp
#include <hip/hip_runtime.h>
#include <hip/hip_bf16.h>
#include <type_traits>
#include <utility>

typedef unsigned short u16;
typedef __bf16 bf16x8 __attribute__((ext_vector_type(8)));
typedef __bf16 bf16x4 __attribute__((ext_vector_type(4)));
typedef __bf16 bf16x2 __attribute__((ext_vector_type(2)));
typedef float f32x16 __attribute__((ext_vector_type(16)));
typedef float f32x4 __attribute__((ext_vector_type(4)));
typedef float f32x2 __attribute__((ext_vector_type(2)));
typedef unsigned u32x2 __attribute__((ext_vector_type(2)));
typedef unsigned u32x4 __attribute__((ext_vector_type(4)));
typedef __amdgpu_buffer_rsrc_t rsrc_t;
#define DI __device__ __forceinline__
#define AI __attribute__((always_inline))
#define LDSP(T, p) ((__attribute__((address_space(3))) T*)(p))
#define SCHED_FENCE() __builtin_amdgcn_sched_barrier(0)
#define PIN(x) asm volatile("" : "+v"(x))
#define MEM_FENCE() asm volatile("" ::: "memory")
#define WAIT_VM0() __builtin_amdgcn_s_waitcnt(0x0F70)
#define STAMP(i)
#define STAMPK(k, i)
#define RT(k, i)

#define ROUND_S 1
#define ABL 0
#define SM_SHIFT 100
#define NT_SRC 0
#define K0_WARM 3
constexpr int S_ = 2048, D_ = 512, HQ = 32, HKV = 8, DH = 128, SK = 2048, WIN = 512, F_ = 1792;

constexpr size_t NXB = (size_t)S_ * D_ * 2;
constexpr size_t WS_MOD = 0;
constexpr size_t WS_Y   = 8192;
constexpr size_t WS_X2  = WS_Y  + NXB;
constexpr size_t WS_HB  = WS_X2 + NXB;
constexpr size_t WS_H1  = WS_HB + NXB;
constexpr size_t WS_X1  = WS_H1 + NXB;
constexpr size_t WS_U   = WS_X1 + NXB;
constexpr size_t WS_PART = WS_U + (size_t)S_ * F_ * 2;
constexpr size_t WS_WQT = WS_PART + 8 * NXB;
constexpr size_t WS_WOT = WS_WQT + (size_t)D_ * 4096 * 2;
constexpr size_t WS_W1T = WS_WOT + (size_t)D_ * 4096 * 2;
constexpr size_t WS_W2T = WS_W1T + (size_t)D_ * D_ * 2;
constexpr size_t WS_W3T = WS_W2T + (size_t)D_ * D_ * 2;
constexpr size_t WS_W4T = WS_W3T + (size_t)D_ * F_ * 2;
constexpr size_t WS_ST  = WS_W4T + (size_t)D_ * F_ * 2;
constexpr size_t WS_FLAG = WS_ST + (size_t)S_ * 32;
constexpr int FL_W = 0, FL_P = 8, FL_H1 = 40, FL_X1 = 168, FL_U = 200, FL_P0 = 232, FL_N = 264;
constexpr size_t WS_END = WS_FLAG + FL_N * 128;

DI float bflo(unsigned u) { return __uint_as_float(u << 16); }
DI float bfhi(unsigned u) { return __uint_as_float(u & 0xffff0000u); }
DI unsigned pk2(float lo, float hi) { f32x2 v = {lo, hi}; return __builtin_bit_cast(unsigned, __builtin_convertvector(v, bf16x2)); }
DI float rb(float x) { return __uint_as_float(pk2(0.f, x)); }
DI rsrc_t mk_rsrc(const void* p, unsigned bytes) { return __builtin_amdgcn_make_buffer_rsrc((void*)p, (short)0, (int)bytes, 0x00020000); }
DI unsigned rfl(unsigned x) { return __builtin_amdgcn_readfirstlane(x); }
DI bf16x8 ldfrag(rsrc_t r, unsigned voff, unsigned soff) { return __builtin_bit_cast(bf16x8, __builtin_amdgcn_raw_buffer_load_b128(r, voff, soff, 0)); }
template <int CTRL> DI float dpp_add(float v) {
  return v + __uint_as_float(__builtin_amdgcn_update_dpp(0u, __float_as_uint(v), CTRL, 0xf, 0xf, true));
}
DI float swap16_add(float v) { auto r = __builtin_amdgcn_permlane16_swap(__float_as_uint(v), __float_as_uint(v), false, false); return __uint_as_float(r[0]) + __uint_as_float(r[1]); }
DI float swap32_add(float v) { auto r = __builtin_amdgcn_permlane32_swap(__float_as_uint(v), __float_as_uint(v), false, false); return __uint_as_float(r[0]) + __uint_as_float(r[1]); }
DI float swap32_max(float v) { auto r = __builtin_amdgcn_permlane32_swap(__float_as_uint(v), __float_as_uint(v), false, false); return fmaxf(__uint_as_float(r[0]), __uint_as_float(r[1])); }
DI float wave_sum(float v) {
  v = dpp_add<0xB1>(v); v = dpp_add<0x4E>(v); v = dpp_add<0x141>(v); v = dpp_add<0x140>(v);
  v = swap16_add(v); v = swap32_add(v); return v;
}
DI float div_rn(float a, float d, float r) {
  float q = a * r; float e = __builtin_fmaf(-q, d, a); return __builtin_fmaf(e, r, q);
}
DI float gelu_tanh(float x) {
  const float u = 0.7978845608028654f * (x + 0.044715f * x * x * x);
  return x * __builtin_amdgcn_rcpf(1.f + __builtin_amdgcn_exp2f(-2.885390081777927f * u));
}
DI void ln_consts(float mean, float var, float& m_bf, float& d_bf, float& r) {
  m_bf = rb(mean); d_bf = rb(__builtin_sqrtf(rb(rb(var) + 1e-5f))); r = __builtin_amdgcn_rcpf(d_bf);
}
DI float ln_apply(float x, float m_bf, float d_bf, float r) { return rb(div_rn(rb(x - m_bf), d_bf, r)); }
DI u32x4 ln_row(const u32x4 xv) {
  float f[8];
#pragma unroll
  for (int e = 0; e < 4; ++e) { f[2 * e] = bflo(xv[e]); f[2 * e + 1] = bfhi(xv[e]); }
  float s = 0;
#pragma unroll
  for (int e = 0; e < 8; ++e) s += f[e];
  const float mean = wave_sum(s) * (1.f / 512.f);
  float q = 0;
#pragma unroll
  for (int e = 0; e < 8; ++e) { float d = f[e] - mean; q += d * d; }
  const float var = wave_sum(q) * (1.f / 512.f);
  float mb, db, r; ln_consts(mean, var, mb, db, r);
  u32x4 o;
#pragma unroll
  for (int e = 0; e < 4; ++e) o[e] = pk2(div_rn(rb(f[2 * e] - mb), db, r), div_rn(rb(f[2 * e + 1] - mb), db, r));
  return o;
}

#define MFMA16(a, b, c) __builtin_amdgcn_mfma_f32_16x16x32_bf16((a), (b), (c), 0, 0, 0)
#define MFMA32(a, b, c) __builtin_amdgcn_mfma_f32_32x32x16_bf16((a), (b), (c), 0, 0, 0)

DI void dma16(unsigned lds_dst, unsigned voff, rsrc_t r, unsigned soff) {
  asm volatile("s_mov_b32 m0, %0\n\ts_nop 0\n\tbuffer_load_dwordx4 %1, %2, %3 offen lds" ::"s"(lds_dst), "v"(voff), "s"(r), "s"(soff) : "memory");
}

struct Retile { u32x2 d[8]; };
DI void retile_load(Retile& t, const u16* __restrict__ W, int ldw, int k0, bool perm, int n0, int lane) {
  const int a = lane & 15, g4 = lane >> 4;
  const u16* p = W + (size_t)(k0 + (perm ? 16 : 8) * g4) * ldw + n0 + 4 * a;
  const int s2 = (perm ? 4 : 2) * ldw;
#pragma unroll
  for (int j = 0; j < 8; ++j) {
    const u32x2* q = (const u32x2*)(p + (j >> 1) * s2 + (j & 1) * ldw);
    t.d[j] = NT_SRC ? __builtin_nontemporal_load(q) : *q;
  }
}
DI u32x4 retile_frag(const Retile& t, int i) {
  u32x4 f;
#pragma unroll
  for (int p = 0; p < 4; ++p) f[p] = __builtin_amdgcn_perm(t.d[2 * p + 1][i >> 1], t.d[2 * p][i >> 1], (i & 1) ? 0x07060302u : 0x05040100u);
  return f;
}

struct K0Args { const u16 *x, *c, *wmod, *wq, *w1, *kc, *vc; char* ws; };
constexpr int K0_BLOCKS = 352 + 128 + 16;
__global__ __launch_bounds__(512) void k0_prep(const K0Args p) {
  __shared__ float red[8][64];
  const int tid = threadIdx.x, lane = tid & 63, wid = tid >> 6;
  int b = blockIdx.x;
#define WSP p.ws
  RT(0, 0);
  if (b < 96) {
    const int n0 = b * 32, cg = tid & 3, kq = tid >> 2;
    u32x4 wv[4]; float sv[4];
#pragma unroll
    for (int i = 0; i < 4; ++i) wv[i] = *(const u32x4*)(p.wmod + (size_t)(kq + 128 * i) * 3072 + n0 + cg * 8);
#pragma unroll
    for (int i = 0; i < 4; ++i) { float cv = bflo(p.c[kq + 128 * i]); sv[i] = rb(cv / (1.f + __expf(-cv))); }
    float acc[8] = {};
#pragma unroll
    for (int i = 0; i < 4; ++i)
#pragma unroll
      for (int e = 0; e < 4; ++e) { acc[2 * e] += sv[i] * bflo(wv[i][e]); acc[2 * e + 1] += sv[i] * bfhi(wv[i][e]); }
#pragma unroll
    for (int e = 0; e < 8; ++e) { float v = acc[e]; v += __shfl_xor(v, 4); v += __shfl_xor(v, 8); v = swap16_add(v); v = swap32_add(v); acc[e] = v; }
    if (lane < 4)
#pragma unroll
      for (int e = 0; e < 8; ++e) red[wid][lane * 8 + e] = acc[e];
    __syncthreads();
    if (tid < 32) {
      float s = 0;
#pragma unroll
      for (int w = 0; w < 8; ++w) s += red[w][tid];
      s = rb(s);
      const int n = n0 + tid, seg = n >> 9;
      if (seg == 1 || seg == 4) s = rb(1.f + s);
      ((u16*)(p.ws + WS_MOD))[n] = (u16)(__float_as_uint(s) >> 16);
    }
    RT(0, 1);
    return;
  }
  b -= 96;
  if (b < 256) {
    const int row = b * 8 + wid;
    const u32x4 xv = *(const u32x4*)(p.x + (size_t)row * D_ + lane * 8);
    u32x4 wv[8];
#pragma unroll
    for (int i = 0; i < 8; ++i) wv[i] = *(const u32x4*)(p.x + (size_t)((b >> 3) * 64 + wid * 8 + i) * D_ + lane * 8);
    float f[8];
#pragma unroll
    for (int e = 0; e < 4; ++e) { f[2 * e] = bflo(xv[e]); f[2 * e + 1] = bfhi(xv[e]); }
    float s = 0;
#pragma unroll
    for (int e = 0; e < 8; ++e) s += f[e];
    const float mean = wave_sum(s) * (1.f / 512.f);
    float q = 0;
#pragma unroll
    for (int e = 0; e < 8; ++e) { float d = f[e] - mean; q += d * d; }
    const float var = wave_sum(q) * (1.f / 512.f);
    float mb, db, r; ln_consts(mean, var, mb, db, r);
    if (lane == 0) *(f32x2*)(p.ws + WS_Y + (size_t)row * 8) = (f32x2){mb, db};
#pragma unroll
    for (int i = 0; i < 8; ++i) asm volatile("" ::"v"(wv[i]));
    RT(0, 1);
    return;
  }
  b -= 256;
  Retile t; char* dst;
  if (b < 128) { const int g = b & 7, s = b >> 3, ng = g * 8 + wid; retile_load(t, p.wq, 4096, 32 * s, false, 64 * ng, lane); dst = p.ws + WS_WQT + (size_t)(ng * 16 + s) * 4096; }
  else {
    const int s = b - 128; retile_load(t, p.w1, 512, 32 * s, false, 64 * wid, lane); dst = p.ws + WS_W1T + (size_t)(wid * 16 + s) * 4096;
    if (s == 0 && tid < FL_N) *(unsigned*)(p.ws + WS_FLAG + tid * 128) = 0u;
  }
#pragma unroll
  for (int i = 0; i < 4; ++i) *(u32x4*)(dst + i * 1024 + lane * 16) = retile_frag(t, i);
  RT(0, 1);
#undef WSP
#define WSP ws
}

template <int V> using IC = std::integral_constant<int, V>;
template <int N, class F, int... Is> DI void sfor_(F&& f, std::integer_sequence<int, Is...>) { (f(IC<Is>{}), ...); }
template <int N, class F> DI void sfor(F&& f) { sfor_<N>(f, std::make_integer_sequence<int, N>{}); }
template <int NI, int NQT, int NSTEP, int DEPTH, int NSET, int BD = 1, class ALoad, class BRead, class Hook>
DI void gemm_steps(f32x4 (&acc)[NSET][NI][NQT], bf16x8 (&ring)[DEPTH][NI], ALoad aload, BRead bread, Hook hook) {
  constexpr int NM = NI * NQT, PER = NSTEP / NSET;
  bf16x8 B[BD + 1][NQT];
#pragma unroll
  for (int s = 0; s < BD; ++s)
#pragma unroll
    for (int qt = 0; qt < NQT; ++qt) B[s][qt] = bread(s, qt);
  SCHED_FENCE();
  sfor<NSTEP * NM>([&](auto X) AI {
    constexpr int s = X.value / NM, m = X.value % NM, i = m / NQT, qt = m % NQT;
    if constexpr (s % PER == 0) acc[s / PER][i][qt] = MFMA16(ring[s % DEPTH][i], B[s % (BD + 1)][qt], (f32x4){});
    else                        acc[s / PER][i][qt] = MFMA16(ring[s % DEPTH][i], B[s % (BD + 1)][qt], acc[s / PER][i][qt]);
    if constexpr (m < NQT && s + BD < NSTEP) B[(s + BD) % (BD + 1)][m] = bread(s + BD, m);
    if constexpr (qt == NQT - 1 && s + DEPTH < NSTEP) ring[s % DEPTH][i] = aload(s + DEPTH, i);
    hook(IC<s>{}, IC<m>{});
    SCHED_FENCE();
  });
}

constexpr int HT_STRIDE = 1056, QX_STRIDE = 272;
constexpr int L1_HT = 0, L1_K = 69632, L1_V = L1_K + 3 * 16384, L1_END = L1_V + 2 * 16384;
#define PRIO_G3 21
#define G4_AHEAD 2
#define ROT 1
#define P0_STEP 28
#define PRIO_P 1
#define PRIO_Q 11
#define PRIO_O 21
#define PRIO_A 0
#define GBAR 1
#define WO_SC1 1
#define OPASS 2
#define PF_ON 1
#define RT_AUX 16
#define PART_AUX 16
constexpr float SM_C = 0.12751743082459868f;
constexpr float SM_LIMIT = __builtin_bit_cast(float, (unsigned)(127 + 40 - SM_SHIFT)  __H0__ );

DI float sm_arg(float s, float negmc) {
  return __builtin_fmaf(rb(s), SM_C, negmc);
}
DI float max16(const f32x16& S) {
  float m = fmaxf(S[0], S[1]);
#pragma unroll
  for (int r = 2; r < 16; ++r) m = fmaxf(m, S[r]);
  return m;
}
DI unsigned scale_pk(unsigned p, float f) { return pk2(bflo(p) * f, bfhi(p) * f); }
DI void sm_half(const f32x16& S, float negmc, float& l0, float& l1, u32x4& pa, u32x4& pb) {
#pragma unroll
  for (int p = 0; p < 4; ++p) {
    const float e0 = __builtin_amdgcn_exp2f(sm_arg(S[2 * p], negmc)), e1 = __builtin_amdgcn_exp2f(sm_arg(S[2 * p + 1], negmc));
    l0 += e0; l1 += e1; pa[p] = pk2(e0, e1);
  }
#pragma unroll
  for (int p = 0; p < 4; ++p) {
    const float e0 = __builtin_amdgcn_exp2f(sm_arg(S[8 + 2 * p], negmc)), e1 = __builtin_amdgcn_exp2f(sm_arg(S[8 + 2 * p + 1], negmc));
    l0 += e0; l1 += e1; pb[p] = pk2(e0, e1);
  }
}
struct SmPipe { float r[16], x[16], e[16]; };
DI void sm_gap(const int m, SmPipe& sp, const f32x16& S, float negmc, float& l0, float& l1, u32x4& pa, u32x4& pb) {
  constexpr int D = ROUND_S ? 1 : 0, SPAN = 13 - D;
#pragma unroll
  for (int e = 0; e < 16; ++e) {
    const int f = e * SPAN / 16 + D;
    if (f + 2 == m) {
      if (e & 1) {
        l1 += sp.e[e]; PIN(l1);
        unsigned w = pk2(sp.e[e - 1], sp.e[e]); PIN(w);
        if (e < 8) pa[e >> 1] = w; else pb[(e - 8) >> 1] = w;
      } else { l0 += sp.e[e]; PIN(l0); }
    }
  }
#pragma unroll
  for (int e = 0; e < 16; ++e) if (e * SPAN / 16 + D + 1 == m) sp.e[e] = __builtin_amdgcn_exp2f(sp.x[e]);
#pragma unroll
  for (int e = 0; e < 16; ++e) if (e * SPAN / 16 + D == m) {
    if (ROUND_S == 0) sp.x[e] = __builtin_fmaf(S[e], SM_C, negmc);
    else if (ROUND_S == 1) sp.x[e] = __builtin_fmaf(sp.r[e], SM_C, negmc);
    else sp.x[e] = __builtin_fmaf(rb(sp.r[e]), 1.4426950408889634f, negmc);
  }
  if (ROUND_S) {
#pragma unroll
    for (int e = 0; e < 16; ++e) if (e * SPAN / 16 == m) sp.r[e] = (ROUND_S == 1) ? rb(S[e]) : rb(S[e]) * 0.08838834764831845f;
  }
}
DI void pack_acc16(const f32x4 (&acc)[4][4], int qt, u32x4& w0, u32x4& w1) {
#pragma unroll
  for (int rg = 0; rg < 2; ++rg) {
    w0[2 * rg] = pk2(acc[0][qt][rg], acc[1][qt][rg]); w0[2 * rg + 1] = pk2(acc[2][qt][rg], acc[3][qt][rg]);
    w1[2 * rg] = pk2(acc[0][qt][2 + rg], acc[1][qt][2 + rg]); w1[2 * rg + 1] = pk2(acc[2][qt][2 + rg], acc[3][qt][2 + rg]);
  }
}

typedef __attribute__((address_space(1))) unsigned gu32;
typedef __attribute__((address_space(1))) unsigned long long gu64;
constexpr unsigned SPIN_CAP = 1u << 20;
struct K1Args { const u16 *x, *kc, *vc, *wo, *w2, *w3, *w4; const float* lnst; char* ws; u16* out; };

__global__ __launch_bounds__(512, 2) __attribute__((amdgpu_waves_per_eu(2, 2)))
void k1_attn(const K1Args p) {
  extern __shared__ __attribute__((aligned(16))) char lds[];
  char* __restrict__ ws = p.ws;
  const int tid = threadIdx.x, lane = tid & 63;
  const int wid = (int)rfl((unsigned)tid >> 6);
  const int g = blockIdx.x & 7, rbk = blockIdx.x >> 3;
  const int a = lane & 15, g4 = lane >> 4, r32 = lane & 31, hh = lane >> 5;
  const unsigned ldsb = (unsigned)(size_t)lds;
  const unsigned fo = lane * 16;
  RT(1, 0);
  STAMP(0);
  const rsrc_t rK = mk_rsrc(p.kc + ((size_t)g * SK + (SK - WIN)) * DH, WIN * DH * 2), rV = mk_rsrc(p.vc + ((size_t)g * SK + (SK - WIN)) * DH, WIN * DH * 2);
  const unsigned drow = wid * 8 + ((lane >> 2) & 7);
  const unsigned kvoff = drow * 256 + (lane >> 5) * 64 + (((lane & 3) ^ ((drow >> 2) & 3)) * 16);
  const unsigned vvoff = drow * 256 + (lane >> 5) * 64 + (lane & 3) * 16;
  const unsigned kdst = ldsb + L1_K + wid * 2048, vdst = ldsb + L1_V + wid * 2048;
  auto dmaK = [&](int t) AI { dma16(kdst + (t % 3) * 16384, kvoff, rK, t * 16384); dma16(kdst + (t % 3) * 16384 + 1024, kvoff, rK, t * 16384 + 128); };
  auto dmaV = [&](int t) AI { dma16(vdst + (t & 1) * 16384, vvoff, rV, t * 16384); dma16(vdst + (t & 1) * 16384 + 1024, vvoff, rV, t * 16384 + 128); };
  constexpr int DEPTH = 6;
  bf16x8 ring[DEPTH][4];
  const rsrc_t rWq = mk_rsrc(ws + WS_WQT + (size_t)(g * 8 + wid) * 65536, 65536);
  const int rot = ROT ? (rbk & 15) : 0;
  auto wq_load = [&](int s, int i) AI { return ldfrag(rWq, fo + i * 1024, ((s + rot) & 15) * 4096); };
  {
    const u32x4 sav = *(const u32x4*)(ws + WS_MOD + fo), cav = *(const u32x4*)(ws + WS_MOD + 1024 + fo);
    u32x4 xv[8];
#pragma unroll
    for (int i = 0; i < 8; ++i) xv[i] = *(const u32x4*)((const char*)p.x + (size_t)(rbk * 64 + wid * 8 + i) * 1024 + fo);
    float st[16];
#pragma unroll
    for (int i = 0; i < 16; ++i) st[i] = p.lnst[(rbk * 64 + wid * 8) * 2 + i];
    SCHED_FENCE();
    if (K0_WARM == 3) dma16(kdst, fo, wid < 4 ? rK : rV, (rbk * 4 + (wid & 3)) * 1024);
    SCHED_FENCE();
    float ca[8], sa[8];
#pragma unroll
    for (int e = 0; e < 4; ++e) { ca[2 * e] = bflo(cav[e]); ca[2 * e + 1] = bfhi(cav[e]); sa[2 * e] = bflo(sav[e]); sa[2 * e + 1] = bfhi(sav[e]); }
    if (PRIO_P && wid >= 4) __builtin_amdgcn_s_setprio(1);
#pragma unroll
    for (int hf = 0; hf < 2; ++hf) {
      float lo[16], hi[16], rr[4]; unsigned t[16];
#define H_STAGE(body) _Pragma("unroll") for (int j = 0; j < 16; ++j) { const int i = hf * 4 + (j >> 2), d = j & 3; (void)i; (void)d; body; } SCHED_FENCE()
#pragma unroll
      for (int i = 0; i < 4; ++i) rr[i] = __builtin_amdgcn_rcpf(st[2 * (hf * 4 + i) + 1]);
      H_STAGE(lo[j] = bflo(xv[i][d]); hi[j] = bfhi(xv[i][d]));
      H_STAGE(lo[j] -= st[2 * i]; hi[j] -= st[2 * i]);
      H_STAGE(lo[j] = rb(lo[j]); hi[j] = rb(hi[j]));
      if (PRIO_P && hf == 1) __builtin_amdgcn_s_setprio(0);
      H_STAGE(lo[j] *= rr[j >> 2]; hi[j] *= rr[j >> 2]);
      H_STAGE(lo[j] = rb(lo[j]); hi[j] = rb(hi[j]));
#pragma unroll
      for (int i = 0; i < 4; ++i) ring[hf][i] = wq_load(hf, i);
      H_STAGE(lo[j] *= ca[2 * d]; hi[j] *= ca[2 * d + 1]);
      H_STAGE(lo[j] = rb(lo[j]); hi[j] = rb(hi[j]));
      H_STAGE(lo[j] += sa[2 * d]; hi[j] += sa[2 * d + 1]);
      H_STAGE(t[j] = pk2(lo[j], hi[j]));
#pragma unroll
      for (int i = 0; i < 4; ++i) *(u32x4*)(lds + L1_HT + (wid * 8 + hf * 4 + i) * HT_STRIDE + fo) = (u32x4){t[4 * i], t[4 * i + 1], t[4 * i + 2], t[4 * i + 3]};
#pragma unroll
      for (int i = 0; i < 2; ++i) ring[2][hf * 2 + i] = wq_load(2, hf * 2 + i);
      SCHED_FENCE();
    }
  }

  Retile rtl; const u16* rsrc_w; int rld, rk0, rn0; bool rperm; size_t rdst; unsigned rhalf = 2048;
  if (wid < 4) { const int id = rbk * 4 + wid, s = id >> 3, ng = id & 7; rsrc_w = p.wo; rld = 512; rk0 = g * 512 + 32 * s; rn0 = 64 * ng; rperm = false; rdst = WS_WOT + (size_t)(g * 8 + ng) * 65536 + s * 2048; rhalf = 32768; }
  else {
    int id = blockIdx.x * 4 + wid - 4, s, ng; rperm = true;
    if (id < 128)      { ng = id >> 4; s = id & 15; rsrc_w = p.w2; rld = 512; rdst = WS_W2T + (size_t)id * 4096; }
    else if (id < 576) { id -= 128; ng = id >> 4; s = id & 15; rsrc_w = p.w3; rld = F_; rdst = WS_W3T + (size_t)id * 4096; }
    else               { id -= 576; ng = id & 7; s = id >> 3; rsrc_w = p.w4; rld = 512; rdst = WS_W4T + (size_t)(ng * 56 + s) * 4096; }
    rk0 = 64 * (s >> 1) + 2 * (s & 1); rn0 = 64 * ng;
  }
  gu32* flags = (gu32*)(ws + WS_FLAG);
  gu32* flag = flags + (FL_W + g) * 32;
  gu32* flagw = flags + (FL_W + (lane & 7)) * 32;
  STAMP(1);
  __syncthreads();
  STAMP(2);
  if (wid >= 4 && PRIO_Q > 0) __builtin_amdgcn_s_setprio(1);

  const char* bl = lds + L1_HT + a * HT_STRIDE + g4 * 16;
  auto bread = [&](int s, int qt) AI { return *(const bf16x8*)(bl + 64 * ((s + rot) & 15) + qt * 16 * HT_STRIDE); };
  f32x16 o[4]; bf16x8 qf[8]; float l0, l1, negmc;
  {
    f32x4 acc[1][4][4];
    gemm_steps<4, 4, 16, DEPTH, 1>(acc, ring, wq_load, bread, [&](auto S_, auto M_) AI {
      constexpr int s = S_.value, m = M_.value;
      if constexpr (s < 3 && (m & 3) == 1) ring[3 + s][m >> 2] = wq_load(3 + s, m >> 2);
      if constexpr (s == PRIO_Q && m == 0) __builtin_amdgcn_s_setprio(0);
      if constexpr (s == 11 && m == 8) { WAIT_VM0(); dmaK(0); }
      if constexpr (s == 12 && m == 0) dmaK(1);
      if constexpr (s == 12 && m == 8) dmaV(0);
      if constexpr (s == 13 && m == 0) dmaK(2);
      if constexpr (s == 13 && m == 8) dmaV(1);
    });
    STAMP(3);
    __syncthreads();
#pragma unroll
    for (int qt = 0; qt < 4; ++qt) {
      u32x4 w0, w1; pack_acc16(acc[0], qt, w0, w1);
      char* dst = lds + L1_HT + ((wid >> 1) * 64 + qt * 16 + a) * QX_STRIDE + ((wid & 1) * 64 + g4 * 16) * 2;
      *(u32x4*)dst = w0; *(u32x4*)(dst + 16) = w1;
    }
    asm volatile("s_waitcnt vmcnt(0)" ::: "memory");
    __syncthreads();
#pragma unroll
    for (int s = 0; s < 8; ++s) qf[s] = *(const bf16x8*)(lds + L1_HT + ((wid >> 1) * 64 + (wid & 1) * 32 + r32) * QX_STRIDE + 32 * s + 16 * hh);
  }
  STAMP(4);

  const unsigned kx = (r32 >> 2) & 3;
  const char* kb0 = lds + L1_K + 2048 * (r32 >> 3) + 64 * (r32 & 7) + 16 * ((0 + hh) ^ kx);
  const char* kb1 = lds + L1_K + 2048 * (r32 >> 3) + 64 * (r32 & 7) + 16 * ((2 + hh) ^ kx);
  const char* vb = lds + L1_V + (4 * hh + ((lane >> 2) & 3)) * 64 + ((lane >> 4) & 1) * 32 + (lane & 3) * 8;
  auto kfrag = [&](int t, int m) AI { return *(const bf16x8*)(((m & 1) ? kb1 : kb0) + (t % 3) * 16384 + 512 * ((m & 7) >> 1) + 8192 * (m >> 3)); };
  auto vfrag = [&](int t, int m) AI {
    const char* q = vb + (t & 1) * 16384 + (m >> 2) * 4096 + (m & 3) * 512;
    bf16x4 lo = __builtin_amdgcn_ds_read_tr16_b64_v4bf16(LDSP(bf16x4, q)), hi = __builtin_amdgcn_ds_read_tr16_b64_v4bf16(LDSP(bf16x4, q + 2048));
    return __builtin_shufflevector(lo, hi, 0, 1, 2, 3, 4, 5, 6, 7);
  };
  auto fixup = [&](const f32x16& S, float l0p, float l1p, u32x4& pa, u32x4& pb, bool pend, u32x4& pend0, u32x4& pend1) AI {
    const float mx = swap32_max(sm_arg(max16(S), negmc));
    const float dm = fmaxf(__builtin_ceilf(mx) + (float)SM_SHIFT, 0.f);
    const float f = __builtin_amdgcn_exp2f(-dm);
    negmc -= dm; l0 = l0p * f; l1 = l1p * f;
#pragma unroll
    for (int db = 0; db < 4; ++db) o[db] *= f;
    if (pend) {
#pragma unroll
      for (int q = 0; q < 4; ++q) { pend0[q] = scale_pk(pend0[q], f); pend1[q] = scale_pk(pend1[q], f); }
    }
    sm_half(S, negmc, l0, l1, pa, pb);
  };

  f32x16 Sa0, Sa1, Sb0, Sb1;
  u32x4 Pa0, Pa1, Pb0, Pb1, P2, P3;
  bf16x8 kf[4], vf[4];
  {
#pragma unroll
    for (int m = 0; m < 16; ++m) {
      const bf16x8 k = kfrag(0, m);
      if (m < 8) Sa0 = MFMA32(k, qf[m & 7], m == 0 ? (f32x16){} : Sa0); else Sa1 = MFMA32(k, qf[m & 7], m == 8 ? (f32x16){} : Sa1);
    }
  }
#pragma unroll
  for (int m = 0; m < 3; ++m) kf[m] = kfrag(1, m);
  negmc = -sm_arg(swap32_max(fmaxf(max16(Sa0), max16(Sa1))), (float)SM_SHIFT);
  l0 = 0.f; l1 = 0.f;
  sm_half(Sa0, negmc, l0, l1, Pa0, Pa1);
#pragma unroll
  for (int db = 0; db < 4; ++db) o[db] = (f32x16){};
  SCHED_FENCE();

  const rsrc_t rWo = mk_rsrc(ws + WS_WOT + (size_t)(g * 8 + wid) * 65536, 65536);
  const rsrc_t rRt = mk_rsrc(ws + rdst, 36864);
  bf16x8 ring2[12][2];
  auto wo_load = [&](int sg, int i) AI { return __builtin_bit_cast(bf16x8, __builtin_amdgcn_raw_buffer_load_b128(rWo, fo + i * 1024, (sg >> 4) * 32768 + ((sg + rot) & 15) * 2048, 16)); };
  unsigned flagv = 0;

  auto tile = [&](auto T_, const f32x16& C1, f32x16& N0, f32x16& N1, u32x4& Pc0, u32x4& Pc1, u32x4& Pn0, u32x4& Pn1) AI {
    constexpr int T = T_.value;
    if (PRIO_A && wid >= 4) __builtin_amdgcn_s_setprio(1);
    {
      const float l0p = l0, l1p = l1; SmPipe sp;
#pragma unroll
      for (int m = 0; m < 16; ++m) {
        if (T < 7) {
          if (m < 8) N0 = MFMA32(kf[m & 3], qf[m & 7], m == 0 ? (f32x16){} : N0); else N1 = MFMA32(kf[m & 3], qf[m & 7], m == 8 ? (f32x16){} : N1);
          if (m + 3 < 16) kf[(m + 3) & 3] = kfrag(T + 1, m + 3);
        }
        if (m >= 13) vf[m - 13] = vfrag(T, m - 13);
        if (!(ABL & 2)) sm_gap(m, sp, C1, negmc, l0, l1, P2, P3); else if (m == 0) { P2 = Pc0; P3 = Pc1; asm volatile("" :: "v"(C1)); }
        if (T == 7) ring2[m >> 1][m & 1] = wo_load(m >> 1, m & 1);
        if (T < 7) SCHED_FENCE();
      }
      MEM_FENCE();
      if (__builtin_expect(__any(!(l0 + l1 <= SM_LIMIT)), 0)) fixup(C1, l0p, l1p, P2, P3, true, Pc0, Pc1);
    }
    if (PRIO_A == 1) __builtin_amdgcn_s_setprio(0);
    {
      const float l0p = l0, l1p = l1; SmPipe sp;
#pragma unroll
      for (int m = 0; m < 16; ++m) {
        const u32x4 pp = (m < 4) ? Pc0 : (m < 8) ? Pc1 : (m < 12) ? P2 : P3;
        o[m & 3] = MFMA32(vf[m & 3], __builtin_bit_cast(bf16x8, pp), o[m & 3]);
        if (m + 3 < 16) vf[(m + 3) & 3] = vfrag(T, m + 3);
        if (T < 7 && !(ABL & 2)) sm_gap(m, sp, N0, negmc, l0, l1, Pn0, Pn1); else if (T < 7 && m == 0) { Pn0 = Pc0; Pn1 = Pc1; asm volatile("" :: "v"(N0)); }
        if (T == 7 && m < 8) ring2[8 + (m >> 1)][m & 1] = wo_load(8 + (m >> 1), m & 1);
        if (PRIO_A == 2 && m == 4) __builtin_amdgcn_s_setprio(0);
        if (T < 7 && m == 13) {
          if (T == 6 && wid == 0) {
            for (unsigned spins = 0; !__all(flagv == 32u) && spins < SPIN_CAP; ++spins) { __builtin_amdgcn_s_sleep(2); flagv = __hip_atomic_load(flagw, __ATOMIC_RELAXED, __HIP_MEMORY_SCOPE_AGENT); }
          }
          if (T == 2) WAIT_VM0();
          if (T == 0)      asm volatile("s_waitcnt lgkmcnt(0)" : "+v"(vf[2]), "+v"(vf[3])::"memory");
          else if (T == 1) asm volatile("s_waitcnt vmcnt(8) lgkmcnt(0)" : "+v"(vf[2]), "+v"(vf[3])::"memory");
          else if (T == 3) asm volatile("s_waitcnt vmcnt(4) lgkmcnt(0)" : "+v"(vf[2]), "+v"(vf[3])::"memory");
          else             asm volatile("s_waitcnt vmcnt(0) lgkmcnt(0)" : "+v"(vf[2]), "+v"(vf[3])::"memory");
          if (!(ABL & 1)) __builtin_amdgcn_s_barrier();
          MEM_FENCE();
          if (T == 4 && tid == 0) __hip_atomic_fetch_add(flag, 1u, __ATOMIC_RELAXED, __HIP_MEMORY_SCOPE_AGENT);
          if (T == 5 && wid == 0) flagv = __hip_atomic_load(flagw, __ATOMIC_RELAXED, __HIP_MEMORY_SCOPE_AGENT);
        }
        if (T < 6 && m == 13) { kf[0] = kfrag(T + 2, 0); kf[1] = kfrag(T + 2, 1); }
        if (T < 6 && m == 14) kf[2] = kfrag(T + 2, 2);
        if (T < 5 && m == 14) dmaK(T + 3);
        if (T < 6 && m == 15) dmaV(T + 2);
        if (T == 0 && m == 15) { retile_load(rtl, rsrc_w, rld, rk0, rperm, rn0, lane); MEM_FENCE(); }
        if (T == 2 && m == 15) {
#pragma unroll
          for (int i = 0; i < 4; ++i) __builtin_amdgcn_raw_buffer_store_b128(retile_frag(rtl, i), rRt, fo + (i >> 1) * rhalf + (i & 1) * 1024, 0, 16);
        }
        SCHED_FENCE();
      }
      MEM_FENCE();
      if (T < 7) {
        if (__builtin_expect(__any(!(l0 + l1 <= SM_LIMIT)), 0)) fixup(N0, l0p, l1p, Pn0, Pn1, false, Pc0, Pc1);
      }
    }
  };
  STAMP(5);
  tile(IC<0>{}, Sa1, Sb0, Sb1, Pa0, Pa1, Pb0, Pb1);
  tile(IC<1>{}, Sb1, Sa0, Sa1, Pb0, Pb1, Pa0, Pa1);
  tile(IC<2>{}, Sa1, Sb0, Sb1, Pa0, Pa1, Pb0, Pb1);
  tile(IC<3>{}, Sb1, Sa0, Sa1, Pb0, Pb1, Pa0, Pa1);
  tile(IC<4>{}, Sa1, Sb0, Sb1, Pa0, Pa1, Pb0, Pb1);
  tile(IC<5>{}, Sb1, Sa0, Sa1, Pb0, Pb1, Pa0, Pa1);
  STAMP(6);
  tile(IC<6>{}, Sa1, Sb0, Sb1, Pa0, Pa1, Pb0, Pb1);
  tile(IC<7>{}, Sb1, Sa0, Sa1, Pb0, Pb1, Pa0, Pa1);
  STAMP(7);

  {
    const float inv = 1.f / swap32_add(l0 + l1);
    char* dst = lds + L1_HT + ((wid & 1) * 32 + r32) * HT_STRIDE + ((wid >> 1) * 128 + 4 * hh) * 2;
#pragma unroll
    for (int db = 0; db < 4; ++db)
#pragma unroll
      for (int gq = 0; gq < 4; ++gq) {
        u32x2 w = {pk2(o[db][4 * gq] * inv, o[db][4 * gq + 1] * inv), pk2(o[db][4 * gq + 2] * inv, o[db][4 * gq + 3] * inv)};
        *(u32x2*)(dst + (32 * db + 8 * gq) * 2) = w;
      }
  }
  __syncthreads();
  STAMP(8);
  if (wid >= 4 && PRIO_O > 0) __builtin_amdgcn_s_setprio(1);
  {
    f32x4 acc[2][2][4];
    const rsrc_t rP = mk_rsrc(ws + WS_PART + (size_t)((rbk * 8 + g) * 8 + wid) * 8192, 8192);
    auto pstore = [&](int ps, int qt) AI {
      u32x4 w;
#pragma unroll
      for (int r = 0; r < 4; ++r) w[r] = pk2(acc[ps][0][qt][r], acc[ps][1][qt][r]);
      __builtin_amdgcn_raw_buffer_store_b128(w, rP, fo + (qt * 2 + ps) * 1024, 0, PART_AUX);
    };
    gemm_steps<2, 4, 32, 12, 2, 2>(acc, ring2, wo_load, bread, [&](auto S_, auto M_) AI {
      if constexpr (S_.value == PRIO_O && M_.value == 0) __builtin_amdgcn_s_setprio(0);
      if constexpr (S_.value >= 20 && S_.value < 22 && (M_.value & 3) == 1) pstore(0, (S_.value - 20) * 2 + (M_.value >> 2));
      if constexpr (S_.value == P0_STEP && M_.value == 7) {
        WAIT_VM0(); asm volatile("s_waitcnt vmcnt(0)" ::: "memory"); __builtin_amdgcn_s_barrier(); MEM_FENCE();
        if (tid == 0) __hip_atomic_fetch_add(flags + (FL_P0 + rbk) * 32, 1u, __ATOMIC_RELAXED, __HIP_MEMORY_SCOPE_AGENT);
      } });
    STAMP(9);
#pragma unroll
    for (int qt = 0; qt < 4; ++qt) pstore(1, qt);
  }
  STAMP(10);

  auto tail = [&](const int lane, const int a, const int g4, const unsigned fo, const int tid, const char* bl) AI {
  const rsrc_t rWs = mk_rsrc(ws, (unsigned)WS_END);
  auto ld1 = [&](size_t off, unsigned soff) AI { return __builtin_amdgcn_raw_buffer_load_b128(rWs, (unsigned)off, soff, 16); };
  auto ldf1 = [&](size_t off, unsigned soff) AI { return __builtin_bit_cast(bf16x8, __builtin_amdgcn_raw_buffer_load_b128(rWs, (unsigned)off, soff, 16)); };
  auto st1 = [&](size_t off, u32x4 v) AI { __builtin_amdgcn_raw_buffer_store_b128(v, rWs, (unsigned)off, 0, 16); };
  auto publish = [&](int idx) AI {
    asm volatile("s_waitcnt vmcnt(0) lgkmcnt(0)" ::: "memory"); __builtin_amdgcn_s_barrier(); MEM_FENCE();
    if (tid == 0) __hip_atomic_fetch_add(flags + idx * 32, 1u, __ATOMIC_RELAXED, __HIP_MEMORY_SCOPE_AGENT);
  };
  auto await = [&](int pw, int idx, unsigned want) AI {
    if (wid == pw) {
      unsigned spins = 0;
      while (!__all(__hip_atomic_load(flags + idx * 32, __ATOMIC_RELAXED, __HIP_MEMORY_SCOPE_AGENT) >= want) && ++spins < SPIN_CAP) __builtin_amdgcn_s_sleep(1);
    }
    MEM_FENCE(); __builtin_amdgcn_s_barrier(); MEM_FENCE();
  };
  const int b = blockIdx.x;
  const u16* modb = (const u16*)(ws + WS_MOD);
  publish(FL_P + rbk);
  STAMP(11);
  RT(1, 1);

  RT(2, 0);
  STAMPK(2, 0);
  {
    f32x2* red = (f32x2*)(lds + 16 * HT_STRIDE);
    const int rt = (b & 7) * 16 + (b >> 4), nh = (b >> 3) & 1;
    const int prb = rt >> 2, qt = rt & 3, row = rt * 16 + a, col = wid * 64 + g4 * 16;
    constexpr int D1 = 12, D1E = 12;
    bf16x8 A[D1][2];
    const int G = nh * 4 + (wid >> 1), ps = wid & 1;
    const rsrc_t rW = mk_rsrc(ws + WS_W1T + (size_t)G * 65536 + ps * 2048, 65536);
    auto aload = [&](int s, int i) AI { return ldfrag(rW, fo, (s * 4 + i) * 1024); };
    u32x4 pv[8][2];
    const size_t poff = WS_PART + (size_t)((prb * 64 + wid) * 8 + qt * 2) * 1024 + fo;
    const int prot = 0;
    await(0, FL_P0 + prb, 8);
    if (wid != 7) {
#pragma unroll
      for (int gg = 0; gg < 8; ++gg) pv[gg][0] = ld1(poff, (((gg + prot) & 7) * 64) * 1024);
    }
    SCHED_FENCE();
    await(7, FL_P + prb, 8);
    STAMPK(2, 1);
#pragma unroll
    for (int gg = 0; gg < 8; ++gg) pv[gg][1] = ld1(poff, (((gg + prot) & 7) * 64 + 1) * 1024);
    if (wid == 7) {
#pragma unroll
      for (int gg = 0; gg < 8; ++gg) pv[gg][0] = ld1(poff, (((gg + prot) & 7) * 64) * 1024);
    }
    u32x4 xv[2], gav[2], cbv[2], sbv[2];
#pragma unroll
    for (int h = 0; h < 2; ++h) {
      xv[h] = *(const u32x4*)(p.x + (size_t)row * D_ + col + 8 * h);
      gav[h] = *(const u32x4*)(modb + 1024 + col + 8 * h); sbv[h] = *(const u32x4*)(modb + 1536 + col + 8 * h); cbv[h] = *(const u32x4*)(modb + 2048 + col + 8 * h);
    }
    SCHED_FENCE();
    __builtin_amdgcn_s_barrier();
#pragma unroll
    for (int s = 0; s < D1E; ++s) { A[s][0] = aload(s, 0); A[s][1] = aload(s, 1); }
    SCHED_FENCE();
    float v[16];
#pragma unroll
    for (int d = 0; d < 8; ++d) { v[2 * d] = bflo(pv[0][d & 1][d >> 1]); v[2 * d + 1] = bfhi(pv[0][d & 1][d >> 1]); }
#pragma unroll
    for (int gg = 1; gg < 8; ++gg) {
#pragma unroll
      for (int d = 0; d < 8; ++d) { v[2 * d] += bflo(pv[gg][d & 1][d >> 1]); v[2 * d + 1] += bfhi(pv[gg][d & 1][d >> 1]); }
      SCHED_FENCE();
    }
#pragma unroll
    for (int d = 0; d < 8; ++d) {
      const unsigned ga = gav[d >> 2][d & 3], xx = xv[d >> 2][d & 3];
      const unsigned x2 = pk2(__builtin_fmaf(bflo(ga), v[2 * d], bflo(xx)), __builtin_fmaf(bfhi(ga), v[2 * d + 1], bfhi(xx)));
      xv[d >> 2][d & 3] = x2; v[2 * d] = bflo(x2); v[2 * d + 1] = bfhi(x2);
    }
    if (nh == 0) { st1(WS_X2 + (size_t)row * 1024 + col * 2, xv[0]); st1(WS_X2 + (size_t)row * 1024 + col * 2 + 16, xv[1]); }
    STAMPK(2, 2);
    auto row_stats = [&](f32x2* rd, float& mean, float& rstd) AI {
      float s0 = 0, s1 = 0, q0 = 0, q1 = 0;
#pragma unroll
      for (int e = 0; e < 8; ++e) { s0 += v[2 * e]; s1 += v[2 * e + 1]; q0 = __builtin_fmaf(v[2 * e], v[2 * e], q0); q1 = __builtin_fmaf(v[2 * e + 1], v[2 * e + 1], q1); }
      float s = swap32_add(swap16_add(s0 + s1)), q = swap32_add(swap16_add(q0 + q1));
      if (g4 == 0) rd[wid * 16 + a] = (f32x2){s, q};
      asm volatile("s_waitcnt lgkmcnt(0)" ::: "memory"); __builtin_amdgcn_s_barrier(); MEM_FENCE();
      f32x2 t = rd[a];
#pragma unroll
      for (int w = 1; w < 8; ++w) t += rd[w * 16 + a];
      mean = t[0] * (1.f / 512.f);
      rstd = __builtin_amdgcn_rsqf(fmaxf(t[1] * (1.f / 512.f) - mean * mean, 0.f) + 1e-5f);
    };
    float mean, rstd;
#pragma unroll
    for (int s = D1E; s < D1; ++s) { A[s][0] = aload(s, 0); A[s][1] = aload(s, 1); }
    SCHED_FENCE();
    row_stats(red, mean, rstd);
    u32x4 hv[2];
#pragma unroll
    for (int d = 0; d < 8; ++d) {
      const unsigned cb = cbv[d >> 2][d & 3], sb = sbv[d >> 2][d & 3];
      const unsigned hb = pk2((v[2 * d] - mean) * rstd * bflo(cb) + bflo(sb), (v[2 * d + 1] - mean) * rstd * bfhi(cb) + bfhi(sb));
      hv[d & 1][d >> 1] = hb; v[2 * d] = bflo(hb); v[2 * d + 1] = bfhi(hb);
    }
    if (nh == 1) { st1(WS_HB + (size_t)row * 1024 + wid * 128 + g4 * 16, hv[0]); st1(WS_HB + (size_t)row * 1024 + wid * 128 + 64 + g4 * 16, hv[1]); }
    row_stats(red + 128, mean, rstd);
    const float nm = -mean * rstd;
#pragma unroll
    for (int h = 0; h < 2; ++h) {
      u32x4 o;
#pragma unroll
      for (int e = 0; e < 4; ++e) o[e] = pk2(__builtin_fmaf(v[8 * h + 2 * e], rstd, nm), __builtin_fmaf(v[8 * h + 2 * e + 1], rstd, nm));
      *(u32x4*)(lds + a * HT_STRIDE + (col + 8 * h) * 2) = o;
    }
    STAMPK(2, 3);
    asm volatile("s_waitcnt lgkmcnt(0)" ::: "memory"); __builtin_amdgcn_s_barrier(); MEM_FENCE();
    STAMPK(2, 4);
    f32x4 acc[1][2][1];
    gemm_steps<2, 1, 16, D1, 1, 4>(acc, A, aload, [&](int s, int) AI { return *(const bf16x8*)(bl + 64 * s); }, [](auto, auto) AI {});
    STAMPK(2, 5);
    u32x4 ov;
#pragma unroll
    for (int rg = 0; rg < 4; ++rg) ov[rg] = pk2(gelu_tanh(acc[0][0][0][rg]), gelu_tanh(acc[0][1][0][rg]));
    st1(WS_H1 + (size_t)(rt * 16 + 2 * G + ps) * 1024 + fo, ov);
    STAMPK(2, 6);
    publish(FL_H1 + rt);
    RT(2, 1);
    STAMPK(2, 7);
  }

  const int rb2 = (b & 7) * 4 + (b >> 6), ns = (b >> 3) & 7;
  const int ng = ns * 7 + (wid < 7 ? wid : 6);
  const size_t w3off = WS_W3T + (size_t)(ng >> 1) * 65536 + (ng & 1) * 2048 + fo;
  bf16x8 A[16][2];
  RT(3, 0);
  STAMPK(3, 0);
  {
    const int rt2 = (b & 7) * 8 + (b >> 5), nq = (b >> 3) & 3;
    const int G = nq * 2 + ((wid & 3) >> 1), ps = wid & 1;
    if (wid < 4) {
      const size_t aoff = WS_W2T + (size_t)G * 65536 + ps * 2048 + fo;
#pragma unroll
      for (int s = 0; s < 16; ++s) { A[s][0] = ldf1(aoff, (s * 4) * 1024); A[s][1] = ldf1(aoff, (s * 4 + 1) * 1024); }
    }
    SCHED_FENCE();
    { const int t = 2 * rt2 - 1 + (lane & 3); await(7, FL_H1 + (t < 0 ? 0 : t > 127 ? 127 : t), 2); }
    STAMPK(3, 1);
    const rsrc_t rH = mk_rsrc(ws + WS_H1, (unsigned)NXB);
    const int sh = (wid < 2) ? -1 : (wid < 4) ? 1 : (wid < 6) ? -2 : 2;
    u32x4 hf[2][2], hbv[2];
#pragma unroll
    for (int qt = 0; qt < 2; ++qt) {
      const int r = rt2 * 32 + qt * 16 + a + sh;
      const unsigned ho = (unsigned)((r >> 4) * 16384 + ((r & 15) + 16 * g4) * 16);
      hf[qt][0] = __builtin_amdgcn_raw_buffer_load_b128(rH, ho, (wid * 2) * 1024, 16); hf[qt][1] = __builtin_amdgcn_raw_buffer_load_b128(rH, ho, (wid * 2 + 1) * 1024, 16);
    }
    const size_t xoff = (size_t)(rt2 * 32 + a) * 1024 + G * 128 + ps * 64 + g4 * 16;
    if (wid < 4) { hbv[0] = ld1(WS_HB + xoff, 0); hbv[1] = ld1(WS_HB + xoff, 16384); }
#pragma unroll
    for (int qt = 0; qt < 2; ++qt) { *(u32x4*)(lds + (qt * 16 + wid * 2) * 1024 + fo) = hf[qt][0]; *(u32x4*)(lds + (qt * 16 + wid * 2 + 1) * 1024 + fo) = hf[qt][1]; }
    asm volatile("s_waitcnt lgkmcnt(0)" ::: "memory"); __builtin_amdgcn_s_barrier(); MEM_FENCE();
    STAMPK(3, 2);
    f32x2* red = (f32x2*)(lds + 32768);
    if (wid < 4) {
      f32x4 acc[1][2][2];
      gemm_steps<2, 2, 16, 16, 1, 3>(acc, A, [&](int, int) AI { return bf16x8{}; }, [&](int s, int qt) AI { return *(const bf16x8*)(lds + (qt * 16 + s) * 1024 + fo); }, [](auto, auto) AI {});
#pragma unroll
      for (int qt = 0; qt < 2; ++qt) {
        u32x4 ov; float s = 0, q = 0;
#pragma unroll
        for (int rg = 0; rg < 4; ++rg) {
          ov[rg] = pk2(bflo(hbv[qt][rg]) + acc[0][0][qt][rg], bfhi(hbv[qt][rg]) + acc[0][1][qt][rg]);
          const float lo = bflo(ov[rg]), hi = bfhi(ov[rg]); s += lo + hi; q += lo * lo + hi * hi;
        }
        st1(WS_X1 + xoff + qt * 16384, ov);
        s = swap32_add(swap16_add(s)); q = swap32_add(swap16_add(q));
        if (g4 == 0) red[wid * 32 + qt * 16 + a] = (f32x2){s, q};
      }
    } else if (wid < 7) {
#pragma unroll
      for (int s = 0; s < 16; ++s) { A[s][0] = ldf1(w3off, (s * 4) * 1024); A[s][1] = ldf1(w3off, (s * 4 + 1) * 1024); }
    }
    asm volatile("s_waitcnt lgkmcnt(0)" ::: "memory"); __builtin_amdgcn_s_barrier(); MEM_FENCE();
    if (tid < 32) {
      const f32x2 t = red[tid] + red[32 + tid] + red[64 + tid] + red[96 + tid];
      __hip_atomic_store((gu64*)(ws + WS_ST + (size_t)(rt2 * 32 + tid) * 32 + nq * 8), __builtin_bit_cast(unsigned long long, t), __ATOMIC_RELAXED, __HIP_MEMORY_SCOPE_AGENT);
    }
    STAMPK(3, 3);
    publish(FL_X1 + (rt2 >> 1));
    RT(3, 1);
    STAMPK(3, 4);
  }

  bf16x8 W[7][4];
  const size_t woff = WS_W4T + (size_t)(ns * 56 + wid * 7) * 4096 + fo;
  auto rot7 = [&](int s) AI { const int t = s + (ROT ? (ns < 7 ? ns : 0) : 0); return t >= 7 ? t - 7 : t; };
  RT(4, 0);
  STAMPK(4, 0);
  {
    if (wid < 4) {
#pragma unroll
      for (int s = 0; s < 16; ++s) { A[s][0] = ldf1(w3off, (s * 4) * 1024); A[s][1] = ldf1(w3off, (s * 4 + 1) * 1024); }
    }
    SCHED_FENCE();
    await(7, FL_X1 + rb2, 8);
    STAMPK(4, 1);
    u32x4 xv[8];
    const u32x4 stv = ld1(WS_ST + (size_t)(rb2 * 64 + wid * 8) * 32 + (lane & 15) * 16, 0);
#pragma unroll
    for (int i = 0; i < 8; ++i) xv[i] = ld1(WS_X1 + (size_t)(rb2 * 64 + wid * 8) * 1024 + fo, i * 1024);
    SCHED_FENCE();
    const float sm = dpp_add<0xB1>(__uint_as_float(stv[0]) + __uint_as_float(stv[2])) * (1.f / 512.f);
    const float sq = dpp_add<0xB1>(__uint_as_float(stv[1]) + __uint_as_float(stv[3])) * (1.f / 512.f);
    const float rsl = __builtin_amdgcn_rsqf(fmaxf(sq - sm * sm, 0.f) + 1e-5f), nml = -sm * rsl;
    float rs[8], nm[8];
#pragma unroll
    for (int i = 0; i < 8; ++i) { rs[i] = __uint_as_float(__builtin_amdgcn_readlane(__float_as_uint(rsl), 2 * i)); nm[i] = __uint_as_float(__builtin_amdgcn_readlane(__float_as_uint(nml), 2 * i)); }
    SCHED_FENCE();
#pragma unroll
    for (int hf = 0; hf < 2; ++hf) {
      float lo[16], hi[16]; unsigned t[16];
      H_STAGE(lo[j] = bflo(xv[i][d]); hi[j] = bfhi(xv[i][d]));
      H_STAGE(lo[j] = __builtin_fmaf(lo[j], rs[i], nm[i]); hi[j] = __builtin_fmaf(hi[j], rs[i], nm[i]));
      H_STAGE(t[j] = pk2(lo[j], hi[j]));
#pragma unroll
      for (int i = 0; i < 4; ++i) *(u32x4*)(lds + (wid * 8 + hf * 4 + i) * HT_STRIDE + fo) = (u32x4){t[4 * i], t[4 * i + 1], t[4 * i + 2], t[4 * i + 3]};
      SCHED_FENCE();
    }
    STAMPK(4, 2);
    asm volatile("s_waitcnt lgkmcnt(0)" ::: "memory"); __builtin_amdgcn_s_barrier(); MEM_FENCE();
    STAMPK(4, 3);
    if (wid < 7) {
      f32x4 acc[2][2][2];
      if (wid >= 4) __builtin_amdgcn_s_setprio(1);
      const size_t dst = WS_U + (size_t)(rb2 * 4 * 56 + ng) * 1024 + fo;
      u32x4 ow;
      auto gpart = [&](int ps, int k) AI {
        unsigned w = pk2(gelu_tanh(acc[ps][0][k >> 2][k & 3]), gelu_tanh(acc[ps][1][k >> 2][k & 3])); PIN(w); ow[k & 3] = w;
        if ((k & 3) == 3) st1(dst + (size_t)(2 * ps + (k >> 2)) * 56 * 1024, ow);
      };
      gemm_steps<2, 2, 32, 16, 2, 3>(acc, A, [&](int s, int i) AI { return A[s & 15][i]; },
                                     [&](int s, int qt) AI { return *(const bf16x8*)(bl + ((s >> 4) * 2 + qt) * 16 * HT_STRIDE + 64 * (s & 15)); },
                                     [&](auto S_, auto M_) AI {
                                       constexpr int s = S_.value, m = M_.value;
                                       if constexpr (s == PRIO_G3 && m == 0) __builtin_amdgcn_s_setprio(0);
                                       if constexpr (s >= 17 && (s & 1) && m == 1) gpart(0, (s - 17) >> 1);
                                       if constexpr (s >= 17 && s < 31 && m >= 2) { constexpr int k = (s - 17) * 2 + m - 2; W[k >> 2][k & 3] = ldf1(woff + (k & 3) * 1024, rot7(k >> 2) * 4096); }
                                     });
      STAMPK(4, 4);
#pragma unroll
      for (int k = 0; k < 8; ++k) gpart(1, k);
    } else {
#pragma unroll
      for (int k = 0; k < 28; ++k) W[k >> 2][k & 3] = ldf1(woff + (k & 3) * 1024, rot7(k >> 2) * 4096);
    }
    STAMPK(4, 5);
    publish(FL_U + rb2);
    RT(4, 1);
    STAMPK(4, 6);
  }

  RT(5, 0);
  STAMPK(5, 0);
  {
    bf16x8 B[7][4];
    const size_t uoff = WS_U + (size_t)(rb2 * 4 * 56 + wid * 7) * 1024 + fo;
    const int oqt = wid >> 1, orp = wid & 1, orow = rb2 * 64 + oqt * 16 + a, ocol = ns * 64 + g4 * 16 + orp * 8;
    u32x2 x1a, x1b; u32x4 x2v, gbv;
    SCHED_FENCE();
    await(0, FL_U + rb2, 8);
#pragma unroll
    for (int s = 0; s < G4_AHEAD; ++s)
#pragma unroll
      for (int i = 0; i < 4; ++i) B[s][i] = ldf1(uoff, (i * 56 + rot7(s)) * 1024);
    SCHED_FENCE();
    STAMPK(5, 1);
    f32x4 acc[4][4] = {};
#pragma unroll
    for (int s = 0; s < 7; ++s) {
#pragma unroll
      for (int i = 0; i < 4; ++i) {
#pragma unroll
        for (int qt = 0; qt < 4; ++qt) acc[i][qt] = MFMA16(W[s][i], B[s][qt], acc[i][qt]);
        if (s + G4_AHEAD < 7) B[s + G4_AHEAD][i] = ldf1(uoff, (i * 56 + rot7(s + G4_AHEAD)) * 1024);
        SCHED_FENCE();
      }
      if (s == 5) {
        const size_t x1o = WS_X1 + (size_t)orow * 1024 + ns * 128 + g4 * 16 + orp * 8;
        x1a = __builtin_amdgcn_raw_buffer_load_b64(rWs, (unsigned)x1o, 0, 16); x1b = __builtin_amdgcn_raw_buffer_load_b64(rWs, (unsigned)x1o, 64, 16);
        x2v = ld1(WS_X2 + (size_t)orow * 1024 + ocol * 2, 0); gbv = *(const u32x4*)(modb + 2560 + ocol);
        SCHED_FENCE();
      }
    }
    STAMPK(5, 2);
#pragma unroll
    for (int qt = 0; qt < 4; ++qt)
#pragma unroll
      for (int rg = 0; rg < 4; ++rg)
        *(f32x4*)(lds + ((wid * 4 + qt) * 4 + rg) * 1024 + fo) = (f32x4){acc[0][qt][rg], acc[1][qt][rg], acc[2][qt][rg], acc[3][qt][rg]};
    STAMPK(5, 3);
    __syncthreads();
    STAMPK(5, 4);
    f32x4 s0 = {}, s1 = {};
#pragma unroll
    for (int w = 0; w < 8; ++w) {
      s0 += *(const f32x4*)(lds + ((w * 4 + oqt) * 4 + 2 * orp) * 1024 + fo);
      s1 += *(const f32x4*)(lds + ((w * 4 + oqt) * 4 + 2 * orp + 1) * 1024 + fo);
    }
    const float h2[8] = {s0[0], s0[1], s0[2], s0[3], s1[0], s1[1], s1[2], s1[3]};
    const unsigned x1v[4] = {x1a[0], x1b[0], x1a[1], x1b[1]};
    u32x4 ov;
#pragma unroll
    for (int e = 0; e < 4; ++e)
      ov[e] = pk2(bflo(x2v[e]) + bflo(gbv[e]) * (bflo(x1v[e]) + h2[2 * e]), bfhi(x2v[e]) + bfhi(gbv[e]) * (bfhi(x1v[e]) + h2[2 * e + 1]));
    *(u32x4*)(p.out + (size_t)orow * D_ + ocol) = ov;
    STAMPK(5, 5);
    RT(5, 1);
    STAMPK(5, 6);
  }
  };
  {
    int l2; asm volatile("v_mbcnt_lo_u32_b32 %0, -1, 0\n\tv_mbcnt_hi_u32_b32 %0, -1, %0" : "=v"(l2));
    tail(l2, l2 & 15, l2 >> 4, (unsigned)l2 * 16u, wid * 64 + l2, lds + (l2 & 15) * HT_STRIDE + (l2 >> 4) * 16);
  }
}

extern "C" void kernel_launch(void* const* d_in, const int* in_sizes, int n_in, void* d_out, int out_size, void* d_ws, size_t ws_size, hipStream_t stream) {
  char* ws = (char*)d_ws;
  K0Args p0;
  p0.x = (const u16*)d_in[0]; p0.c = (const u16*)d_in[10]; p0.wmod = (const u16*)d_in[11]; p0.wq = (const u16*)d_in[3]; p0.w1 = (const u16*)d_in[6]; p0.kc = (const u16*)d_in[1]; p0.vc = (const u16*)d_in[2]; p0.ws = ws;
  K1Args p1;
  p1.x = p0.x; p1.kc = (const u16*)d_in[1]; p1.vc = (const u16*)d_in[2]; p1.wo = (const u16*)d_in[4];
  p1.w2 = (const u16*)d_in[7]; p1.w3 = (const u16*)d_in[8]; p1.w4 = (const u16*)d_in[9]; p1.lnst = (const float*)(ws + WS_Y); p1.ws = ws; p1.out = (u16*)d_out;
  (void)hipFuncSetAttribute((const void*)k1_attn, hipFuncAttributeMaxDynamicSharedMemorySize, L1_END);
  k0_prep<<<K0_BLOCKS, 512, 0, stream>>>(p0);
  k1_attn<<<256, 512, L1_END, stream>>>(p1);
}
```

```cpp
#include <hip/hip_runtime.h>
#include <hip/hip_bf16.h>
#include <type_traits>
#include <utility>

typedef unsigned short u16;
typedef __bf16 bf16x8 __attribute__((ext_vector_type(8)));
typedef __bf16 bf16x4 __attribute__((ext_vector_type(4)));
typedef __bf16 bf16x2 __attribute__((ext_vector_type(2)));
typedef float f32x16 __attribute__((ext_vector_type(16)));
typedef float f32x4 __attribute__((ext_vector_type(4)));
typedef float f32x2 __attribute__((ext_vector_type(2)));
typedef unsigned u32x2 __attribute__((ext_vector_type(2)));
typedef unsigned u32x4 __attribute__((ext_vector_type(4)));
typedef __amdgpu_buffer_rsrc_t rsrc_t;
#define DI __device__ __forceinline__
#define AI __attribute__((always_inline))
#define LDSP(T, p) ((__attribute__((address_space(3))) T*)(p))
#define SCHED_FENCE() __builtin_amdgcn_sched_barrier(0)
#define PIN(x) asm volatile("" : "+v"(x))
#define MEM_FENCE() asm volatile("" ::: "memory")
#define WAIT_VM0() __builtin_amdgcn_s_waitcnt(0x0F70)
#define STAMP(i)
#define STAMPK(k, i)
#define RT(k, i)

#define ROUND_S 1
#define ABL 0
#define SM_THR 40
#define SM_SHIFT 100
#define NT_SRC 1
#define K0_WARM 3
constexpr int S_ = 2048, D_ = 512, HQ = 32, HKV = 8, DH = 128, SK = 2048, WIN = 512, F_ = 1792;

constexpr size_t NXB = (size_t)S_ * D_ * 2;
constexpr size_t WS_MOD = 0;
constexpr size_t WS_Y   = 8192;
constexpr size_t WS_X2  = WS_Y  + NXB;
constexpr size_t WS_HB  = WS_X2 + NXB;
constexpr size_t WS_H1  = WS_HB + NXB;
constexpr size_t WS_X1  = WS_H1 + NXB;
constexpr size_t WS_U   = WS_X1 + NXB;
constexpr size_t WS_PART = WS_U + (size_t)S_ * F_ * 2;
constexpr size_t WS_WQT = WS_PART + 8 * NXB;
constexpr size_t WS_WOT = WS_WQT + (size_t)D_ * 4096 * 2;
constexpr size_t WS_W1T = WS_WOT + (size_t)D_ * 4096 * 2;
constexpr size_t WS_W2T = WS_W1T + (size_t)D_ * D_ * 2;
constexpr size_t WS_W3T = WS_W2T + (size_t)D_ * D_ * 2;
constexpr size_t WS_W4T = WS_W3T + (size_t)D_ * F_ * 2;
constexpr size_t WS_ST  = WS_W4T + (size_t)D_ * F_ * 2;
constexpr size_t WS_FLAG = WS_ST + (size_t)S_ * 32;
constexpr int FL_W = 0, FL_P = 8, FL_H1 = 40, FL_X1 = 168, FL_U = 200, FL_P0 = 232, FL_N = 264;
constexpr size_t WS_END = WS_FLAG + FL_N * 128;

DI float bflo(unsigned u) { return __uint_as_float(u << 16); }
DI float bfhi(unsigned u) { return __uint_as_float(u & 0xffff0000u); }
DI unsigned pk2(float lo, float hi) { f32x2 v = {lo, hi}; return __builtin_bit_cast(unsigned, __builtin_convertvector(v, bf16x2)); }
DI float rb(float x) { return __uint_as_float(pk2(0.f, x)); }
DI rsrc_t mk_rsrc(const void* p, unsigned bytes) { return __builtin_amdgcn_make_buffer_rsrc((void*)p, (short)0, (int)bytes, 0x00020000); }
DI unsigned rfl(unsigned x) { return __builtin_amdgcn_readfirstlane(x); }
DI bf16x8 ldfrag(rsrc_t r, unsigned voff, unsigned soff) { return __builtin_bit_cast(bf16x8, __builtin_amdgcn_raw_buffer_load_b128(r, voff, soff, 0)); }
template <int CTRL> DI float dpp_add(float v) {
  return v + __uint_as_float(__builtin_amdgcn_update_dpp(0u, __float_as_uint(v), CTRL, 0xf, 0xf, true));
}
DI float swap16_add(float v) { auto r = __builtin_amdgcn_permlane16_swap(__float_as_uint(v), __float_as_uint(v), false, false); return __uint_as_float(r[0]) + __uint_as_float(r[1]); }
DI float swap32_add(float v) { auto r = __builtin_amdgcn_permlane32_swap(__float_as_uint(v), __float_as_uint(v), false, false); return __uint_as_float(r[0]) + __uint_as_float(r[1]); }
DI float swap32_max(float v) { auto r = __builtin_amdgcn_permlane32_swap(__float_as_uint(v), __float_as_uint(v), false, false); return fmaxf(__uint_as_float(r[0]), __uint_as_float(r[1])); }
DI float wave_sum(float v) {
  v = dpp_add<0xB1>(v); v = dpp_add<0x4E>(v); v = dpp_add<0x141>(v); v = dpp_add<0x140>(v);
  v = swap16_add(v); v = swap32_add(v); return v;
}
DI float div_rn(float a, float d, float r) {
  float q = a * r; float e = __builtin_fmaf(-q, d, a); return __builtin_fmaf(e, r, q);
}
DI float gelu_tanh(float x) {
  const float u = 0.7978845608028654f * (x + 0.044715f * x * x * x);
  return x * __builtin_amdgcn_rcpf(1.f + __builtin_amdgcn_exp2f(-2.885390081777927f * u));
}
DI void ln_consts(float mean, float var, float& m_bf, float& d_bf, float& r) {
  m_bf = rb(mean); d_bf = rb(__builtin_sqrtf(rb(rb(var) + 1e-5f))); r = __builtin_amdgcn_rcpf(d_bf);
}
DI float ln_apply(float x, float m_bf, float d_bf, float r) { return rb(div_rn(rb(x - m_bf), d_bf, r)); }
DI u32x4 ln_row(const u32x4 xv) {
  float f[8];
#pragma unroll
  for (int e = 0; e < 4; ++e) { f[2 * e] = bflo(xv[e]); f[2 * e + 1] = bfhi(xv[e]); }
  float s = 0;
#pragma unroll
  for (int e = 0; e < 8; ++e) s += f[e];
  const float mean = wave_sum(s) * (1.f / 512.f);
  float q = 0;
#pragma unroll
  for (int e = 0; e < 8; ++e) { float d = f[e] - mean; q += d * d; }
  const float var = wave_sum(q) * (1.f / 512.f);
  float mb, db, r; ln_consts(mean, var, mb, db, r);
  u32x4 o;
#pragma unroll
  for (int e = 0; e < 4; ++e) o[e] = pk2(div_rn(rb(f[2 * e] - mb), db, r), div_rn(rb(f[2 * e + 1] - mb), db, r));
  return o;
}

#define MFMA16(a, b, c) __builtin_amdgcn_mfma_f32_16x16x32_bf16((a), (b), (c), 0, 0, 0)
#define MFMA32(a, b, c) __builtin_amdgcn_mfma_f32_32x32x16_bf16((a), (b), (c), 0, 0, 0)

DI void dma16(unsigned lds_dst, unsigned voff, rsrc_t r, unsigned soff) {
  asm volatile("s_mov_b32 m0, %0\n\ts_nop 0\n\tbuffer_load_dwordx4 %1, %2, %3 offen lds" ::"s"(lds_dst), "v"(voff), "s"(r), "s"(soff) : "memory");
}

struct Retile { u32x2 d[8]; };
DI void retile_load(Retile& t, const u16* __restrict__ W, int ldw, int k0, bool perm, int n0, int lane) {
  const int a = lane & 15, g4 = lane >> 4;
  const u16* p = W + (size_t)(k0 + (perm ? 16 : 8) * g4) * ldw + n0 + 4 * a;
  const int s2 = (perm ? 4 : 2) * ldw;
#pragma unroll
  for (int j = 0; j < 8; ++j) {
    const u32x2* q = (const u32x2*)(p + (j >> 1) * s2 + (j & 1) * ldw);
    t.d[j] = NT_SRC ? __builtin_nontemporal_load(q) : *q;
  }
}
DI u32x4 retile_frag(const Retile& t, int i) {
  u32x4 f;
#pragma unroll
  for (int p = 0; p < 4; ++p) f[p] = __builtin_amdgcn_perm(t.d[2 * p + 1][i >> 1], t.d[2 * p][i >> 1], (i & 1) ? 0x07060302u : 0x05040100u);
  return f;
}

struct K0Args { const u16 *x, *c, *wmod, *wq, *w1, *kc, *vc; char* ws; };
constexpr int K0_BLOCKS = 352 + 128 + 16;
__global__ __launch_bounds__(512) void k0_prep(const K0Args p) {
  __shared__ float red[8][64];
  const int tid = threadIdx.x, lane = tid & 63, wid = tid >> 6;
  int b = blockIdx.x;
#define WSP p.ws
  RT(0, 0);
  if (b < 96) {
    const int n0 = b * 32, cg = tid & 3, kq = tid >> 2;
    u32x4 wv[4]; float sv[4];
#pragma unroll
    for (int i = 0; i < 4; ++i) wv[i] = *(const u32x4*)(p.wmod + (size_t)(kq + 128 * i) * 3072 + n0 + cg * 8);
#pragma unroll
    for (int i = 0; i < 4; ++i) { float cv = bflo(p.c[kq + 128 * i]); sv[i] = rb(cv / (1.f + __expf(-cv))); }
    float acc[8] = {};
#pragma unroll
    for (int i = 0; i < 4; ++i)
#pragma unroll
      for (int e = 0; e < 4; ++e) { acc[2 * e] += sv[i] * bflo(wv[i][e]); acc[2 * e + 1] += sv[i] * bfhi(wv[i][e]); }
#pragma unroll
    for (int e = 0; e < 8; ++e) { float v = acc[e]; v += __shfl_xor(v, 4); v += __shfl_xor(v, 8); v = swap16_add(v); v = swap32_add(v); acc[e] = v; }
    if (lane < 4)
#pragma unroll
      for (int e = 0; e < 8; ++e) red[wid][lane * 8 + e] = acc[e];
    __syncthreads();
    if (tid < 32) {
      float s = 0;
#pragma unroll
      for (int w = 0; w < 8; ++w) s += red[w][tid];
      s = rb(s);
      const int n = n0 + tid, seg = n >> 9;
      if (seg == 1 || seg == 4) s = rb(1.f + s);
      ((u16*)(p.ws + WS_MOD))[n] = (u16)(__float_as_uint(s) >> 16);
    }
    RT(0, 1);
    return;
  }
  b -= 96;
  if (b < 256) {
    const int row = b * 8 + wid;
    const u32x4 xv = *(const u32x4*)(p.x + (size_t)row * D_ + lane * 8);
    u32x4 wv[8];
#pragma unroll
    for (int i = 0; i < 8; ++i) wv[i] = *(const u32x4*)(p.x + (size_t)((b >> 3) * 64 + wid * 8 + i) * D_ + lane * 8);
    float f[8];
#pragma unroll
    for (int e = 0; e < 4; ++e) { f[2 * e] = bflo(xv[e]); f[2 * e + 1] = bfhi(xv[e]); }
    float s = 0;
#pragma unroll
    for (int e = 0; e < 8; ++e) s += f[e];
    const float mean = wave_sum(s) * (1.f / 512.f);
    float q = 0;
#pragma unroll
    for (int e = 0; e < 8; ++e) { float d = f[e] - mean; q += d * d; }
    const float var = wave_sum(q) * (1.f / 512.f);
    float mb, db, r; ln_consts(mean, var, mb, db, r);
    if (lane == 0) *(f32x2*)(p.ws + WS_Y + (size_t)row * 8) = (f32x2){mb, db};
#pragma unroll
    for (int i = 0; i < 8; ++i) asm volatile("" ::"v"(wv[i]));
    RT(0, 1);
    return;
  }
  b -= 256;
  Retile t; char* dst;
  if (b < 128) { const int g = b & 7, s = b >> 3, ng = g * 8 + wid; retile_load(t, p.wq, 4096, 32 * s, false, 64 * ng, lane); dst = p.ws + WS_WQT + (size_t)(ng * 16 + s) * 4096; }
  else {
    const int s = b - 128; retile_load(t, p.w1, 512, 32 * s, false, 64 * wid, lane); dst = p.ws + WS_W1T + (size_t)(wid * 16 + s) * 4096;
    if (s == 0 && tid < FL_N) *(unsigned*)(p.ws + WS_FLAG + tid * 128) = 0u;
  }
#pragma unroll
  for (int i = 0; i < 4; ++i) *(u32x4*)(dst + i * 1024 + lane * 16) = retile_frag(t, i);
  RT(0, 1);
#undef WSP
#define WSP ws
}

template <int V> using IC = std::integral_constant<int, V>;
template <int N, class F, int... Is> DI void sfor_(F&& f, std::integer_sequence<int, Is...>) { (f(IC<Is>{}), ...); }
template <int N, class F> DI void sfor(F&& f) { sfor_<N>(f, std::make_integer_sequence<int, N>{}); }
template <int NI, int NQT, int NSTEP, int DEPTH, int NSET, int BD = 1, class ALoad, class BRead, class Hook>
DI void gemm_steps(f32x4 (&acc)[NSET][NI][NQT], bf16x8 (&ring)[DEPTH][NI], ALoad aload, BRead bread, Hook hook) {
  constexpr int NM = NI * NQT, PER = NSTEP / NSET;
  bf16x8 B[BD + 1][NQT];
#pragma unroll
  for (int s = 0; s < BD; ++s)
#pragma unroll
    for (int qt = 0; qt < NQT; ++qt) B[s][qt] = bread(s, qt);
  SCHED_FENCE();
  sfor<NSTEP * NM>([&](auto X) AI {
    constexpr int s = X.value / NM, m = X.value % NM, i = m / NQT, qt = m % NQT;
    if constexpr (s % PER == 0) acc[s / PER][i][qt] = MFMA16(ring[s % DEPTH][i], B[s % (BD + 1)][qt], (f32x4){});
    else                        acc[s / PER][i][qt] = MFMA16(ring[s % DEPTH][i], B[s % (BD + 1)][qt], acc[s / PER][i][qt]);
    if constexpr (m < NQT && s + BD < NSTEP) B[(s + BD) % (BD + 1)][m] = bread(s + BD, m);
    if constexpr (qt == NQT - 1 && s + DEPTH < NSTEP) ring[s % DEPTH][i] = aload(s + DEPTH, i);
    hook(IC<s>{}, IC<m>{});
    SCHED_FENCE();
  });
}

constexpr int HT_STRIDE = 1056, QX_STRIDE = 272;
constexpr int L1_HT = 0, L1_K = 69632, L1_V = L1_K + 3 * 16384, L1_END = L1_V + 2 * 16384;
#define PRIO_G3 21
#define G4_AHEAD 2
#define ROT 1
#define P0_STEP 28
#define PRIO_P 1
#define PRIO_Q 11
#define PRIO_O 21
#define PRIO_A 0
#define GBAR 1
#define WO_SC1 1
#define OPASS 2
#define PF_ON 1
#define RT_AUX 16
#define PART_AUX 16
constexpr int SL_[9] = {L1_K, L1_K + 16384, L1_K + 32768, L1_K + 49152, L1_K + 65536, 0, 16384, 32768, 49152};
constexpr int kslot(int t) { constexpr int k[8] = {0, 1, 2, 0, 3, 6, 8, 4}; return SL_[k[t]]; }
constexpr int vslot(int t) { constexpr int v[8] = {3, 4, 1, 5, 7, 2, 0, 1}; return SL_[v[t]]; }
constexpr float SM_C = 0.12751743082459868f;
constexpr float SM_LIMIT = __builtin_bit_cast(float, (unsigned)(127 + 40 - SM_SHIFT)  __H0__ );

DI float sm_arg(float s, float negmc) {
  return __builtin_fmaf(rb(s), SM_C, negmc);
}
DI float max16(const f32x16& S) {
  float m = fmaxf(S[0], S[1]);
#pragma unroll
  for (int r = 2; r < 16; ++r) m = fmaxf(m, S[r]);
  return m;
}
DI unsigned scale_pk(unsigned p, float f) { return pk2(bflo(p) * f, bfhi(p) * f); }
DI void sm_half(const f32x16& S, float negmc, float& l0, float& l1, u32x4& pa, u32x4& pb) {
#pragma unroll
  for (int p = 0; p < 4; ++p) {
    const float e0 = __builtin_amdgcn_exp2f(sm_arg(S[2 * p], negmc)), e1 = __builtin_amdgcn_exp2f(sm_arg(S[2 * p + 1], negmc));
    l0 += e0; l1 += e1; pa[p] = pk2(e0, e1);
  }
#pragma unroll
  for (int p = 0; p < 4; ++p) {
    const float e0 = __builtin_amdgcn_exp2f(sm_arg(S[8 + 2 * p], negmc)), e1 = __builtin_amdgcn_exp2f(sm_arg(S[8 + 2 * p + 1], negmc));
    l0 += e0; l1 += e1; pb[p] = pk2(e0, e1);
  }
}
struct SmPipe { float r[16], x[16], e[16]; };
DI void sm_gap(const int m, SmPipe& sp, const f32x16& S, float negmc, float& l0, float& l1, u32x4& pa, u32x4& pb) {
  constexpr int D = ROUND_S ? 1 : 0, SPAN = 13 - D;
#pragma unroll
  for (int e = 0; e < 16; ++e) {
    const int f = e * SPAN / 16 + D;
    if (f + 2 == m) {
      if (e & 1) {
        l1 += sp.e[e]; PIN(l1);
        unsigned w = pk2(sp.e[e - 1], sp.e[e]); PIN(w);
        if (e < 8) pa[e >> 1] = w; else pb[(e - 8) >> 1] = w;
      } else { l0 += sp.e[e]; PIN(l0); }
    }
  }
#pragma unroll
  for (int e = 0; e < 16; ++e) if (e * SPAN / 16 + D + 1 == m) sp.e[e] = __builtin_amdgcn_exp2f(sp.x[e]);
#pragma unroll
  for (int e = 0; e < 16; ++e) if (e * SPAN / 16 + D == m) {
    if (ROUND_S == 0) sp.x[e] = __builtin_fmaf(S[e], SM_C, negmc);
    else if (ROUND_S == 1) sp.x[e] = __builtin_fmaf(sp.r[e], SM_C, negmc);
    else sp.x[e] = __builtin_fmaf(rb(sp.r[e]), 1.4426950408889634f, negmc);
  }
  if (ROUND_S) {
#pragma unroll
    for (int e = 0; e < 16; ++e) if (e * SPAN / 16 == m) sp.r[e] = (ROUND_S == 1) ? rb(S[e]) : rb(S[e]) * 0.08838834764831845f;
  }
}
DI void pack_acc16(const f32x4 (&acc)[4][4], int qt, u32x4& w0, u32x4& w1) {
#pragma unroll
  for (int rg = 0; rg < 2; ++rg) {
    w0[2 * rg] = pk2(acc[0][qt][rg], acc[1][qt][rg]); w0[2 * rg + 1] = pk2(acc[2][qt][rg], acc[3][qt][rg]);
    w1[2 * rg] = pk2(acc[0][qt][2 + rg], acc[1][qt][2 + rg]); w1[2 * rg + 1] = pk2(acc[2][qt][2 + rg], acc[3][qt][2 + rg]);
  }
}

typedef __attribute__((address_space(1))) unsigned gu32;
typedef __attribute__((address_space(1))) unsigned long long gu64;
constexpr unsigned SPIN_CAP = 1u << 20;
struct K1Args { const u16 *x, *kc, *vc, *wo, *w2, *w3, *w4; const float* lnst; char* ws; u16* out; };

__global__ __launch_bounds__(512, 2) __attribute__((amdgpu_waves_per_eu(2, 2)))
void k1_attn(const K1Args p) {
  extern __shared__ __attribute__((aligned(16))) char lds[];
  char* __restrict__ ws = p.ws;
  const int tid = threadIdx.x, lane = tid & 63;
  const int wid = (int)rfl((unsigned)tid >> 6);
  const int g = blockIdx.x & 7, rbk = blockIdx.x >> 3;
  const int a = lane & 15, g4 = lane >> 4, r32 = lane & 31, hh = lane >> 5;
  const unsigned ldsb = (unsigned)(size_t)lds;
  const unsigned fo = lane * 16;
  RT(1, 0);
  STAMP(0);
  const rsrc_t rK = mk_rsrc(p.kc + ((size_t)g * SK + (SK - WIN)) * DH, WIN * DH * 2), rV = mk_rsrc(p.vc + ((size_t)g * SK + (SK - WIN)) * DH, WIN * DH * 2);
  const unsigned drow = wid * 8 + ((lane >> 2) & 7);
  const unsigned kvoff = drow * 256 + (lane >> 5) * 64 + (((lane & 3) ^ ((drow >> 2) & 3)) * 16);
  const unsigned vvoff = drow * 256 + (lane >> 5) * 64 + (lane & 3) * 16;
  const unsigned kdst = ldsb + wid * 2048;
  auto dmaK1 = [&](int t, int j) AI { dma16(kdst + kslot(t) + j * 1024, kvoff, rK, t * 16384 + j * 128); };
  auto dmaV1 = [&](int t, int j) AI { dma16(kdst + vslot(t) + j * 1024, vvoff, rV, t * 16384 + j * 128); };
  auto dmaK = [&](int t) AI { dmaK1(t, 0); dmaK1(t, 1); };
  auto dmaV = [&](int t) AI { dmaV1(t, 0); dmaV1(t, 1); };
  constexpr int DEPTH = 6;
  bf16x8 ring[DEPTH][4];
  const rsrc_t rWq = mk_rsrc(ws + WS_WQT + (size_t)(g * 8 + wid) * 65536, 65536);
  const int rot = ROT ? (rbk & 15) : 0;
  auto wq_load = [&](int s, int i) AI { return ldfrag(rWq, fo + i * 1024, ((s + rot) & 15) * 4096); };
  {
    const u32x4 sav = *(const u32x4*)(ws + WS_MOD + fo), cav = *(const u32x4*)(ws + WS_MOD + 1024 + fo);
    u32x4 xv[8];
#pragma unroll
    for (int i = 0; i < 8; ++i) xv[i] = *(const u32x4*)((const char*)p.x + (size_t)(rbk * 64 + wid * 8 + i) * 1024 + fo);
    float st[16];
#pragma unroll
    for (int i = 0; i < 16; ++i) st[i] = p.lnst[(rbk * 64 + wid * 8) * 2 + i];
    SCHED_FENCE();
    if (K0_WARM == 3) dma16(kdst + L1_K, fo, wid < 4 ? rK : rV, (rbk * 4 + (wid & 3)) * 1024);
    SCHED_FENCE();
    float ca[8], sa[8];
#pragma unroll
    for (int e = 0; e < 4; ++e) { ca[2 * e] = bflo(cav[e]); ca[2 * e + 1] = bfhi(cav[e]); sa[2 * e] = bflo(sav[e]); sa[2 * e + 1] = bfhi(sav[e]); }
    if (PRIO_P && wid >= 4) __builtin_amdgcn_s_setprio(1);
#pragma unroll
    for (int hf = 0; hf < 2; ++hf) {
      float lo[16], hi[16], rr[4]; unsigned t[16];
#define H_STAGE(body) _Pragma("unroll") for (int j = 0; j < 16; ++j) { const int i = hf * 4 + (j >> 2), d = j & 3; (void)i; (void)d; body; } SCHED_FENCE()
#pragma unroll
      for (int i = 0; i < 4; ++i) rr[i] = __builtin_amdgcn_rcpf(st[2 * (hf * 4 + i) + 1]);
      H_STAGE(lo[j] = bflo(xv[i][d]); hi[j] = bfhi(xv[i][d]));
      H_STAGE(lo[j] -= st[2 * i]; hi[j] -= st[2 * i]);
      H_STAGE(lo[j] = rb(lo[j]); hi[j] = rb(hi[j]));
      if (PRIO_P && hf == 1) __builtin_amdgcn_s_setprio(0);
      H_STAGE(lo[j] *= rr[j >> 2]; hi[j] *= rr[j >> 2]);
      H_STAGE(lo[j] = rb(lo[j]); hi[j] = rb(hi[j]));
#pragma unroll
      for (int i = 0; i < 4; ++i) ring[hf][i] = wq_load(hf, i);
      H_STAGE(lo[j] *= ca[2 * d]; hi[j] *= ca[2 * d + 1]);
      H_STAGE(lo[j] = rb(lo[j]); hi[j] = rb(hi[j]));
      H_STAGE(lo[j] += sa[2 * d]; hi[j] += sa[2 * d + 1]);
      H_STAGE(t[j] = pk2(lo[j], hi[j]));
#pragma unroll
      for (int i = 0; i < 4; ++i) *(u32x4*)(lds + L1_HT + (wid * 8 + hf * 4 + i) * HT_STRIDE + fo) = (u32x4){t[4 * i], t[4 * i + 1], t[4 * i + 2], t[4 * i + 3]};
#pragma unroll
      for (int i = 0; i < 2; ++i) ring[2][hf * 2 + i] = wq_load(2, hf * 2 + i);
      SCHED_FENCE();
    }
  }

  Retile rtl; const u16* rsrc_w; int rld, rk0, rn0; bool rperm; size_t rdst; unsigned rhalf = 2048;
  if (wid < 4) { const int id = rbk * 4 + wid, s = id >> 3, ng = id & 7; rsrc_w = p.wo; rld = 512; rk0 = g * 512 + 32 * s; rn0 = 64 * ng; rperm = false; rdst = WS_WOT + (size_t)(g * 8 + ng) * 65536 + s * 2048; rhalf = 32768; }
  else {
    int id = blockIdx.x * 4 + wid - 4, s, ng; rperm = true;
    if (id < 128)      { ng = id >> 4; s = id & 15; rsrc_w = p.w2; rld = 512; rdst = WS_W2T + (size_t)id * 4096; }
    else if (id < 576) { id -= 128; ng = id >> 4; s = id & 15; rsrc_w = p.w3; rld = F_; rdst = WS_W3T + (size_t)id * 4096; }
    else               { id -= 576; ng = id & 7; s = id >> 3; rsrc_w = p.w4; rld = 512; rdst = WS_W4T + (size_t)(ng * 56 + s) * 4096; }
    rk0 = 64 * (s >> 1) + 2 * (s & 1); rn0 = 64 * ng;
  }
  gu32* flags = (gu32*)(ws + WS_FLAG);
  gu32* flag = flags + (FL_W + g) * 32;
  gu32* flagw = flags + (FL_W + (lane & 7)) * 32;
  STAMP(1);
  __syncthreads();
  STAMP(2);
  if (wid >= 4 && PRIO_Q > 0) __builtin_amdgcn_s_setprio(1);

  const char* bl = lds + L1_HT + a * HT_STRIDE + g4 * 16;
  auto bread = [&](int s, int qt) AI { return *(const bf16x8*)(bl + 64 * ((s + rot) & 15) + qt * 16 * HT_STRIDE); };
  f32x16 o[4]; bf16x8 qf[8]; float l0, l1, negmc;
  {
    f32x4 acc[1][4][4];
    gemm_steps<4, 4, 16, DEPTH, 1>(acc, ring, wq_load, bread, [&](auto S_, auto M_) AI {
      constexpr int s = S_.value, m = M_.value;
      if constexpr (s < 3 && (m & 3) == 1) ring[3 + s][m >> 2] = wq_load(3 + s, m >> 2);
      if constexpr (s == PRIO_Q && m == 0) __builtin_amdgcn_s_setprio(0);
      if constexpr (s == 11 && m == 8) { WAIT_VM0(); dmaK(0); }
      if constexpr (s == 12 && m == 0) dmaK(1);
      if constexpr (s == 12 && m == 8) dmaV(0);
      if constexpr (s == 13 && m == 0) dmaK(2);
      if constexpr (s == 13 && m == 8) dmaV(1);
    });
    STAMP(3);
    __syncthreads();
#pragma unroll
    for (int qt = 0; qt < 4; ++qt) {
      u32x4 w0, w1; pack_acc16(acc[0], qt, w0, w1);
      char* dst = lds + L1_HT + ((wid >> 1) * 64 + qt * 16 + a) * QX_STRIDE + ((wid & 1) * 64 + g4 * 16) * 2;
      *(u32x4*)dst = w0; *(u32x4*)(dst + 16) = w1;
    }
    asm volatile("s_waitcnt vmcnt(0)" ::: "memory");
    __syncthreads();
#pragma unroll
    for (int s = 0; s < 8; ++s) qf[s] = *(const bf16x8*)(lds + L1_HT + ((wid >> 1) * 64 + (wid & 1) * 32 + r32) * QX_STRIDE + 32 * s + 16 * hh);
  }
  STAMP(4);

  const unsigned kx = (r32 >> 2) & 3;
  const unsigned kb0 = ldsb + 2048 * (r32 >> 3) + 64 * (r32 & 7) + 16 * ((0 + hh) ^ kx);
  const unsigned vb0 = ldsb + (4 * hh + ((lane >> 2) & 3)) * 64 + ((lane >> 4) & 1) * 32 + (lane & 3) * 8;
  unsigned ka0, ka1, va;
  auto kbase = [&](int t) AI { ka0 = kb0 + kslot(t); ka1 = (kb0 ^ 32u) + kslot(t); PIN(ka0); PIN(ka1); };
  auto vbase = [&](int t) AI { va = vb0 + vslot(t); PIN(va); };
  auto kfrag = [&](int m) AI { return *LDSP(const bf16x8, (size_t)(((m & 1) ? ka1 : ka0) + 512 * ((m & 7) >> 1) + 8192 * (m >> 3))); };
  auto vfrag = [&](int m) AI {
    const unsigned q = va + (m >> 2) * 4096 + (m & 3) * 512;
    bf16x4 lo = __builtin_amdgcn_ds_read_tr16_b64_v4bf16(LDSP(bf16x4, (size_t)q)), hi = __builtin_amdgcn_ds_read_tr16_b64_v4bf16(LDSP(bf16x4, (size_t)(q + 2048)));
    return __builtin_shufflevector(lo, hi, 0, 1, 2, 3, 4, 5, 6, 7);
  };
  auto fixup = [&](const f32x16& S, float l0p, float l1p, u32x4& pa, u32x4& pb, bool pend, u32x4& pend0, u32x4& pend1) AI {
    const float mx = swap32_max(sm_arg(max16(S), negmc));
    const float dm = fmaxf(__builtin_ceilf(mx) + (float)SM_SHIFT, 0.f);
    const float f = __builtin_amdgcn_exp2f(-dm);
    negmc -= dm; l0 = l0p * f; l1 = l1p * f;
#pragma unroll
    for (int db = 0; db < 4; ++db) o[db] *= f;
    if (pend) {
#pragma unroll
      for (int q = 0; q < 4; ++q) { pend0[q] = scale_pk(pend0[q], f); pend1[q] = scale_pk(pend1[q], f); }
    }
    sm_half(S, negmc, l0, l1, pa, pb);
  };

  f32x16 Sa0, Sa1, Sb0, Sb1;
  u32x4 Pa0, Pa1, Pb0, Pb1, P2, P3;
  bf16x8 kf[4], vf[4];
  kbase(0);
  {
#pragma unroll
    for (int m = 0; m < 16; ++m) {
      const bf16x8 k = kfrag(m);
      if (m < 8) Sa0 = MFMA32(k, qf[m & 7], m == 0 ? (f32x16){} : Sa0); else Sa1 = MFMA32(k, qf[m & 7], m == 8 ? (f32x16){} : Sa1);
      if (m == 9) { SCHED_FENCE(); retile_load(rtl, rsrc_w, rld, rk0, rperm, rn0, lane); MEM_FENCE(); SCHED_FENCE(); }
    }
  }
  kbase(1);
#pragma unroll
  for (int m = 0; m < 3; ++m) kf[m] = kfrag(m);
  negmc = -sm_arg(swap32_max(fmaxf(max16(Sa0), max16(Sa1))), (float)SM_SHIFT);
  l0 = 0.f; l1 = 0.f;
  sm_half(Sa0, negmc, l0, l1, Pa0, Pa1);
#pragma unroll
  for (int db = 0; db < 4; ++db) o[db] = (f32x16){};
  SCHED_FENCE();

  const rsrc_t rWo = mk_rsrc(ws + WS_WOT + (size_t)(g * 8 + wid) * 65536, 65536);
  const rsrc_t rRt = mk_rsrc(ws + rdst, 36864);
  bf16x8 ring2[12][2];
  auto wo_load = [&](int sg, int i) AI { return __builtin_bit_cast(bf16x8, __builtin_amdgcn_raw_buffer_load_b128(rWo, fo + i * 1024, (sg >> 4) * 32768 + ((sg + rot) & 15) * 2048, 16)); };
  unsigned flagv = 0;

  auto tile = [&](auto T_, const f32x16& C1, f32x16& N0, f32x16& N1, u32x4& Pc0, u32x4& Pc1, u32x4& Pn0, u32x4& Pn1) AI {
    constexpr int T = T_.value;
    if (PRIO_A && wid >= 4) __builtin_amdgcn_s_setprio(1);
    {
      const float l0p = l0, l1p = l1; SmPipe sp;
#pragma unroll
      for (int m = 0; m < 16; ++m) {
        if (T < 7) {
          if (m < 8) N0 = MFMA32(kf[m & 3], qf[m & 7], m == 0 ? (f32x16){} : N0); else N1 = MFMA32(kf[m & 3], qf[m & 7], m == 8 ? (f32x16){} : N1);
          if (m + 3 < 16) kf[(m + 3) & 3] = kfrag(m + 3);
        }
        if (m == 12) vbase(T);
        if (m >= 13) vf[m - 13] = vfrag(m - 13);
        if (!(ABL & 2)) sm_gap(m, sp, C1, negmc, l0, l1, P2, P3); else if (m == 0) { P2 = Pc0; P3 = Pc1; asm volatile("" :: "v"(C1)); }
        if (T == 1 && m < 15 && m % 3 != 2) { const int k = m / 3, j = m % 3; if (k == 0) dmaK1(4, j); if (k == 1) dmaV1(3, j); if (k == 2) dmaK1(5, j); if (k == 3) dmaV1(4, j); if (k == 4) dmaK1(6, j); }
        if (T == 7) ring2[m >> 1][m & 1] = wo_load(m >> 1, m & 1);
        if (T < 7) SCHED_FENCE();
      }
      MEM_FENCE();
      if (__builtin_expect(__any(!(l0 + l1 <= SM_LIMIT)), 0)) fixup(C1, l0p, l1p, P2, P3, true, Pc0, Pc1);
    }
    if (PRIO_A == 1) __builtin_amdgcn_s_setprio(0);
    {
      const float l0p = l0, l1p = l1; SmPipe sp;
#pragma unroll
      for (int m = 0; m < 16; ++m) {
        const u32x4 pp = (m < 4) ? Pc0 : (m < 8) ? Pc1 : (m < 12) ? P2 : P3;
        o[m & 3] = MFMA32(vf[m & 3], __builtin_bit_cast(bf16x8, pp), o[m & 3]);
        if (m + 3 < 16) vf[(m + 3) & 3] = vfrag(m + 3);
        if (T < 7 && !(ABL & 2)) sm_gap(m, sp, N0, negmc, l0, l1, Pn0, Pn1); else if (T < 7 && m == 0) { Pn0 = Pc0; Pn1 = Pc1; asm volatile("" :: "v"(N0)); }
        if (T == 7 && m < 8) ring2[8 + (m >> 1)][m & 1] = wo_load(8 + (m >> 1), m & 1);
        if (PRIO_A == 2 && m == 4) __builtin_amdgcn_s_setprio(0);
        if ((T == 0 || T == 1 || T == 3 || T == 5) && m == 13) {
          if (T == 5 && wid == 0) {
            for (unsigned spins = 0; !__all(flagv == 32u) && spins < SPIN_CAP; ++spins) { __builtin_amdgcn_s_sleep(2); flagv = __hip_atomic_load(flagw, __ATOMIC_RELAXED, __HIP_MEMORY_SCOPE_AGENT); }
          }
          if (T == 1) WAIT_VM0();
          if (T == 0) asm volatile("s_waitcnt lgkmcnt(0)" : "+v"(vf[2]), "+v"(vf[3])::"memory");
          else        asm volatile("s_waitcnt vmcnt(0) lgkmcnt(0)" : "+v"(vf[2]), "+v"(vf[3])::"memory");
          __builtin_amdgcn_s_barrier();
          MEM_FENCE();
          if (T == 3 && tid == 0) __hip_atomic_fetch_add(flag, 1u, __ATOMIC_RELAXED, __HIP_MEMORY_SCOPE_AGENT);
        }
        if (T == 4 && m == 13 && wid == 0) flagv = __hip_atomic_load(flagw, __ATOMIC_RELAXED, __HIP_MEMORY_SCOPE_AGENT);
        if (T < 6 && m == 12) kbase(T + 2);
        if (T < 6 && m == 13) { kf[0] = kfrag(0); kf[1] = kfrag(1); }
        if (T < 6 && m == 14) kf[2] = kfrag(2);
        if (T == 0 && m == 14) dmaK(3);
        if (T == 0 && m == 15) dmaV(2);
        if (T == 1 && m == 14) dmaV(5);
        if (T == 1 && m == 15) dmaK(7);
        if (T == 3 && m == 14) dmaV(6);
        if (T == 3 && m == 15) dmaV(7);
        if (T == 1 && m == 15) {
#pragma unroll
          for (int i = 0; i < 4; ++i) __builtin_amdgcn_raw_buffer_store_b128(retile_frag(rtl, i), rRt, fo + (i >> 1) * rhalf + (i & 1) * 1024, 0, 16);
        }
        SCHED_FENCE();
      }
      MEM_FENCE();
      if (T < 7) {
        if (__builtin_expect(__any(!(l0 + l1 <= SM_LIMIT)), 0)) fixup(N0, l0p, l1p, Pn0, Pn1, false, Pc0, Pc1);
      }
    }
  };
  STAMP(5);
  tile(IC<0>{}, Sa1, Sb0, Sb1, Pa0, Pa1, Pb0, Pb1);
  tile(IC<1>{}, Sb1, Sa0, Sa1, Pb0, Pb1, Pa0, Pa1);
  tile(IC<2>{}, Sa1, Sb0, Sb1, Pa0, Pa1, Pb0, Pb1);
  tile(IC<3>{}, Sb1, Sa0, Sa1, Pb0, Pb1, Pa0, Pa1);
  tile(IC<4>{}, Sa1, Sb0, Sb1, Pa0, Pa1, Pb0, Pb1);
  tile(IC<5>{}, Sb1, Sa0, Sa1, Pb0, Pb1, Pa0, Pa1);
  STAMP(6);
  tile(IC<6>{}, Sa1, Sb0, Sb1, Pa0, Pa1, Pb0, Pb1);
  tile(IC<7>{}, Sb1, Sa0, Sa1, Pb0, Pb1, Pa0, Pa1);
  STAMP(7);

  {
    const float inv = 1.f / swap32_add(l0 + l1);
    char* dst = lds + L1_HT + ((wid & 1) * 32 + r32) * HT_STRIDE + ((wid >> 1) * 128 + 4 * hh) * 2;
#pragma unroll
    for (int db = 0; db < 4; ++db)
#pragma unroll
      for (int gq = 0; gq < 4; ++gq) {
        u32x2 w = {pk2(o[db][4 * gq] * inv, o[db][4 * gq + 1] * inv), pk2(o[db][4 * gq + 2] * inv, o[db][4 * gq + 3] * inv)};
        *(u32x2*)(dst + (32 * db + 8 * gq) * 2) = w;
      }
  }
  __syncthreads();
  STAMP(8);
  if (wid >= 4 && PRIO_O > 0) __builtin_amdgcn_s_setprio(1);
  {
    f32x4 acc[2][2][4];
    const rsrc_t rP = mk_rsrc(ws + WS_PART + (size_t)((rbk * 8 + g) * 8 + wid) * 8192, 8192);
    auto pstore = [&](int ps, int qt) AI {
      u32x4 w;
#pragma unroll
      for (int r = 0; r < 4; ++r) w[r] = pk2(acc[ps][0][qt][r], acc[ps][1][qt][r]);
      __builtin_amdgcn_raw_buffer_store_b128(w, rP, fo + (qt * 2 + ps) * 1024, 0, PART_AUX);
    };
    gemm_steps<2, 4, 32, 12, 2, 2>(acc, ring2, wo_load, bread, [&](auto S_, auto M_) AI {
      if constexpr (S_.value == PRIO_O && M_.value == 0) __builtin_amdgcn_s_setprio(0);
      if constexpr (S_.value >= 20 && S_.value < 22 && (M_.value & 3) == 1) pstore(0, (S_.value - 20) * 2 + (M_.value >> 2));
      if constexpr (S_.value == P0_STEP && M_.value == 7) {
        WAIT_VM0(); asm volatile("s_waitcnt vmcnt(0)" ::: "memory"); __builtin_amdgcn_s_barrier(); MEM_FENCE();
        if (tid == 0) __hip_atomic_fetch_add(flags + (FL_P0 + rbk) * 32, 1u, __ATOMIC_RELAXED, __HIP_MEMORY_SCOPE_AGENT);
      } });
    STAMP(9);
#pragma unroll
    for (int qt = 0; qt < 4; ++qt) pstore(1, qt);
  }
  STAMP(10);

  auto tail = [&](const int lane, const int a, const int g4, const unsigned fo, const int tid, const char* bl) AI {
  const rsrc_t rWs = mk_rsrc(ws, (unsigned)WS_END);
  auto ld1 = [&](size_t off, unsigned soff) AI { return __builtin_amdgcn_raw_buffer_load_b128(rWs, (unsigned)off, soff, 16); };
  auto ldf1 = [&](size_t off, unsigned soff) AI { return __builtin_bit_cast(bf16x8, __builtin_amdgcn_raw_buffer_load_b128(rWs, (unsigned)off, soff, 16)); };
  auto st1 = [&](size_t off, u32x4 v) AI { __builtin_amdgcn_raw_buffer_store_b128(v, rWs, (unsigned)off, 0, 16); };
  auto publish = [&](int idx) AI {
    asm volatile("s_waitcnt vmcnt(0) lgkmcnt(0)" ::: "memory"); __builtin_amdgcn_s_barrier(); MEM_FENCE();
    if (tid == 0) __hip_atomic_fetch_add(flags + idx * 32, 1u, __ATOMIC_RELAXED, __HIP_MEMORY_SCOPE_AGENT);
  };
  auto await = [&](int pw, int idx, unsigned want) AI {
    if (wid == pw) {
      unsigned spins = 0;
      while (!__all(__hip_atomic_load(flags + idx * 32, __ATOMIC_RELAXED, __HIP_MEMORY_SCOPE_AGENT) >= want) && ++spins < SPIN_CAP) __builtin_amdgcn_s_sleep(1);
    }
    MEM_FENCE(); __builtin_amdgcn_s_barrier(); MEM_FENCE();
  };
  const int b = blockIdx.x;
  const u16* modb = (const u16*)(ws + WS_MOD);
  publish(FL_P + rbk);
  STAMP(11);
  RT(1, 1);

  RT(2, 0);
  STAMPK(2, 0);
  {
    f32x2* red = (f32x2*)(lds + 16 * HT_STRIDE);
    const int rt = (b & 7) * 16 + (b >> 4), nh = (b >> 3) & 1;
    const int prb = rt >> 2, qt = rt & 3, row = rt * 16 + a, col = wid * 64 + g4 * 16;
    constexpr int D1 = 12, D1E = 12;
    bf16x8 A[D1][2];
    const int G = nh * 4 + (wid >> 1), ps = wid & 1;
    const rsrc_t rW = mk_rsrc(ws + WS_W1T + (size_t)G * 65536 + ps * 2048, 65536);
    auto aload = [&](int s, int i) AI { return ldfrag(rW, fo, (s * 4 + i) * 1024); };
    u32x4 pv[8][2];
    const size_t poff = WS_PART + (size_t)((prb * 64 + wid) * 8 + qt * 2) * 1024 + fo;
    const int prot = 0;
    await(0, FL_P0 + prb, 8);
    if (wid != 7) {
#pragma unroll
      for (int gg = 0; gg < 8; ++gg) pv[gg][0] = ld1(poff, (((gg + prot) & 7) * 64) * 1024);
    }
    SCHED_FENCE();
    await(7, FL_P + prb, 8);
    STAMPK(2, 1);
#pragma unroll
    for (int gg = 0; gg < 8; ++gg) pv[gg][1] = ld1(poff, (((gg + prot) & 7) * 64 + 1) * 1024);
    if (wid == 7) {
#pragma unroll
      for (int gg = 0; gg < 8; ++gg) pv[gg][0] = ld1(poff, (((gg + prot) & 7) * 64) * 1024);
    }
    u32x4 xv[2], gav[2], cbv[2], sbv[2];
#pragma unroll
    for (int h = 0; h < 2; ++h) {
      xv[h] = *(const u32x4*)(p.x + (size_t)row * D_ + col + 8 * h);
      gav[h] = *(const u32x4*)(modb + 1024 + col + 8 * h); sbv[h] = *(const u32x4*)(modb + 1536 + col + 8 * h); cbv[h] = *(const u32x4*)(modb + 2048 + col + 8 * h);
    }
    SCHED_FENCE();
    __builtin_amdgcn_s_barrier();
#pragma unroll
    for (int s = 0; s < D1E; ++s) { A[s][0] = aload(s, 0); A[s][1] = aload(s, 1); }
    SCHED_FENCE();
    float v[16];
#pragma unroll
    for (int d = 0; d < 8; ++d) { v[2 * d] = bflo(pv[0][d & 1][d >> 1]); v[2 * d + 1] = bfhi(pv[0][d & 1][d >> 1]); }
#pragma unroll
    for (int gg = 1; gg < 8; ++gg) {
#pragma unroll
      for (int d = 0; d < 8; ++d) { v[2 * d] += bflo(pv[gg][d & 1][d >> 1]); v[2 * d + 1] += bfhi(pv[gg][d & 1][d >> 1]); }
      SCHED_FENCE();
    }
#pragma unroll
    for (int d = 0; d < 8; ++d) {
      const unsigned ga = gav[d >> 2][d & 3], xx = xv[d >> 2][d & 3];
      const unsigned x2 = pk2(__builtin_fmaf(bflo(ga), v[2 * d], bflo(xx)), __builtin_fmaf(bfhi(ga), v[2 * d + 1], bfhi(xx)));
      xv[d >> 2][d & 3] = x2; v[2 * d] = bflo(x2); v[2 * d + 1] = bfhi(x2);
    }
    if (nh == 0) { st1(WS_X2 + (size_t)row * 1024 + col * 2, xv[0]); st1(WS_X2 + (size_t)row * 1024 + col * 2 + 16, xv[1]); }
    STAMPK(2, 2);
    auto row_stats = [&](f32x2* rd, float& mean, float& rstd) AI {
      float s0 = 0, s1 = 0, q0 = 0, q1 = 0;
#pragma unroll
      for (int e = 0; e < 8; ++e) { s0 += v[2 * e]; s1 += v[2 * e + 1]; q0 = __builtin_fmaf(v[2 * e], v[2 * e], q0); q1 = __builtin_fmaf(v[2 * e + 1], v[2 * e + 1], q1); }
      float s = swap32_add(swap16_add(s0 + s1)), q = swap32_add(swap16_add(q0 + q1));
      if (g4 == 0) rd[wid * 16 + a] = (f32x2){s, q};
      asm volatile("s_waitcnt lgkmcnt(0)" ::: "memory"); __builtin_amdgcn_s_barrier(); MEM_FENCE();
      f32x2 t = rd[a];
#pragma unroll
      for (int w = 1; w < 8; ++w) t += rd[w * 16 + a];
      mean = t[0] * (1.f / 512.f);
      rstd = __builtin_amdgcn_rsqf(fmaxf(t[1] * (1.f / 512.f) - mean * mean, 0.f) + 1e-5f);
    };
    float mean, rstd;
#pragma unroll
    for (int s = D1E; s < D1; ++s) { A[s][0] = aload(s, 0); A[s][1] = aload(s, 1); }
    SCHED_FENCE();
    row_stats(red, mean, rstd);
    u32x4 hv[2];
#pragma unroll
    for (int d = 0; d < 8; ++d) {
      const unsigned cb = cbv[d >> 2][d & 3], sb = sbv[d >> 2][d & 3];
      const unsigned hb = pk2((v[2 * d] - mean) * rstd * bflo(cb) + bflo(sb), (v[2 * d + 1] - mean) * rstd * bfhi(cb) + bfhi(sb));
      hv[d & 1][d >> 1] = hb; v[2 * d] = bflo(hb); v[2 * d + 1] = bfhi(hb);
    }
    if (nh == 1) { st1(WS_HB + (size_t)row * 1024 + wid * 128 + g4 * 16, hv[0]); st1(WS_HB + (size_t)row * 1024 + wid * 128 + 64 + g4 * 16, hv[1]); }
    row_stats(red + 128, mean, rstd);
    const float nm = -mean * rstd;
#pragma unroll
    for (int h = 0; h < 2; ++h) {
      u32x4 o;
#pragma unroll
      for (int e = 0; e < 4; ++e) o[e] = pk2(__builtin_fmaf(v[8 * h + 2 * e], rstd, nm), __builtin_fmaf(v[8 * h + 2 * e + 1], rstd, nm));
      *(u32x4*)(lds + a * HT_STRIDE + (col + 8 * h) * 2) = o;
    }
    STAMPK(2, 3);
    asm volatile("s_waitcnt lgkmcnt(0)" ::: "memory"); __builtin_amdgcn_s_barrier(); MEM_FENCE();
    STAMPK(2, 4);
    f32x4 acc[1][2][1];
    gemm_steps<2, 1, 16, D1, 1, 4>(acc, A, aload, [&](int s, int) AI { return *(const bf16x8*)(bl + 64 * s); }, [](auto, auto) AI {});
    STAMPK(2, 5);
    u32x4 ov;
#pragma unroll
    for (int rg = 0; rg < 4; ++rg) ov[rg] = pk2(gelu_tanh(acc[0][0][0][rg]), gelu_tanh(acc[0][1][0][rg]));
    st1(WS_H1 + (size_t)(rt * 16 + 2 * G + ps) * 1024 + fo, ov);
    STAMPK(2, 6);
    publish(FL_H1 + rt);
    RT(2, 1);
    STAMPK(2, 7);
  }

  const int rb2 = (b & 7) * 4 + (b >> 6), ns = (b >> 3) & 7;
  const int ng = ns * 7 + (wid < 7 ? wid : 6);
  const size_t w3off = WS_W3T + (size_t)(ng >> 1) * 65536 + (ng & 1) * 2048 + fo;
  bf16x8 A[16][2];
  RT(3, 0);
  STAMPK(3, 0);
  {
    const int rt2 = (b & 7) * 8 + (b >> 5), nq = (b >> 3) & 3;
    const int G = nq * 2 + ((wid & 3) >> 1), ps = wid & 1;
    if (wid < 4) {
      const size_t aoff = WS_W2T + (size_t)G * 65536 + ps * 2048 + fo;
#pragma unroll
      for (int s = 0; s < 16; ++s) { A[s][0] = ldf1(aoff, (s * 4) * 1024); A[s][1] = ldf1(aoff, (s * 4 + 1) * 1024); }
    }
    SCHED_FENCE();
    { const int t = 2 * rt2 - 1 + (lane & 3); await(7, FL_H1 + (t < 0 ? 0 : t > 127 ? 127 : t), 2); }
    STAMPK(3, 1);
    const rsrc_t rH = mk_rsrc(ws + WS_H1, (unsigned)NXB);
    const int sh = (wid < 2) ? -1 : (wid < 4) ? 1 : (wid < 6) ? -2 : 2;
    u32x4 hf[2][2], hbv[2];
#pragma unroll
    for (int qt = 0; qt < 2; ++qt) {
      const int r = rt2 * 32 + qt * 16 + a + sh;
      const unsigned ho = (unsigned)((r >> 4) * 16384 + ((r & 15) + 16 * g4) * 16);
      hf[qt][0] = __builtin_amdgcn_raw_buffer_load_b128(rH, ho, (wid * 2) * 1024, 16); hf[qt][1] = __builtin_amdgcn_raw_buffer_load_b128(rH, ho, (wid * 2 + 1) * 1024, 16);
    }
    const size_t xoff = (size_t)(rt2 * 32 + a) * 1024 + G * 128 + ps * 64 + g4 * 16;
    if (wid < 4) { hbv[0] = ld1(WS_HB + xoff, 0); hbv[1] = ld1(WS_HB + xoff, 16384); }
#pragma unroll
    for (int qt = 0; qt < 2; ++qt) { *(u32x4*)(lds + (qt * 16 + wid * 2) * 1024 + fo) = hf[qt][0]; *(u32x4*)(lds + (qt * 16 + wid * 2 + 1) * 1024 + fo) = hf[qt][1]; }
    asm volatile("s_waitcnt lgkmcnt(0)" ::: "memory"); __builtin_amdgcn_s_barrier(); MEM_FENCE();
    STAMPK(3, 2);
    f32x2* red = (f32x2*)(lds + 32768);
    if (wid < 4) {
      f32x4 acc[1][2][2];
      gemm_steps<2, 2, 16, 16, 1, 3>(acc, A, [&](int, int) AI { return bf16x8{}; }, [&](int s, int qt) AI { return *(const bf16x8*)(lds + (qt * 16 + s) * 1024 + fo); }, [](auto, auto) AI {});
#pragma unroll
      for (int qt = 0; qt < 2; ++qt) {
        u32x4 ov; float s = 0, q = 0;
#pragma unroll
        for (int rg = 0; rg < 4; ++rg) {
          ov[rg] = pk2(bflo(hbv[qt][rg]) + acc[0][0][qt][rg], bfhi(hbv[qt][rg]) + acc[0][1][qt][rg]);
          const float lo = bflo(ov[rg]), hi = bfhi(ov[rg]); s += lo + hi; q += lo * lo + hi * hi;
        }
        st1(WS_X1 + xoff + qt * 16384, ov);
        s = swap32_add(swap16_add(s)); q = swap32_add(swap16_add(q));
        if (g4 == 0) red[wid * 32 + qt * 16 + a] = (f32x2){s, q};
      }
    } else if (wid < 7) {
#pragma unroll
      for (int s = 0; s < 16; ++s) { A[s][0] = ldf1(w3off, (s * 4) * 1024); A[s][1] = ldf1(w3off, (s * 4 + 1) * 1024); }
    }
    asm volatile("s_waitcnt lgkmcnt(0)" ::: "memory"); __builtin_amdgcn_s_barrier(); MEM_FENCE();
    if (tid < 32) {
      const f32x2 t = red[tid] + red[32 + tid] + red[64 + tid] + red[96 + tid];
      __hip_atomic_store((gu64*)(ws + WS_ST + (size_t)(rt2 * 32 + tid) * 32 + nq * 8), __builtin_bit_cast(unsigned long long, t), __ATOMIC_RELAXED, __HIP_MEMORY_SCOPE_AGENT);
    }
    STAMPK(3, 3);
    publish(FL_X1 + (rt2 >> 1));
    RT(3, 1);
    STAMPK(3, 4);
  }

  bf16x8 W[7][4];
  const size_t woff = WS_W4T + (size_t)(ns * 56 + wid * 7) * 4096 + fo;
  auto rot7 = [&](int s) AI { const int t = s + (ROT ? (ns < 7 ? ns : 0) : 0); return t >= 7 ? t - 7 : t; };
  RT(4, 0);
  STAMPK(4, 0);
  {
    if (wid < 4) {
#pragma unroll
      for (int s = 0; s < 16; ++s) { A[s][0] = ldf1(w3off, (s * 4) * 1024); A[s][1] = ldf1(w3off, (s * 4 + 1) * 1024); }
    }
    SCHED_FENCE();
    await(7, FL_X1 + rb2, 8);
    STAMPK(4, 1);
    u32x4 xv[8];
    const u32x4 stv = ld1(WS_ST + (size_t)(rb2 * 64 + wid * 8) * 32 + (lane & 15) * 16, 0);
#pragma unroll
    for (int i = 0; i < 8; ++i) xv[i] = ld1(WS_X1 + (size_t)(rb2 * 64 + wid * 8) * 1024 + fo, i * 1024);
    SCHED_FENCE();
    const float sm = dpp_add<0xB1>(__uint_as_float(stv[0]) + __uint_as_float(stv[2])) * (1.f / 512.f);
    const float sq = dpp_add<0xB1>(__uint_as_float(stv[1]) + __uint_as_float(stv[3])) * (1.f / 512.f);
    const float rsl = __builtin_amdgcn_rsqf(fmaxf(sq - sm * sm, 0.f) + 1e-5f), nml = -sm * rsl;
    float rs[8], nm[8];
#pragma unroll
    for (int i = 0; i < 8; ++i) { rs[i] = __uint_as_float(__builtin_amdgcn_readlane(__float_as_uint(rsl), 2 * i)); nm[i] = __uint_as_float(__builtin_amdgcn_readlane(__float_as_uint(nml), 2 * i)); }
    SCHED_FENCE();
#pragma unroll
    for (int hf = 0; hf < 2; ++hf) {
      float lo[16], hi[16]; unsigned t[16];
      H_STAGE(lo[j] = bflo(xv[i][d]); hi[j] = bfhi(xv[i][d]));
      H_STAGE(lo[j] = __builtin_fmaf(lo[j], rs[i], nm[i]); hi[j] = __builtin_fmaf(hi[j], rs[i], nm[i]));
      H_STAGE(t[j] = pk2(lo[j], hi[j]));
#pragma unroll
      for (int i = 0; i < 4; ++i) *(u32x4*)(lds + (wid * 8 + hf * 4 + i) * HT_STRIDE + fo) = (u32x4){t[4 * i], t[4 * i + 1], t[4 * i + 2], t[4 * i + 3]};
      SCHED_FENCE();
    }
    STAMPK(4, 2);
    asm volatile("s_waitcnt lgkmcnt(0)" ::: "memory"); __builtin_amdgcn_s_barrier(); MEM_FENCE();
    STAMPK(4, 3);
    if (wid < 7) {
      f32x4 acc[2][2][2];
      if (wid >= 4) __builtin_amdgcn_s_setprio(1);
      const size_t dst = WS_U + (size_t)(rb2 * 4 * 56 + ng) * 1024 + fo;
      u32x4 ow;
      auto gpart = [&](int ps, int k) AI {
        unsigned w = pk2(gelu_tanh(acc[ps][0][k >> 2][k & 3]), gelu_tanh(acc[ps][1][k >> 2][k & 3])); PIN(w); ow[k & 3] = w;
        if ((k & 3) == 3) st1(dst + (size_t)(2 * ps + (k >> 2)) * 56 * 1024, ow);
      };
      gemm_steps<2, 2, 32, 16, 2, 3>(acc, A, [&](int s, int i) AI { return A[s & 15][i]; },
                                     [&](int s, int qt) AI { return *(const bf16x8*)(bl + ((s >> 4) * 2 + qt) * 16 * HT_STRIDE + 64 * (s & 15)); },
                                     [&](auto S_, auto M_) AI {
                                       constexpr int s = S_.value, m = M_.value;
                                       if constexpr (s == PRIO_G3 && m == 0) __builtin_amdgcn_s_setprio(0);
                                       if constexpr (s >= 17 && (s & 1) && m == 1) gpart(0, (s - 17) >> 1);
                                       if constexpr (s >= 17 && s < 31 && m >= 2) { constexpr int k = (s - 17) * 2 + m - 2; W[k >> 2][k & 3] = ldf1(woff + (k & 3) * 1024, rot7(k >> 2) * 4096); }
                                     });
      STAMPK(4, 4);
#pragma unroll
      for (int k = 0; k < 8; ++k) gpart(1, k);
    } else {
#pragma unroll
      for (int k = 0; k < 28; ++k) W[k >> 2][k & 3] = ldf1(woff + (k & 3) * 1024, rot7(k >> 2) * 4096);
    }
    STAMPK(4, 5);
    publish(FL_U + rb2);
    RT(4, 1);
    STAMPK(4, 6);
  }

  RT(5, 0);
  STAMPK(5, 0);
  {
    bf16x8 B[7][4];
    const size_t uoff = WS_U + (size_t)(rb2 * 4 * 56 + wid * 7) * 1024 + fo;
    const int oqt = wid >> 1, orp = wid & 1, orow = rb2 * 64 + oqt * 16 + a, ocol = ns * 64 + g4 * 16 + orp * 8;
    u32x2 x1a, x1b; u32x4 x2v, gbv;
    SCHED_FENCE();
    await(0, FL_U + rb2, 8);
#pragma unroll
    for (int s = 0; s < G4_AHEAD; ++s)
#pragma unroll
      for (int i = 0; i < 4; ++i) B[s][i] = ldf1(uoff, (i * 56 + rot7(s)) * 1024);
    SCHED_FENCE();
    STAMPK(5, 1);
    f32x4 acc[4][4] = {};
#pragma unroll
    for (int s = 0; s < 7; ++s) {
#pragma unroll
      for (int i = 0; i < 4; ++i) {
#pragma unroll
        for (int qt = 0; qt < 4; ++qt) acc[i][qt] = MFMA16(W[s][i], B[s][qt], acc[i][qt]);
        if (s + G4_AHEAD < 7) B[s + G4_AHEAD][i] = ldf1(uoff, (i * 56 + rot7(s + G4_AHEAD)) * 1024);
        SCHED_FENCE();
      }
      if (s == 5) {
        const size_t x1o = WS_X1 + (size_t)orow * 1024 + ns * 128 + g4 * 16 + orp * 8;
        x1a = __builtin_amdgcn_raw_buffer_load_b64(rWs, (unsigned)x1o, 0, 16); x1b = __builtin_amdgcn_raw_buffer_load_b64(rWs, (unsigned)x1o, 64, 16);
        x2v = ld1(WS_X2 + (size_t)orow * 1024 + ocol * 2, 0); gbv = *(const u32x4*)(modb + 2560 + ocol);
        SCHED_FENCE();
      }
    }
    STAMPK(5, 2);
#pragma unroll
    for (int qt = 0; qt < 4; ++qt)
#pragma unroll
      for (int rg = 0; rg < 4; ++rg)
        *(f32x4*)(lds + ((wid * 4 + qt) * 4 + rg) * 1024 + fo) = (f32x4){acc[0][qt][rg], acc[1][qt][rg], acc[2][qt][rg], acc[3][qt][rg]};
    STAMPK(5, 3);
    __syncthreads();
    STAMPK(5, 4);
    f32x4 s0 = {}, s1 = {};
#pragma unroll
    for (int w = 0; w < 8; ++w) {
      s0 += *(const f32x4*)(lds + ((w * 4 + oqt) * 4 + 2 * orp) * 1024 + fo);
      s1 += *(const f32x4*)(lds + ((w * 4 + oqt) * 4 + 2 * orp + 1) * 1024 + fo);
    }
    const float h2[8] = {s0[0], s0[1], s0[2], s0[3], s1[0], s1[1], s1[2], s1[3]};
    const unsigned x1v[4] = {x1a[0], x1b[0], x1a[1], x1b[1]};
    u32x4 ov;
#pragma unroll
    for (int e = 0; e < 4; ++e)
      ov[e] = pk2(bflo(x2v[e]) + bflo(gbv[e]) * (bflo(x1v[e]) + h2[2 * e]), bfhi(x2v[e]) + bfhi(gbv[e]) * (bfhi(x1v[e]) + h2[2 * e + 1]));
    *(u32x4*)(p.out + (size_t)orow * D_ + ocol) = ov;
    STAMPK(5, 5);
    RT(5, 1);
    STAMPK(5, 6);
  }
  };
  {
    int l2; asm volatile("v_mbcnt_lo_u32_b32 %0, -1, 0\n\tv_mbcnt_hi_u32_b32 %0, -1, %0" : "=v"(l2));
    tail(l2, l2 & 15, l2 >> 4, (unsigned)l2 * 16u, wid * 64 + l2, lds + (l2 & 15) * HT_STRIDE + (l2 >> 4) * 16);
  }
}

extern "C" void kernel_launch(void* const* d_in, const int* in_sizes, int n_in, void* d_out, int out_size, void* d_ws, size_t ws_size, hipStream_t stream) {
  char* ws = (char*)d_ws;
  K0Args p0;
  p0.x = (const u16*)d_in[0]; p0.c = (const u16*)d_in[10]; p0.wmod = (const u16*)d_in[11]; p0.wq = (const u16*)d_in[3]; p0.w1 = (const u16*)d_in[6]; p0.kc = (const u16*)d_in[1]; p0.vc = (const u16*)d_in[2]; p0.ws = ws;
  K1Args p1;
  p1.x = p0.x; p1.kc = (const u16*)d_in[1]; p1.vc = (const u16*)d_in[2]; p1.wo = (const u16*)d_in[4];
  p1.w2 = (const u16*)d_in[7]; p1.w3 = (const u16*)d_in[8]; p1.w4 = (const u16*)d_in[9]; p1.lnst = (const float*)(ws + WS_Y); p1.ws = ws; p1.out = (u16*)d_out;
  (void)hipFuncSetAttribute((const void*)k1_attn, hipFuncAttributeMaxDynamicSharedMemorySize, L1_END);
  k0_prep<<<K0_BLOCKS, 512, 0, stream>>>(p0);
  k1_attn<<<256, 512, L1_END, stream>>>(p1);
}
```

```cpp
#include <hip/hip_runtime.h>
#include <hip/hip_bf16.h>
#include <type_traits>
#include <utility>

typedef unsigned short u16;
typedef __bf16 bf16x8 __attribute__((ext_vector_type(8)));
typedef __bf16 bf16x4 __attribute__((ext_vector_type(4)));
typedef __bf16 bf16x2 __attribute__((ext_vector_type(2)));
typedef float f32x16 __attribute__((ext_vector_type(16)));
typedef float f32x4 __attribute__((ext_vector_type(4)));
typedef float f32x2 __attribute__((ext_vector_type(2)));
typedef unsigned u32x2 __attribute__((ext_vector_type(2)));
typedef unsigned u32x4 __attribute__((ext_vector_type(4)));
typedef __amdgpu_buffer_rsrc_t rsrc_t;
#define DI __device__ __forceinline__
#define AI __attribute__((always_inline))
#define LDSP(T, p) ((__attribute__((address_space(3))) T*)(p))
#define SCHED_FENCE() __builtin_amdgcn_sched_barrier(0)
#define PIN(x) asm volatile("" : "+v"(x))
#define MEM_FENCE() asm volatile("" ::: "memory")
#define WAIT_VM0() __builtin_amdgcn_s_waitcnt(0x0F70)

constexpr int S_ = 2048, D_ = 512, HQ = 32, HKV = 8, DH = 128, SK = 2048, WIN = 512, F_ = 1792;

constexpr size_t NXB = (size_t)S_ * D_ * 2;
constexpr size_t WS_MOD = 0;
constexpr size_t WS_Y   = 8192;
constexpr size_t WS_X2  = 65536;
constexpr size_t WS_HB  = WS_X2 + NXB;
constexpr size_t WS_H1  = WS_HB + NXB;
constexpr size_t WS_X1  = WS_H1 + NXB;
constexpr size_t WS_U   = WS_X1 + NXB;
constexpr size_t WS_PART = WS_U + (size_t)S_ * F_ * 2;
constexpr size_t WS_WQT = WS_PART + 8 * NXB;
constexpr size_t WS_WOT = WS_WQT + (size_t)D_ * 4096 * 2;
constexpr size_t WS_W1T = WS_WOT + (size_t)D_ * 4096 * 2;
constexpr size_t WS_W2T = WS_W1T + (size_t)D_ * D_ * 2;
constexpr size_t WS_W3T = WS_W2T + (size_t)D_ * D_ * 2;
constexpr size_t WS_W4T = WS_W3T + (size_t)D_ * F_ * 2;
constexpr size_t WS_ST  = WS_W4T + (size_t)D_ * F_ * 2;
constexpr size_t WS_FLAG = WS_ST + (size_t)S_ * 32;
constexpr int FL_W = 0, FL_P = 8, FL_H1 = 40, FL_X1 = 168, FL_U = 200, FL_P0 = 232, FL_N = 264;
constexpr size_t WS_END = WS_FLAG + FL_N * 128;

DI float bflo(unsigned u) { return __uint_as_float(u << 16); }
DI float bfhi(unsigned u) { return __uint_as_float(u & 0xffff0000u); }
DI unsigned pk2(float lo, float hi) { f32x2 v = {lo, hi}; return __builtin_bit_cast(unsigned, __builtin_convertvector(v, bf16x2)); }
DI float rb(float x) { return __uint_as_float(pk2(0.f, x)); }
DI rsrc_t mk_rsrc(const void* p, unsigned bytes) { return __builtin_amdgcn_make_buffer_rsrc((void*)p, (short)0, (int)bytes, 0x00020000); }
DI unsigned rfl(unsigned x) { return __builtin_amdgcn_readfirstlane(x); }
DI bf16x8 ldfrag(rsrc_t r, unsigned voff, unsigned soff) { return __builtin_bit_cast(bf16x8, __builtin_amdgcn_raw_buffer_load_b128(r, voff, soff, 0)); }
template <int CTRL> DI float dpp_add(float v) {
  return v + __uint_as_float(__builtin_amdgcn_update_dpp(0u, __float_as_uint(v), CTRL, 0xf, 0xf, true));
}
DI float swap16_add(float v) { auto r = __builtin_amdgcn_permlane16_swap(__float_as_uint(v), __float_as_uint(v), false, false); return __uint_as_float(r[0]) + __uint_as_float(r[1]); }
DI float swap32_add(float v) { auto r = __builtin_amdgcn_permlane32_swap(__float_as_uint(v), __float_as_uint(v), false, false); return __uint_as_float(r[0]) + __uint_as_float(r[1]); }
DI float swap32_max(float v) { auto r = __builtin_amdgcn_permlane32_swap(__float_as_uint(v), __float_as_uint(v), false, false); return fmaxf(__uint_as_float(r[0]), __uint_as_float(r[1])); }
DI float wave_sum(float v) {
  v = dpp_add<0xB1>(v); v = dpp_add<0x4E>(v); v = dpp_add<0x141>(v); v = dpp_add<0x140>(v);
  v = swap16_add(v); v = swap32_add(v); return v;
}
DI float gelu_tanh(float x) {
  const float u = 0.7978845608028654f * (x + 0.044715f * x * x * x);
  return x * __builtin_amdgcn_rcpf(1.f + __builtin_amdgcn_exp2f(-2.885390081777927f * u));
}
DI void ln_consts(float mean, float var, float& m_bf, float& d_bf) { m_bf = rb(mean); d_bf = rb(__builtin_sqrtf(rb(rb(var) + 1e-5f))); }

#define MFMA16(a, b, c) __builtin_amdgcn_mfma_f32_16x16x32_bf16((a), (b), (c), 0, 0, 0)
#define MFMA32(a, b, c) __builtin_amdgcn_mfma_f32_32x32x16_bf16((a), (b), (c), 0, 0, 0)

DI void dma16(unsigned lds_dst, unsigned voff, rsrc_t r, unsigned soff) {
  asm volatile("s_mov_b32 m0, %0\n\ts_nop 0\n\tbuffer_load_dwordx4 %1, %2, %3 offen lds" ::"s"(lds_dst), "v"(voff), "s"(r), "s"(soff) : "memory");
}

struct Retile { u32x2 d[8]; };
DI void retile_load(Retile& t, const u16* __restrict__ W, int ldw, int k0, bool perm, int n0, int lane) {
  const int a = lane & 15, g4 = lane >> 4;
  const u16* p = W + (size_t)(k0 + (perm ? 16 : 8) * g4) * ldw + n0 + 4 * a;
  const int s2 = (perm ? 4 : 2) * ldw;
#pragma unroll
  for (int j = 0; j < 8; ++j) {
    const u32x2* q = (const u32x2*)(p + (j >> 1) * s2 + (j & 1) * ldw);
    t.d[j] = __builtin_nontemporal_load(q);
  }
}
DI u32x4 retile_frag(const Retile& t, int i) {
  u32x4 f;
#pragma unroll
  for (int p = 0; p < 4; ++p) f[p] = __builtin_amdgcn_perm(t.d[2 * p + 1][i >> 1], t.d[2 * p][i >> 1], (i & 1) ? 0x07060302u : 0x05040100u);
  return f;
}

struct K0Args { const u16 *x, *c, *wmod, *wq, *w1; char* ws; };
constexpr int K0_BLOCKS = 352 + 128 + 16;
__global__ __launch_bounds__(512) void k0_prep(const K0Args p) {
  __shared__ float red[8][64];
  const int tid = threadIdx.x, lane = tid & 63, wid = tid >> 6;
  int b = blockIdx.x;
  if (b < 96) {
    const int n0 = b * 32, cg = tid & 3, kq = tid >> 2;
    u32x4 wv[4]; float sv[4];
#pragma unroll
    for (int i = 0; i < 4; ++i) wv[i] = *(const u32x4*)(p.wmod + (size_t)(kq + 128 * i) * 3072 + n0 + cg * 8);
#pragma unroll
    for (int i = 0; i < 4; ++i) { float cv = bflo(p.c[kq + 128 * i]); sv[i] = rb(cv / (1.f + __expf(-cv))); }
    float acc[8] = {};
#pragma unroll
    for (int i = 0; i < 4; ++i)
#pragma unroll
      for (int e = 0; e < 4; ++e) { acc[2 * e] += sv[i] * bflo(wv[i][e]); acc[2 * e + 1] += sv[i] * bfhi(wv[i][e]); }
#pragma unroll
    for (int e = 0; e < 8; ++e) { float v = acc[e]; v += __shfl_xor(v, 4); v += __shfl_xor(v, 8); v = swap16_add(v); v = swap32_add(v); acc[e] = v; }
    if (lane < 4)
#pragma unroll
      for (int e = 0; e < 8; ++e) red[wid][lane * 8 + e] = acc[e];
    __syncthreads();
    if (tid < 32) {
      float s = 0;
#pragma unroll
      for (int w = 0; w < 8; ++w) s += red[w][tid];
      s = rb(s);
      const int n = n0 + tid, seg = n >> 9;
      if (seg == 1 || seg == 4) s = rb(1.f + s);
      ((u16*)(p.ws + WS_MOD))[n] = (u16)(__float_as_uint(s) >> 16);
    }
    return;
  }
  b -= 96;
  if (b < 256) {
    const int row = b * 8 + wid;
    const u32x4 xv = *(const u32x4*)(p.x + (size_t)row * D_ + lane * 8);
    u32x4 wv[8];
#pragma unroll
    for (int i = 0; i < 8; ++i) wv[i] = *(const u32x4*)(p.x + (size_t)((b >> 3) * 64 + wid * 8 + i) * D_ + lane * 8);
    float f[8];
#pragma unroll
    for (int e = 0; e < 4; ++e) { f[2 * e] = bflo(xv[e]); f[2 * e + 1] = bfhi(xv[e]); }
    float s = 0;
#pragma unroll
    for (int e = 0; e < 8; ++e) s += f[e];
    const float mean = wave_sum(s) * (1.f / 512.f);
    float q = 0;
#pragma unroll
    for (int e = 0; e < 8; ++e) { float d = f[e] - mean; q += d * d; }
    const float var = wave_sum(q) * (1.f / 512.f);
    float mb, db; ln_consts(mean, var, mb, db);
    if (lane == 0) *(f32x2*)(p.ws + WS_Y + (size_t)row * 8) = (f32x2){mb, db};
#pragma unroll
    for (int i = 0; i < 8; ++i) asm volatile("" ::"v"(wv[i]));
    return;
  }
  b -= 256;
  Retile t; char* dst;
  if (b < 128) { const int g = b & 7, s = b >> 3, ng = g * 8 + wid; retile_load(t, p.wq, 4096, 32 * s, false, 64 * ng, lane); dst = p.ws + WS_WQT + (size_t)(ng * 16 + s) * 4096; }
  else {
    const int s = b - 128; retile_load(t, p.w1, 512, 32 * s, false, 64 * wid, lane); dst = p.ws + WS_W1T + (size_t)(wid * 16 + s) * 4096;
    if (s == 0 && tid < FL_N) *(unsigned*)(p.ws + WS_FLAG + tid * 128) = 0u;
  }
#pragma unroll
  for (int i = 0; i < 4; ++i) *(u32x4*)(dst + i * 1024 + lane * 16) = retile_frag(t, i);
}

template <int V> using IC = std::integral_constant<int, V>;
template <int N, class F, int... Is> DI void sfor_(F&& f, std::integer_sequence<int, Is...>) { (f(IC<Is>{}), ...); }
template <int N, class F> DI void sfor(F&& f) { sfor_<N>(f, std::make_integer_sequence<int, N>{}); }
template <int NI, int NQT, int NSTEP, int DEPTH, int NSET, int BD = 1, class ALoad, class BRead, class Hook>
DI void gemm_steps(f32x4 (&acc)[NSET][NI][NQT], bf16x8 (&ring)[DEPTH][NI], ALoad aload, BRead bread, Hook hook) {
  constexpr int NM = NI * NQT, PER = NSTEP / NSET;
  bf16x8 B[BD + 1][NQT];
#pragma unroll
  for (int s = 0; s < BD; ++s)
#pragma unroll
    for (int qt = 0; qt < NQT; ++qt) B[s][qt] = bread(s, qt);
  SCHED_FENCE();
  sfor<NSTEP * NM>([&](auto X) AI {
    constexpr int s = X.value / NM, m = X.value % NM, i = m / NQT, qt = m % NQT;
    if constexpr (s % PER == 0) acc[s / PER][i][qt] = MFMA16(ring[s % DEPTH][i], B[s % (BD + 1)][qt], (f32x4){});
    else                        acc[s / PER][i][qt] = MFMA16(ring[s % DEPTH][i], B[s % (BD + 1)][qt], acc[s / PER][i][qt]);
    if constexpr (m < NQT && s + BD < NSTEP) B[(s + BD) % (BD + 1)][m] = bread(s + BD, m);
    if constexpr (qt == NQT - 1 && s + DEPTH < NSTEP) ring[s % DEPTH][i] = aload(s + DEPTH, i);
    hook(IC<s>{}, IC<m>{});
    SCHED_FENCE();
  });
}

constexpr int HT_STRIDE = 1056, QX_STRIDE = 272;
constexpr int L1_HT = 0, L1_K = 69632, L1_V = L1_K + 3 * 16384, L1_END = L1_V + 2 * 16384;
constexpr int SL_[9] = {L1_K, L1_K + 16384, L1_K + 32768, L1_K + 49152, L1_K + 65536, 0, 16384, 32768, 49152};
constexpr int kslot(int t) { constexpr int k[8] = {0, 1, 2, 0, 3, 6, 8, 4}; return SL_[k[t]]; }
constexpr int vslot(int t) { constexpr int v[8] = {3, 4, 1, 5, 7, 2, 0, 1}; return SL_[v[t]]; }
constexpr float SM_C = 0.12751743082459868f;
constexpr int SM_SHIFT = 100;
constexpr float SM_LIMIT = __builtin_bit_cast(float, (unsigned)(127 + 40 - SM_SHIFT) << 23);

DI float sm_arg(float s, float negmc) { return __builtin_fmaf(rb(s), SM_C, negmc); }
DI float max16(const f32x16& S) {
  float m = fmaxf(S[0], S[1]);
#pragma unroll
  for (int r = 2; r < 16; ++r) m = fmaxf(m, S[r]);
  return m;
}
DI unsigned scale_pk(unsigned p, float f) { return pk2(bflo(p) * f, bfhi(p) * f); }
DI void sm_half(const f32x16& S, float negmc, float& l0, float& l1, u32x4& pa, u32x4& pb) {
#pragma unroll
  for (int p = 0; p < 4; ++p) {
    const float e0 = __builtin_amdgcn_exp2f(sm_arg(S[2 * p], negmc)), e1 = __builtin_amdgcn_exp2f(sm_arg(S[2 * p + 1], negmc));
    l0 += e0; l1 += e1; pa[p] = pk2(e0, e1);
  }
#pragma unroll
  for (int p = 0; p < 4; ++p) {
    const float e0 = __builtin_amdgcn_exp2f(sm_arg(S[8 + 2 * p], negmc)), e1 = __builtin_amdgcn_exp2f(sm_arg(S[8 + 2 * p + 1], negmc));
    l0 += e0; l1 += e1; pb[p] = pk2(e0, e1);
  }
}
struct SmPipe { float r[16], x[16], e[16]; };
DI void sm_gap(const int m, SmPipe& sp, const f32x16& S, float negmc, float& l0, float& l1, u32x4& pa, u32x4& pb) {
#pragma unroll
  for (int e = 0; e < 16; ++e) {
    if (e * 12 / 16 + 3 == m) {
      if (e & 1) {
        l1 += sp.e[e]; PIN(l1);
        unsigned w = pk2(sp.e[e - 1], sp.e[e]); PIN(w);
        if (e < 8) pa[e >> 1] = w; else pb[(e - 8) >> 1] = w;
      } else { l0 += sp.e[e]; PIN(l0); }
    }
  }
#pragma unroll
  for (int e = 0; e < 16; ++e) if (e * 12 / 16 + 2 == m) sp.e[e] = __builtin_amdgcn_exp2f(sp.x[e]);
#pragma unroll
  for (int e = 0; e < 16; ++e) if (e * 12 / 16 + 1 == m) sp.x[e] = __builtin_fmaf(sp.r[e], SM_C, negmc);
#pragma unroll
  for (int e = 0; e < 16; ++e) if (e * 12 / 16 == m) sp.r[e] = rb(S[e]);
}
DI void pack_acc16(const f32x4 (&acc)[4][4], int qt, u32x4& w0, u32x4& w1) {
#pragma unroll
  for (int rg = 0; rg < 2; ++rg) {
    w0[2 * rg] = pk2(acc[0][qt][rg], acc[1][qt][rg]); w0[2 * rg + 1] = pk2(acc[2][qt][rg], acc[3][qt][rg]);
    w1[2 * rg] = pk2(acc[0][qt][2 + rg], acc[1][qt][2 + rg]); w1[2 * rg + 1] = pk2(acc[2][qt][2 + rg], acc[3][qt][2 + rg]);
  }
}

typedef __attribute__((address_space(1))) unsigned gu32;
typedef __attribute__((address_space(1))) unsigned long long gu64;
constexpr unsigned SPIN_CAP = 1u << 19;
struct K1Args { const u16 *x, *kc, *vc, *wo, *w2, *w3, *w4; const float* lnst; char* ws; u16* out; };

__global__ __launch_bounds__(512, 2) __attribute__((amdgpu_waves_per_eu(2, 2)))
void k1_attn(const K1Args p) {
  extern __shared__ __attribute__((aligned(16))) char lds[];
  char* __restrict__ ws = p.ws;
  const int tid = threadIdx.x, lane = tid & 63;
  const int wid = (int)rfl((unsigned)tid >> 6);
  const int g = blockIdx.x & 7, rbk = blockIdx.x >> 3;
  const int a = lane & 15, g4 = lane >> 4, r32 = lane & 31, hh = lane >> 5;
  const unsigned ldsb = (unsigned)(size_t)lds;
  const unsigned fo = lane * 16;
  const rsrc_t rK = mk_rsrc(p.kc + ((size_t)g * SK + (SK - WIN)) * DH, WIN * DH * 2), rV = mk_rsrc(p.vc + ((size_t)g * SK + (SK - WIN)) * DH, WIN * DH * 2);
  const unsigned drow = wid * 8 + ((lane >> 2) & 7);
  const unsigned kvoff = drow * 256 + (lane >> 5) * 64 + (((lane & 3) ^ ((drow >> 2) & 3)) * 16);
  const unsigned vvoff = drow * 256 + (lane >> 5) * 64 + (lane & 3) * 16;
  const unsigned kdst = ldsb + wid * 2048;
  auto dmaK1 = [&](int t, int j) AI { dma16(kdst + kslot(t) + j * 1024, kvoff, rK, t * 16384 + j * 128); };
  auto dmaV1 = [&](int t, int j) AI { dma16(kdst + vslot(t) + j * 1024, vvoff, rV, t * 16384 + j * 128); };
  auto dmaK = [&](int t) AI { dmaK1(t, 0); dmaK1(t, 1); };
  auto dmaV = [&](int t) AI { dmaV1(t, 0); dmaV1(t, 1); };
  constexpr int DEPTH = 6;
  bf16x8 ring[DEPTH][4];
  const rsrc_t rWq = mk_rsrc(ws + WS_WQT + (size_t)(g * 8 + wid) * 65536, 65536);
  const int rot = rbk & 15;
  auto wq_load = [&](int s, int i) AI { return ldfrag(rWq, fo + i * 1024, ((s + rot) & 15) * 4096); };
  {
    const u32x4 sav = *(const u32x4*)(ws + WS_MOD + fo), cav = *(const u32x4*)(ws + WS_MOD + 1024 + fo);
    u32x4 xv[8];
#pragma unroll
    for (int i = 0; i < 8; ++i) xv[i] = *(const u32x4*)((const char*)p.x + (size_t)(rbk * 64 + wid * 8 + i) * 1024 + fo);
    float st[16];
#pragma unroll
    for (int i = 0; i < 16; ++i) st[i] = p.lnst[(rbk * 64 + wid * 8) * 2 + i];
    SCHED_FENCE();
    dma16(kdst + L1_K, fo, wid < 4 ? rK : rV, (rbk * 4 + (wid & 3)) * 1024);
    SCHED_FENCE();
    float ca[8], sa[8];
#pragma unroll
    for (int e = 0; e < 4; ++e) { ca[2 * e] = bflo(cav[e]); ca[2 * e + 1] = bfhi(cav[e]); sa[2 * e] = bflo(sav[e]); sa[2 * e + 1] = bfhi(sav[e]); }
    if (wid >= 4) __builtin_amdgcn_s_setprio(1);
#pragma unroll
    for (int hf = 0; hf < 2; ++hf) {
      float lo[16], hi[16], rr[4]; unsigned t[16];
#define H_STAGE(body) _Pragma("unroll") for (int j = 0; j < 16; ++j) { const int i = hf * 4 + (j >> 2), d = j & 3; (void)i; (void)d; body; } SCHED_FENCE()
#pragma unroll
      for (int i = 0; i < 4; ++i) rr[i] = __builtin_amdgcn_rcpf(st[2 * (hf * 4 + i) + 1]);
#define W_PRE(k) do { ring[(hf * 12 + (k)) >> 2][(k) & 3] = wq_load((hf * 12 + (k)) >> 2, (k) & 3); } while (0)
      H_STAGE(lo[j] = bflo(xv[i][d]); hi[j] = bfhi(xv[i][d]));
      W_PRE(0); W_PRE(1);
      H_STAGE(lo[j] -= st[2 * i]; hi[j] -= st[2 * i]);
      W_PRE(2);
      H_STAGE(lo[j] = rb(lo[j]); hi[j] = rb(hi[j]));
      if (hf == 1) __builtin_amdgcn_s_setprio(0);
      W_PRE(3); W_PRE(4);
      H_STAGE(lo[j] *= rr[j >> 2]; hi[j] *= rr[j >> 2]);
      W_PRE(5);
      H_STAGE(lo[j] = rb(lo[j]); hi[j] = rb(hi[j]));
      W_PRE(6); W_PRE(7);
      H_STAGE(lo[j] *= ca[2 * d]; hi[j] *= ca[2 * d + 1]);
      W_PRE(8);
      H_STAGE(lo[j] = rb(lo[j]); hi[j] = rb(hi[j]));
      W_PRE(9); W_PRE(10);
      H_STAGE(lo[j] += sa[2 * d]; hi[j] += sa[2 * d + 1]);
      W_PRE(11);
      H_STAGE(t[j] = pk2(lo[j], hi[j]));
#pragma unroll
      for (int i = 0; i < 4; ++i) *(u32x4*)(lds + L1_HT + (wid * 8 + hf * 4 + i) * HT_STRIDE + fo) = (u32x4){t[4 * i], t[4 * i + 1], t[4 * i + 2], t[4 * i + 3]};
      SCHED_FENCE();
    }
  }

  Retile rtl; const u16* rsrc_w; int rld, rk0, rn0; bool rperm; size_t rdst; unsigned rhalf = 2048;
  if (wid < 4) { const int id = rbk * 4 + wid, s = id >> 3, ng = id & 7; rsrc_w = p.wo; rld = 512; rk0 = g * 512 + 32 * s; rn0 = 64 * ng; rperm = false; rdst = WS_WOT + (size_t)(g * 8 + ng) * 65536 + s * 2048; rhalf = 32768; }
  else {
    int id = blockIdx.x * 4 + wid - 4, s, ng; rperm = true;
    if (id < 128)      { ng = id >> 4; s = id & 15; rsrc_w = p.w2; rld = 512; rdst = WS_W2T + (size_t)id * 4096; }
    else if (id < 576) { id -= 128; ng = id >> 4; s = id & 15; rsrc_w = p.w3; rld = F_; rdst = WS_W3T + (size_t)id * 4096; }
    else               { id -= 576; ng = id & 7; s = id >> 3; rsrc_w = p.w4; rld = 512; rdst = WS_W4T + (size_t)(ng * 56 + s) * 4096; }
    rk0 = 64 * (s >> 1) + 2 * (s & 1); rn0 = 64 * ng;
  }
  gu32* flags = (gu32*)(ws + WS_FLAG);
  gu32* flag = flags + (FL_W + g) * 32;
  gu32* flagw = flags + (FL_W + (lane & 7)) * 32;
  __syncthreads();
  if (wid >= 4) __builtin_amdgcn_s_setprio(1);

  const char* bl = lds + L1_HT + a * HT_STRIDE + g4 * 16;
  auto bread = [&](int s, int qt) AI { return *(const bf16x8*)(bl + 64 * ((s + rot) & 15) + qt * 16 * HT_STRIDE); };
  f32x16 o[4]; bf16x8 qf[8]; float l0, l1, negmc;
  {
    f32x4 acc[1][4][4];
    gemm_steps<4, 4, 16, DEPTH, 1>(acc, ring, wq_load, bread, [&](auto S_, auto M_) AI {
      constexpr int s = S_.value, m = M_.value;
      if constexpr (s == 11 && m == 0) __builtin_amdgcn_s_setprio(0);
      if constexpr (s == 11 && m == 8) { WAIT_VM0(); dmaK(0); }
      if constexpr (s == 12 && m == 0) dmaK(1);
      if constexpr (s == 12 && m == 8) dmaV(0);
      if constexpr (s == 13 && m == 0) dmaK(2);
      if constexpr (s == 13 && m == 8) dmaV(1);
    });
    __syncthreads();
#pragma unroll
    for (int qt = 0; qt < 4; ++qt) {
      u32x4 w0, w1; pack_acc16(acc[0], qt, w0, w1);
      char* dst = lds + L1_HT + ((wid >> 1) * 64 + qt * 16 + a) * QX_STRIDE + ((wid & 1) * 64 + g4 * 16) * 2;
      *(u32x4*)dst = w0; *(u32x4*)(dst + 16) = w1;
    }
    asm volatile("s_waitcnt vmcnt(0)" ::: "memory");
    __syncthreads();
#pragma unroll
    for (int s = 0; s < 8; ++s) qf[s] = *(const bf16x8*)(lds + L1_HT + ((wid >> 1) * 64 + (wid & 1) * 32 + r32) * QX_STRIDE + 32 * s + 16 * hh);
  }
  const unsigned kx = (r32 >> 2) & 3;
  const unsigned kb0 = ldsb + 2048 * (r32 >> 3) + 64 * (r32 & 7) + 16 * ((0 + hh) ^ kx);
  const unsigned vb0 = ldsb + (4 * hh + ((lane >> 2) & 3)) * 64 + ((lane >> 4) & 1) * 32 + (lane & 3) * 8;
  unsigned ka0, ka1, va;
  auto kbase = [&](int t) AI { ka0 = kb0 + kslot(t); ka1 = (kb0 ^ 32u) + kslot(t); PIN(ka0); PIN(ka1); };
  auto vbase = [&](int t) AI { va = vb0 + vslot(t); PIN(va); };
  auto kfrag = [&](int m) AI { return *LDSP(const bf16x8, (size_t)(((m & 1) ? ka1 : ka0) + 512 * ((m & 7) >> 1) + 8192 * (m >> 3))); };
  auto vfrag = [&](int m) AI {
    const unsigned q = va + (m >> 2) * 4096 + (m & 3) * 512;
    bf16x4 lo = __builtin_amdgcn_ds_read_tr16_b64_v4bf16(LDSP(bf16x4, (size_t)q)), hi = __builtin_amdgcn_ds_read_tr16_b64_v4bf16(LDSP(bf16x4, (size_t)(q + 2048)));
    return __builtin_shufflevector(lo, hi, 0, 1, 2, 3, 4, 5, 6, 7);
  };
  auto fixup = [&](const f32x16& S, float l0p, float l1p, u32x4& pa, u32x4& pb, bool pend, u32x4& pend0, u32x4& pend1) AI {
    const float mx = swap32_max(sm_arg(max16(S), negmc));
    const float dm = fmaxf(__builtin_ceilf(mx) + (float)SM_SHIFT, 0.f);
    const float f = __builtin_amdgcn_exp2f(-dm);
    negmc -= dm; l0 = l0p * f; l1 = l1p * f;
#pragma unroll
    for (int db = 0; db < 4; ++db) o[db] *= f;
    if (pend) {
#pragma unroll
      for (int q = 0; q < 4; ++q) { pend0[q] = scale_pk(pend0[q], f); pend1[q] = scale_pk(pend1[q], f); }
    }
    sm_half(S, negmc, l0, l1, pa, pb);
  };

  f32x16 Sa0, Sa1, Sb0, Sb1;
  u32x4 Pa0, Pa1, Pb0, Pb1, P2, P3;
  bf16x8 kf[4], vf[4];
  kbase(0);
  {
#pragma unroll
    for (int m = 0; m < 16; ++m) {
      const bf16x8 k = kfrag(m);
      if (m < 8) Sa0 = MFMA32(k, qf[m & 7], m == 0 ? (f32x16){} : Sa0); else Sa1 = MFMA32(k, qf[m & 7], m == 8 ? (f32x16){} : Sa1);
      if (m == 9) { SCHED_FENCE(); retile_load(rtl, rsrc_w, rld, rk0, rperm, rn0, lane); MEM_FENCE(); SCHED_FENCE(); }
    }
  }
  kbase(1);
#pragma unroll
  for (int m = 0; m < 3; ++m) kf[m] = kfrag(m);
  negmc = -sm_arg(swap32_max(fmaxf(max16(Sa0), max16(Sa1))), (float)SM_SHIFT);
  l0 = 0.f; l1 = 0.f;
  sm_half(Sa0, negmc, l0, l1, Pa0, Pa1);
#pragma unroll
  for (int db = 0; db < 4; ++db) o[db] = (f32x16){};
  SCHED_FENCE();

  const rsrc_t rWo = mk_rsrc(ws + WS_WOT + (size_t)(g * 8 + wid) * 65536, 65536);
  const rsrc_t rRt = mk_rsrc(ws + rdst, 36864);
  bf16x8 ring2[12][2];
  auto wo_load = [&](int sg, int i) AI { return __builtin_bit_cast(bf16x8, __builtin_amdgcn_raw_buffer_load_b128(rWo, fo + i * 1024, (sg >> 4) * 32768 + ((sg + rot) & 15) * 2048, 16)); };
  unsigned flagv = 0;

  auto tile = [&](auto T_, const f32x16& C1, f32x16& N0, f32x16& N1, u32x4& Pc0, u32x4& Pc1, u32x4& Pn0, u32x4& Pn1) AI {
    constexpr int T = T_.value;
    {
      const float l0p = l0, l1p = l1; SmPipe sp;
#pragma unroll
      for (int m = 0; m < 16; ++m) {
        if (T < 7) {
          if (m < 8) N0 = MFMA32(kf[m & 3], qf[m & 7], m == 0 ? (f32x16){} : N0); else N1 = MFMA32(kf[m & 3], qf[m & 7], m == 8 ? (f32x16){} : N1);
          if (m + 3 < 16) kf[(m + 3) & 3] = kfrag(m + 3);
        }
        if (m == 12) vbase(T);
        if (m >= 13) vf[m - 13] = vfrag(m - 13);
        sm_gap(m, sp, C1, negmc, l0, l1, P2, P3);
        if (T == 1 && m < 15 && m % 3 != 2) { const int k = m / 3, j = m % 3; if (k == 0) dmaK1(4, j); if (k == 1) dmaV1(3, j); if (k == 2) dmaK1(5, j); if (k == 3) dmaV1(4, j); if (k == 4) dmaK1(6, j); }
        if (T == 7) ring2[m >> 1][m & 1] = wo_load(m >> 1, m & 1);
        if (T < 7) SCHED_FENCE();
      }
      MEM_FENCE();
      if (__builtin_expect(__any(!(l0 + l1 <= SM_LIMIT)), 0)) fixup(C1, l0p, l1p, P2, P3, true, Pc0, Pc1);
    }
    {
      const float l0p = l0, l1p = l1; SmPipe sp;
#pragma unroll
      for (int m = 0; m < 16; ++m) {
        const u32x4 pp = (m < 4) ? Pc0 : (m < 8) ? Pc1 : (m < 12) ? P2 : P3;
        o[m & 3] = MFMA32(vf[m & 3], __builtin_bit_cast(bf16x8, pp), o[m & 3]);
        if (m + 3 < 16) vf[(m + 3) & 3] = vfrag(m + 3);
        if (T < 7) sm_gap(m, sp, N0, negmc, l0, l1, Pn0, Pn1);
        if (T == 7 && m < 8) ring2[8 + (m >> 1)][m & 1] = wo_load(8 + (m >> 1), m & 1);
        if ((T == 0 || T == 1 || T == 3 || T == 5) && m == 13) {
          if (T == 5 && wid == 0) {
            for (unsigned spins = 0; !__all(flagv == 32u) && spins < SPIN_CAP; ++spins) { __builtin_amdgcn_s_sleep(2); flagv = __hip_atomic_load(flagw, __ATOMIC_RELAXED, __HIP_MEMORY_SCOPE_AGENT); }
          }
          if (T == 1) WAIT_VM0();
          if (T == 0) asm volatile("s_waitcnt lgkmcnt(0)" : "+v"(vf[2]), "+v"(vf[3])::"memory");
          else        asm volatile("s_waitcnt vmcnt(0) lgkmcnt(0)" : "+v"(vf[2]), "+v"(vf[3])::"memory");
          __builtin_amdgcn_s_barrier();
          MEM_FENCE();
          if (T == 3 && tid == 0) __hip_atomic_fetch_add(flag, 1u, __ATOMIC_RELAXED, __HIP_MEMORY_SCOPE_AGENT);
        }
        if (T == 4 && m == 13 && wid == 0) flagv = __hip_atomic_load(flagw, __ATOMIC_RELAXED, __HIP_MEMORY_SCOPE_AGENT);
        if (T < 6 && m == 12) kbase(T + 2);
        if (T < 6 && m == 13) { kf[0] = kfrag(0); kf[1] = kfrag(1); }
        if (T < 6 && m == 14) kf[2] = kfrag(2);
        if (T == 0 && m == 14) dmaK(3);
        if (T == 0 && m == 15) dmaV(2);
        if (T == 1 && m == 14) dmaV(5);
        if (T == 1 && m == 15) dmaK(7);
        if (T == 3 && m == 14) dmaV(6);
        if (T == 3 && m == 15) dmaV(7);
        if (T == 1 && m == 15) {
#pragma unroll
          for (int i = 0; i < 4; ++i) __builtin_amdgcn_raw_buffer_store_b128(retile_frag(rtl, i), rRt, fo + (i >> 1) * rhalf + (i & 1) * 1024, 0, 16);
        }
        SCHED_FENCE();
      }
      MEM_FENCE();
      if (T < 7) {
        if (__builtin_expect(__any(!(l0 + l1 <= SM_LIMIT)), 0)) fixup(N0, l0p, l1p, Pn0, Pn1, false, Pc0, Pc1);
      }
    }
  };
  tile(IC<0>{}, Sa1, Sb0, Sb1, Pa0, Pa1, Pb0, Pb1);
  tile(IC<1>{}, Sb1, Sa0, Sa1, Pb0, Pb1, Pa0, Pa1);
  tile(IC<2>{}, Sa1, Sb0, Sb1, Pa0, Pa1, Pb0, Pb1);
  tile(IC<3>{}, Sb1, Sa0, Sa1, Pb0, Pb1, Pa0, Pa1);
  tile(IC<4>{}, Sa1, Sb0, Sb1, Pa0, Pa1, Pb0, Pb1);
  tile(IC<5>{}, Sb1, Sa0, Sa1, Pb0, Pb1, Pa0, Pa1);
  tile(IC<6>{}, Sa1, Sb0, Sb1, Pa0, Pa1, Pb0, Pb1);
  tile(IC<7>{}, Sb1, Sa0, Sa1, Pb0, Pb1, Pa0, Pa1);
  {
    const float inv = 1.f / swap32_add(l0 + l1);
    int l3; asm volatile("v_mbcnt_lo_u32_b32 %0, -1, 0\n\tv_mbcnt_hi_u32_b32 %0, -1, %0" : "=v"(l3));
    char* dst = lds + L1_HT + ((wid & 1) * 32 + (l3 & 31)) * HT_STRIDE + ((wid >> 1) * 128 + 4 * (l3 >> 5)) * 2;
#pragma unroll
    for (int db = 0; db < 4; ++db)
#pragma unroll
      for (int gq = 0; gq < 4; ++gq) {
        u32x2 w = {pk2(o[db][4 * gq] * inv, o[db][4 * gq + 1] * inv), pk2(o[db][4 * gq + 2] * inv, o[db][4 * gq + 3] * inv)};
        *(u32x2*)(dst + (32 * db + 8 * gq) * 2) = w;
      }
  }
  __syncthreads();
  if (wid >= 4) __builtin_amdgcn_s_setprio(1);
  {
    f32x4 acc[2][2][4];
    const rsrc_t rP = mk_rsrc(ws + WS_PART + (size_t)((rbk * 8 + g) * 8 + wid) * 8192, 8192);
    auto pstore = [&](int ps, int qt) AI {
      u32x4 w;
#pragma unroll
      for (int r = 0; r < 4; ++r) w[r] = pk2(acc[ps][0][qt][r], acc[ps][1][qt][r]);
      __builtin_amdgcn_raw_buffer_store_b128(w, rP, fo + (qt * 2 + ps) * 1024, 0, 16);
    };
    gemm_steps<2, 4, 32, 12, 2, 2>(acc, ring2, wo_load, bread, [&](auto S_, auto M_) AI {
      if constexpr (S_.value == 21 && M_.value == 0) __builtin_amdgcn_s_setprio(0);
      if constexpr (S_.value >= 20 && S_.value < 22 && (M_.value & 3) == 1) pstore(0, (S_.value - 20) * 2 + (M_.value >> 2));
      if constexpr (S_.value == 28 && M_.value == 7) {
        WAIT_VM0(); asm volatile("s_waitcnt vmcnt(0)" ::: "memory"); __builtin_amdgcn_s_barrier(); MEM_FENCE();
        if (tid == 0) __hip_atomic_fetch_add(flags + (FL_P0 + rbk) * 32, 1u, __ATOMIC_RELAXED, __HIP_MEMORY_SCOPE_AGENT);
      } });
#pragma unroll
    for (int qt = 0; qt < 4; ++qt) pstore(1, qt);
  }
  auto tail = [&](const int lane, const int a, const int g4, const unsigned fo, const int tid, const char* bl) AI {
  const rsrc_t rWs = mk_rsrc(ws, (unsigned)WS_END);
  auto ld1 = [&](size_t off, unsigned soff) AI { return __builtin_amdgcn_raw_buffer_load_b128(rWs, (unsigned)off, soff, 16); };
  auto ldf1 = [&](size_t off, unsigned soff) AI { return __builtin_bit_cast(bf16x8, __builtin_amdgcn_raw_buffer_load_b128(rWs, (unsigned)off, soff, 16)); };
  auto st1 = [&](size_t off, u32x4 v) AI { __builtin_amdgcn_raw_buffer_store_b128(v, rWs, (unsigned)off, 0, 16); };
  auto publish = [&](int idx) AI {
    asm volatile("s_waitcnt vmcnt(0) lgkmcnt(0)" ::: "memory"); __builtin_amdgcn_s_barrier(); MEM_FENCE();
    if (tid == 0) __hip_atomic_fetch_add(flags + idx * 32, 1u, __ATOMIC_RELAXED, __HIP_MEMORY_SCOPE_AGENT);
  };
  auto await = [&](int pw, int idx, unsigned want) AI {
    if (wid == pw) {
      unsigned spins = 0;
      while (!__all(__hip_atomic_load(flags + idx * 32, __ATOMIC_RELAXED, __HIP_MEMORY_SCOPE_AGENT) >= want) && ++spins < SPIN_CAP) __builtin_amdgcn_s_sleep(1);
    }
    MEM_FENCE(); __builtin_amdgcn_s_barrier(); MEM_FENCE();
  };
  const int b = blockIdx.x;
  const u16* modb = (const u16*)(ws + WS_MOD);
  publish(FL_P + rbk);
  {
    f32x2* red = (f32x2*)(lds + 16 * HT_STRIDE);
    const int rt = (b & 7) * 16 + (b >> 4), nh = (b >> 3) & 1;
    const int prb = rt >> 2, qt = rt & 3, row = rt * 16 + a, col = wid * 64 + g4 * 16;
    constexpr int D1 = 12;
    bf16x8 A[D1][2];
    const int G = nh * 4 + (wid >> 1), ps = wid & 1;
    const rsrc_t rW = mk_rsrc(ws + WS_W1T + (size_t)G * 65536 + ps * 2048, 65536);
    auto aload = [&](int s, int i) AI { return ldfrag(rW, fo, (s * 4 + i) * 1024); };
    u32x4 pv[8][2];
    const size_t poff = WS_PART + (size_t)((prb * 64 + wid) * 8 + qt * 2) * 1024 + fo;
    await(0, FL_P0 + prb, 8);
    if (wid != 7) {
#pragma unroll
      for (int gg = 0; gg < 8; ++gg) pv[gg][0] = ld1(poff, (gg * 64) * 1024);
    }
    SCHED_FENCE();
    await(7, FL_P + prb, 8);
#pragma unroll
    for (int gg = 0; gg < 8; ++gg) pv[gg][1] = ld1(poff, (gg * 64 + 1) * 1024);
    if (wid == 7) {
#pragma unroll
      for (int gg = 0; gg < 8; ++gg) pv[gg][0] = ld1(poff, (gg * 64) * 1024);
    }
    u32x4 xv[2], gav[2], cbv[2], sbv[2];
#pragma unroll
    for (int h = 0; h < 2; ++h) {
      xv[h] = *(const u32x4*)(p.x + (size_t)row * D_ + col + 8 * h);
      gav[h] = *(const u32x4*)(modb + 1024 + col + 8 * h); sbv[h] = *(const u32x4*)(modb + 1536 + col + 8 * h); cbv[h] = *(const u32x4*)(modb + 2048 + col + 8 * h);
    }
    SCHED_FENCE();
    __builtin_amdgcn_s_barrier();
#pragma unroll
    for (int s = 0; s < D1; ++s) { A[s][0] = aload(s, 0); A[s][1] = aload(s, 1); }
    SCHED_FENCE();
    float v[16];
#pragma unroll
    for (int d = 0; d < 8; ++d) { v[2 * d] = bflo(pv[0][d & 1][d >> 1]); v[2 * d + 1] = bfhi(pv[0][d & 1][d >> 1]); }
#pragma unroll
    for (int gg = 1; gg < 8; ++gg) {
#pragma unroll
      for (int d = 0; d < 8; ++d) { v[2 * d] += bflo(pv[gg][d & 1][d >> 1]); v[2 * d + 1] += bfhi(pv[gg][d & 1][d >> 1]); }
      SCHED_FENCE();
    }
#pragma unroll
    for (int d = 0; d < 8; ++d) {
      const unsigned ga = gav[d >> 2][d & 3], xx = xv[d >> 2][d & 3];
      const unsigned x2 = pk2(__builtin_fmaf(bflo(ga), v[2 * d], bflo(xx)), __builtin_fmaf(bfhi(ga), v[2 * d + 1], bfhi(xx)));
      xv[d >> 2][d & 3] = x2; v[2 * d] = bflo(x2); v[2 * d + 1] = bfhi(x2);
    }
    if (nh == 0) { st1(WS_X2 + (size_t)row * 1024 + col * 2, xv[0]); st1(WS_X2 + (size_t)row * 1024 + col * 2 + 16, xv[1]); }
    auto row_stats = [&](f32x2* rd, float& mean, float& rstd) AI {
      float s0 = 0, s1 = 0, q0 = 0, q1 = 0;
#pragma unroll
      for (int e = 0; e < 8; ++e) { s0 += v[2 * e]; s1 += v[2 * e + 1]; q0 = __builtin_fmaf(v[2 * e], v[2 * e], q0); q1 = __builtin_fmaf(v[2 * e + 1], v[2 * e + 1], q1); }
      float s = swap32_add(swap16_add(s0 + s1)), q = swap32_add(swap16_add(q0 + q1));
      if (g4 == 0) rd[wid * 16 + a] = (f32x2){s, q};
      asm volatile("s_waitcnt lgkmcnt(0)" ::: "memory"); __builtin_amdgcn_s_barrier(); MEM_FENCE();
      f32x2 t = rd[a];
#pragma unroll
      for (int w = 1; w < 8; ++w) t += rd[w * 16 + a];
      mean = t[0] * (1.f / 512.f);
      rstd = __builtin_amdgcn_rsqf(fmaxf(t[1] * (1.f / 512.f) - mean * mean, 0.f) + 1e-5f);
    };
    float mean, rstd;
    row_stats(red, mean, rstd);
    u32x4 hv[2];
#pragma unroll
    for (int d = 0; d < 8; ++d) {
      const unsigned cb = cbv[d >> 2][d & 3], sb = sbv[d >> 2][d & 3];
      const unsigned hb = pk2((v[2 * d] - mean) * rstd * bflo(cb) + bflo(sb), (v[2 * d + 1] - mean) * rstd * bfhi(cb) + bfhi(sb));
      hv[d & 1][d >> 1] = hb; v[2 * d] = bflo(hb); v[2 * d + 1] = bfhi(hb);
    }
    if (nh == 1) { st1(WS_HB + (size_t)row * 1024 + wid * 128 + g4 * 16, hv[0]); st1(WS_HB + (size_t)row * 1024 + wid * 128 + 64 + g4 * 16, hv[1]); }
    row_stats(red + 128, mean, rstd);
    const float nm = -mean * rstd;
#pragma unroll
    for (int h = 0; h < 2; ++h) {
      u32x4 o;
#pragma unroll
      for (int e = 0; e < 4; ++e) o[e] = pk2(__builtin_fmaf(v[8 * h + 2 * e], rstd, nm), __builtin_fmaf(v[8 * h + 2 * e + 1], rstd, nm));
      *(u32x4*)(lds + a * HT_STRIDE + (col + 8 * h) * 2) = o;
    }
    asm volatile("s_waitcnt lgkmcnt(0)" ::: "memory"); __builtin_amdgcn_s_barrier(); MEM_FENCE();
    f32x4 acc[1][2][1];
    gemm_steps<2, 1, 16, D1, 1, 4>(acc, A, aload, [&](int s, int) AI { return *(const bf16x8*)(bl + 64 * s); }, [](auto, auto) AI {});
    u32x4 ov;
#pragma unroll
    for (int rg = 0; rg < 4; ++rg) ov[rg] = pk2(gelu_tanh(acc[0][0][0][rg]), gelu_tanh(acc[0][1][0][rg]));
    st1(WS_H1 + (size_t)(rt * 16 + 2 * G + ps) * 1024 + fo, ov);
    publish(FL_H1 + rt);
  }

  const int rb2 = (b & 7) * 4 + (b >> 6), ns = (b >> 3) & 7;
  const int ng = ns * 7 + (wid < 7 ? wid : 6);
  const size_t w3off = WS_W3T + (size_t)(ng >> 1) * 65536 + (ng & 1) * 2048 + fo;
  bf16x8 A[16][2];
  {
    const int rt2 = (b & 7) * 8 + (b >> 5), nq = (b >> 3) & 3;
    const int G = nq * 2 + ((wid & 3) >> 1), ps = wid & 1;
    if (wid < 4) {
      const size_t aoff = WS_W2T + (size_t)G * 65536 + ps * 2048 + fo;
#pragma unroll
      for (int s = 0; s < 16; ++s) { A[s][0] = ldf1(aoff, (s * 4) * 1024); A[s][1] = ldf1(aoff, (s * 4 + 1) * 1024); }
    }
    SCHED_FENCE();
    { const int t = 2 * rt2 - 1 + (lane & 3); await(7, FL_H1 + (t < 0 ? 0 : t > 127 ? 127 : t), 2); }
    const rsrc_t rH = mk_rsrc(ws + WS_H1, (unsigned)NXB);
    const int sh = (wid < 2) ? -1 : (wid < 4) ? 1 : (wid < 6) ? -2 : 2;
    u32x4 hf[2][2], hbv[2];
#pragma unroll
    for (int qt = 0; qt < 2; ++qt) {
      const int r = rt2 * 32 + qt * 16 + a + sh;
      const unsigned ho = (unsigned)((r >> 4) * 16384 + ((r & 15) + 16 * g4) * 16);
      hf[qt][0] = __builtin_amdgcn_raw_buffer_load_b128(rH, ho, (wid * 2) * 1024, 16); hf[qt][1] = __builtin_amdgcn_raw_buffer_load_b128(rH, ho, (wid * 2 + 1) * 1024, 16);
    }
    const size_t xoff = (size_t)(rt2 * 32 + a) * 1024 + G * 128 + ps * 64 + g4 * 16;
    if (wid < 4) { hbv[0] = ld1(WS_HB + xoff, 0); hbv[1] = ld1(WS_HB + xoff, 16384); }
#pragma unroll
    for (int qt = 0; qt < 2; ++qt) { *(u32x4*)(lds + (qt * 16 + wid * 2) * 1024 + fo) = hf[qt][0]; *(u32x4*)(lds + (qt * 16 + wid * 2 + 1) * 1024 + fo) = hf[qt][1]; }
    asm volatile("s_waitcnt lgkmcnt(0)" ::: "memory"); __builtin_amdgcn_s_barrier(); MEM_FENCE();
    f32x2* red = (f32x2*)(lds + 32768);
    if (wid < 4) {
      f32x4 acc[1][2][2];
      gemm_steps<2, 2, 16, 16, 1, 3>(acc, A, [&](int, int) AI { return bf16x8{}; }, [&](int s, int qt) AI { return *(const bf16x8*)(lds + (qt * 16 + s) * 1024 + fo); }, [](auto, auto) AI {});
#pragma unroll
      for (int qt = 0; qt < 2; ++qt) {
        u32x4 ov; float s = 0, q = 0;
#pragma unroll
        for (int rg = 0; rg < 4; ++rg) {
          ov[rg] = pk2(bflo(hbv[qt][rg]) + acc[0][0][qt][rg], bfhi(hbv[qt][rg]) + acc[0][1][qt][rg]);
          const float lo = bflo(ov[rg]), hi = bfhi(ov[rg]); s += lo + hi; q += lo * lo + hi * hi;
        }
        st1(WS_X1 + xoff + qt * 16384, ov);
        s = swap32_add(swap16_add(s)); q = swap32_add(swap16_add(q));
        if (g4 == 0) red[wid * 32 + qt * 16 + a] = (f32x2){s, q};
      }
    } else if (wid < 7) {
#pragma unroll
      for (int s = 0; s < 16; ++s) { A[s][0] = ldf1(w3off, (s * 4) * 1024); A[s][1] = ldf1(w3off, (s * 4 + 1) * 1024); }
    }
    asm volatile("s_waitcnt lgkmcnt(0)" ::: "memory"); __builtin_amdgcn_s_barrier(); MEM_FENCE();
    if (tid < 32) {
      const f32x2 t = red[tid] + red[32 + tid] + red[64 + tid] + red[96 + tid];
      __hip_atomic_store((gu64*)(ws + WS_ST + (size_t)(rt2 * 32 + tid) * 32 + nq * 8), __builtin_bit_cast(unsigned long long, t), __ATOMIC_RELAXED, __HIP_MEMORY_SCOPE_AGENT);
    }
    publish(FL_X1 + (rt2 >> 1));
  }

  bf16x8 W[7][4];
  const size_t woff = WS_W4T + (size_t)(ns * 56 + wid * 7) * 4096 + fo;
  auto rot7 = [&](int s) AI { const int t = s + (ns < 7 ? ns : 0); return t >= 7 ? t - 7 : t; };
  {
    if (wid < 4) {
#pragma unroll
      for (int s = 0; s < 16; ++s) { A[s][0] = ldf1(w3off, (s * 4) * 1024); A[s][1] = ldf1(w3off, (s * 4 + 1) * 1024); }
    }
    SCHED_FENCE();
    await(7, FL_X1 + rb2, 8);
    u32x4 xv[8];
    const u32x4 stv = ld1(WS_ST + (size_t)(rb2 * 64 + wid * 8) * 32 + (lane & 15) * 16, 0);
#pragma unroll
    for (int i = 0; i < 8; ++i) xv[i] = ld1(WS_X1 + (size_t)(rb2 * 64 + wid * 8) * 1024 + fo, i * 1024);
    SCHED_FENCE();
    const float sm = dpp_add<0xB1>(__uint_as_float(stv[0]) + __uint_as_float(stv[2])) * (1.f / 512.f);
    const float sq = dpp_add<0xB1>(__uint_as_float(stv[1]) + __uint_as_float(stv[3])) * (1.f / 512.f);
    const float rsl = __builtin_amdgcn_rsqf(fmaxf(sq - sm * sm, 0.f) + 1e-5f), nml = -sm * rsl;
    float rs[8], nm[8];
#pragma unroll
    for (int i = 0; i < 8; ++i) { rs[i] = __uint_as_float(__builtin_amdgcn_readlane(__float_as_uint(rsl), 2 * i)); nm[i] = __uint_as_float(__builtin_amdgcn_readlane(__float_as_uint(nml), 2 * i)); }
    SCHED_FENCE();
#pragma unroll
    for (int hf = 0; hf < 2; ++hf) {
      float lo[16], hi[16]; unsigned t[16];
      H_STAGE(lo[j] = bflo(xv[i][d]); hi[j] = bfhi(xv[i][d]));
      H_STAGE(lo[j] = __builtin_fmaf(lo[j], rs[i], nm[i]); hi[j] = __builtin_fmaf(hi[j], rs[i], nm[i]));
      H_STAGE(t[j] = pk2(lo[j], hi[j]));
#pragma unroll
      for (int i = 0; i < 4; ++i) *(u32x4*)(lds + (wid * 8 + hf * 4 + i) * HT_STRIDE + fo) = (u32x4){t[4 * i], t[4 * i + 1], t[4 * i + 2], t[4 * i + 3]};
      SCHED_FENCE();
    }
    asm volatile("s_waitcnt lgkmcnt(0)" ::: "memory"); __builtin_amdgcn_s_barrier(); MEM_FENCE();
    if (wid < 7) {
      f32x4 acc[1][2][4];
      if (wid >= 4) __builtin_amdgcn_s_setprio(1);
      const size_t dst = WS_U + (size_t)(rb2 * 4 * 56 + ng) * 1024 + fo;
      gemm_steps<2, 4, 16, 16, 1, 2>(acc, A, [&](int, int) AI { return bf16x8{}; },
                                     [&](int s, int qt) AI { return *(const bf16x8*)(bl + qt * 16 * HT_STRIDE + 64 * s); },
                                     [&](auto S_, auto M_) AI {
                                       constexpr int s = S_.value, m = M_.value;
                                       if constexpr (s == 11 && m == 0) __builtin_amdgcn_s_setprio(0);
                                       if constexpr (s >= 1 && s < 15 && (m == 4 || m == 6)) { constexpr int k = (s - 1) * 2 + (m - 4) / 2; W[k >> 2][k & 3] = ldf1(woff + (k & 3) * 1024, rot7(k >> 2) * 4096); }
                                     });
      {
        float x[32], e[32];
#define G_STAGE(body) _Pragma("unroll") for (int j = 0; j < 32; ++j) { body; } SCHED_FENCE()
        G_STAGE(x[j] = acc[0][j & 1][j >> 3][(j >> 1) & 3]; e[j] = x[j] * x[j]);
        G_STAGE(e[j] = __builtin_fmaf(e[j], -0.10294324f, -2.3022082f));
        G_STAGE(e[j] *= x[j]);
        G_STAGE(e[j] = __builtin_amdgcn_exp2f(e[j]));
        G_STAGE(e[j] += 1.f);
        G_STAGE(e[j] = __builtin_amdgcn_rcpf(e[j]));
        G_STAGE(e[j] *= x[j]);
#pragma unroll
        for (int qt = 0; qt < 4; ++qt)
          st1(dst + (size_t)qt * 56 * 1024, (u32x4){pk2(e[8 * qt], e[8 * qt + 1]), pk2(e[8 * qt + 2], e[8 * qt + 3]), pk2(e[8 * qt + 4], e[8 * qt + 5]), pk2(e[8 * qt + 6], e[8 * qt + 7])});
      }
    } else {
#pragma unroll
      for (int k = 0; k < 28; ++k) W[k >> 2][k & 3] = ldf1(woff + (k & 3) * 1024, rot7(k >> 2) * 4096);
    }
    publish(FL_U + rb2);
  }

  {
    bf16x8 B[7][4];
    const size_t uoff = WS_U + (size_t)(rb2 * 4 * 56 + wid * 7) * 1024 + fo;
    const int oqt = wid >> 1, orp = wid & 1, orow = rb2 * 64 + oqt * 16 + a, ocol = ns * 64 + g4 * 16 + orp * 8;
    u32x2 x1a, x1b; u32x4 x2v, gbv;
    SCHED_FENCE();
    await(0, FL_U + rb2, 8);
#pragma unroll
    for (int s = 0; s < 2; ++s)
#pragma unroll
      for (int i = 0; i < 4; ++i) B[s][i] = ldf1(uoff, (i * 56 + rot7(s)) * 1024);
    SCHED_FENCE();
    f32x4 acc[4][4] = {};
#pragma unroll
    for (int s = 0; s < 7; ++s) {
#pragma unroll
      for (int i = 0; i < 4; ++i) {
#pragma unroll
        for (int qt = 0; qt < 4; ++qt) acc[i][qt] = MFMA16(W[s][i], B[s][qt], acc[i][qt]);
        if (s + 2 < 7) B[s + 2][i] = ldf1(uoff, (i * 56 + rot7(s + 2)) * 1024);
        SCHED_FENCE();
      }
      if (s == 5) {
        const size_t x1o = WS_X1 + (size_t)orow * 1024 + ns * 128 + g4 * 16 + orp * 8;
        x1a = __builtin_amdgcn_raw_buffer_load_b64(rWs, (unsigned)x1o, 0, 16); x1b = __builtin_amdgcn_raw_buffer_load_b64(rWs, (unsigned)x1o, 64, 16);
        x2v = ld1(WS_X2 + (size_t)orow * 1024 + ocol * 2, 0); gbv = *(const u32x4*)(modb + 2560 + ocol);
        SCHED_FENCE();
      }
    }
#pragma unroll
    for (int qt = 0; qt < 4; ++qt)
#pragma unroll
      for (int rg = 0; rg < 4; ++rg)
        *(f32x4*)(lds + ((wid * 4 + qt) * 4 + rg) * 1024 + fo) = (f32x4){acc[0][qt][rg], acc[1][qt][rg], acc[2][qt][rg], acc[3][qt][rg]};
    __syncthreads();
    f32x4 s0 = {}, s1 = {};
#pragma unroll
    for (int w = 0; w < 8; ++w) {
      s0 += *(const f32x4*)(lds + ((w * 4 + oqt) * 4 + 2 * orp) * 1024 + fo);
      s1 += *(const f32x4*)(lds + ((w * 4 + oqt) * 4 + 2 * orp + 1) * 1024 + fo);
    }
    const float h2[8] = {s0[0], s0[1], s0[2], s0[3], s1[0], s1[1], s1[2], s1[3]};
    const unsigned x1v[4] = {x1a[0], x1b[0], x1a[1], x1b[1]};
    u32x4 ov;
#pragma unroll
    for (int e = 0; e < 4; ++e)
      ov[e] = pk2(bflo(x2v[e]) + bflo(gbv[e]) * (bflo(x1v[e]) + h2[2 * e]), bfhi(x2v[e]) + bfhi(gbv[e]) * (bfhi(x1v[e]) + h2[2 * e + 1]));
    *(u32x4*)(p.out + (size_t)orow * D_ + ocol) = ov;
  }
  };
  {
    int l2; asm volatile("v_mbcnt_lo_u32_b32 %0, -1, 0\n\tv_mbcnt_hi_u32_b32 %0, -1, %0" : "=v"(l2));
    tail(l2, l2 & 15, l2 >> 4, (unsigned)l2 * 16u, wid * 64 + l2, lds + (l2 & 15) * HT_STRIDE + (l2 >> 4) * 16);
  }
}

extern "C" void kernel_launch(void* const* d_in, const int* in_sizes, int n_in, void* d_out, int out_size, void* d_ws, size_t ws_size, hipStream_t stream) {
  char* ws = (char*)d_ws;
  K0Args p0;
  p0.x = (const u16*)d_in[0]; p0.c = (const u16*)d_in[10]; p0.wmod = (const u16*)d_in[11]; p0.wq = (const u16*)d_in[3]; p0.w1 = (const u16*)d_in[6]; p0.ws = ws;
  K1Args p1;
  p1.x = p0.x; p1.kc = (const u16*)d_in[1]; p1.vc = (const u16*)d_in[2]; p1.wo = (const u16*)d_in[4];
  p1.w2 = (const u16*)d_in[7]; p1.w3 = (const u16*)d_in[8]; p1.w4 = (const u16*)d_in[9]; p1.lnst = (const float*)(ws + WS_Y); p1.ws = ws; p1.out = (u16*)d_out;
  (void)hipFuncSetAttribute((const void*)k1_attn, hipFuncAttributeMaxDynamicSharedMemorySize, L1_END);
  k0_prep<<<K0_BLOCKS, 512, 0, stream>>>(p0);
  k1_attn<<<256, 512, L1_END, stream>>>(p1);
}
```
